```python
import torch

def _softmax(x, axis=-1):
    return torch.softmax(x, dim=axis)
def _gelu(x):
    return torch.nn.functional.gelu(x, approximate="tanh")
def _silu(x):
    return torch.nn.functional.silu(x)
def _sigmoid(x):
    return torch.sigmoid(x)
def _layernorm(x, axis=-1, eps=1e-5):
    m = x.mean(dim=axis, keepdim=True)
    v = x.var(dim=axis, keepdim=True, unbiased=False)
    return (x - m) / torch.sqrt(v + eps)
def _scatter_add(tgt, idx, src, axis=0):
    return tgt.clone().index_add_(axis, idx, src)
def _segment_sum(src, seg, n):
    out = torch.zeros((n,) + src.shape[1:], dtype=src.dtype)
    return out.index_add_(0, seg, src)
def _pad(x, pw):
    flat = tuple(v for pair in reversed(pw) for v in pair)
    return torch.nn.functional.pad(x, flat)

kernel_name = 'gated_mix__grouped_kv_mqa__mixer_channel_mlp__prompt'

def setup_inputs():

    _w = (lambda a: (lambda t: t.to(torch.bfloat16) if t.is_floating_point() else t)(torch.as_tensor(a)))
    def _A_setup():
        B, S, D, H = 1, 2048, 576, 32
        Dh = D // H
        return {
            "x": _w((lambda *s: torch.randn(*s, dtype=torch.bfloat16) * 0.05)(B, S, D)),
            "wq": _w((lambda *s: torch.randn(*s, dtype=torch.bfloat16) * 0.8)(D, H * Dh)),
            "wk": _w((lambda *s: torch.randn(*s, dtype=torch.bfloat16) * 0.8)(D, Dh)),
            "wv": _w((lambda *s: torch.randn(*s, dtype=torch.bfloat16) * 0.2)(D, Dh)),
            "wo": _w((lambda *s: torch.randn(*s, dtype=torch.bfloat16) * 0.2)(H * Dh, D)),
        }
    _A_in = _A_setup()

    def _B_setup():
        B, S, D, F = 1, 2048, 576, 4608
        return {
            "x": _w((lambda *s: torch.randn(*s, dtype=torch.bfloat16) * 0.05)(B, S, D)),
            "w1": _w((lambda *s: torch.randn(*s, dtype=torch.bfloat16) * 0.8)(D, F) * (D ** -0.5)),
            "b1": _w((lambda *s: torch.randn(*s, dtype=torch.bfloat16) * 0.8)(F)),
            "w2": _w((lambda *s: torch.randn(*s, dtype=torch.bfloat16) * 0.05)(F, D) * (F ** -0.5)),
            "b2": _w((lambda *s: torch.randn(*s, dtype=torch.bfloat16) * 0.05)(D)),
        }
    _B_in = _B_setup()

    return {
        "A_x": _A_in["x"],
        "A_wq": _A_in["wq"],
        "A_wk": _A_in["wk"],
        "A_wv": _A_in["wv"],
        "A_wo": _A_in["wo"],
        "B_x": _B_in["x"],
        "B_w1": _B_in["w1"],
        "B_b1": _B_in["b1"],
        "B_w2": _B_in["w2"],
        "B_b2": _B_in["b2"],
        "gate_w": _w((lambda *s: torch.randn(*s, dtype=torch.bfloat16) * 0.05)(576) * 16.0),
    }

def reference(A_x, A_wq, A_wk, A_wv, A_wo, B_x, B_w1, B_b1, B_w2, B_b2, gate_w):
    def _A_ref(x, wq, wk, wv, wo):
        B, S, D = x.shape
        H = 32
        Dh = D // H
        q = (lambda x, *d: x.permute(*d))((x @ wq).reshape(B, S, H, Dh), 0, 2, 1, 3)
        k = x @ wk
        v = x @ wv
        scores = torch.einsum("bhqd,bkd->bhqk", q, k) * (Dh ** -0.5)
        p = _softmax(scores, axis=-1)
        o = torch.einsum("bhqk,bkd->bhqd", p, v)
        return (lambda x, *d: x.permute(*d))(o, 0, 2, 1, 3).reshape(B, S, H * Dh) @ wo

    def _B_ref(x, w1, b1, w2, b2):
        y = _layernorm(x, axis=-1)
        h = _gelu(torch.einsum("bsd,df->bsf", y, w1) + b1)
        return torch.einsum("bsf,fd->bsd", h, w2) + b2

    x = A_x
    g = _sigmoid((x * gate_w).sum(-1, keepdims=True))
    a = _A_ref(x, A_wq, A_wk, A_wv, A_wo)
    b = _B_ref(x, B_w1, B_b1, B_w2, B_b2)
    return g * a + (1 - g) * b
```

```cpp
#include <hip/hip_runtime.h>
#include <hip/hip_bf16.h>
#include <stdint.h>

typedef __attribute__((ext_vector_type(16))) float f32x16;
typedef __attribute__((ext_vector_type(4))) float f32x4;
typedef __attribute__((ext_vector_type(8))) __bf16 bf16x8;
typedef __attribute__((ext_vector_type(4))) short s16x4;
typedef __attribute__((ext_vector_type(4))) unsigned u32x4;
typedef __attribute__((ext_vector_type(2))) unsigned u32x2;
typedef u32x4 __attribute__((aligned(4))) u32x4_a4;
typedef unsigned short u16;
typedef __attribute__((address_space(3))) char lds_char;

#define OFF_KV 0u
#define OFF_FLAG 163840u
#define OFF_CHK 165888u
#define OFF_EPOCH 166400u
#define OFF_G 166912u
#define OFF_P 262144u
#define OFF_O (OFF_P + 8u * 64u * 36864u)
#define OFF_WF (OFF_O + 64u * 36864u)
#define KVREC 2560u
#define QSCALE 0.3400464821989298f
#define YSCALE -0.4686686930532063f
#define PSCALE -2.1337034344781243f
#define LDS_MAIN 163840
#define LDS_FIN 98304
#define SC1 16

__device__ __forceinline__ unsigned cvtpk(float lo, float hi) {
  typedef __attribute__((ext_vector_type(2))) __bf16 bf16x2;
  typedef __attribute__((ext_vector_type(2))) float f32x2;
  f32x2 v = {lo, hi};
  return __builtin_bit_cast(unsigned, __builtin_convertvector(v, bf16x2));
}
__device__ __forceinline__ float blo(unsigned u) { return __uint_as_float(u << 16); }
__device__ __forceinline__ float bhi(unsigned u) { return __uint_as_float(u & 0xffff0000u); }
__device__ __forceinline__ float xhalf_sum(float v) {
  auto r = __builtin_amdgcn_permlane32_swap(__float_as_uint(v), __float_as_uint(v), false, false);
  return __uint_as_float(r[0]) + __uint_as_float(r[1]);
}
template <int N> __device__ __forceinline__ float ror16(float v) {
  return __uint_as_float(__builtin_amdgcn_update_dpp(0u, __float_as_uint(v), 0x120 + N, 0xf, 0xf, true));
}
__device__ __forceinline__ float row16_sum(float v) {
  v += ror16<8>(v); v += ror16<4>(v); v += ror16<2>(v); v += ror16<1>(v); return v;
}
template <int N> __device__ __forceinline__ unsigned rormax(unsigned v) { return max(v, (unsigned)__builtin_amdgcn_update_dpp(0u, v, 0x120 + N, 0xf, 0xf, true)); }
__device__ __forceinline__ float half_max(unsigned v) {
  v = rormax<1>(rormax<2>(rormax<4>(rormax<8>(v))));
  auto r = __builtin_amdgcn_permlane16_swap(v, v, false, false);
  return __uint_as_float(max((unsigned)r[0], (unsigned)r[1]));
}
__device__ __forceinline__ float sq2(unsigned u, float acc) {
  typedef __attribute__((ext_vector_type(2))) __bf16 bf16x2;
  const bf16x2 v = __builtin_bit_cast(bf16x2, u);
  return __builtin_amdgcn_fdot2_f32_bf16(v, v, acc, false);
}
__device__ __forceinline__ u32x4 pack8(const float* v) {
  unsigned d0 = cvtpk(v[0], v[1]), d1 = cvtpk(v[2], v[3]), d2 = cvtpk(v[4], v[5]), d3 = cvtpk(v[6], v[7]);
  auto a = __builtin_amdgcn_permlane32_swap(d0, d2, false, false);
  auto b = __builtin_amdgcn_permlane32_swap(d1, d3, false, false);
  u32x4 r = {a[0], b[0], a[1], b[1]};
  return r;
}
typedef __amdgpu_buffer_rsrc_t rsrc_t;
__device__ __forceinline__ rsrc_t mkr(const void* p, unsigned bytes) { return __builtin_amdgcn_make_buffer_rsrc((void*)p, 0, bytes, 0x00020000); }
__device__ __forceinline__ u32x4 make_srd(const void* p, unsigned bytes) {
  const uint64_t a = (uint64_t)p;
  u32x4 r = {(unsigned)a, (unsigned)(a >> 32), bytes, 0x00020000u};
  return r;
}
__device__ __forceinline__ u32x4 cvt8(const f32x16& v, int b, float s) {
  u32x4 r = {cvtpk(v[b] * s, v[b + 1] * s), cvtpk(v[b + 2] * s, v[b + 3] * s), cvtpk(v[b + 4] * s, v[b + 5] * s), cvtpk(v[b + 6] * s, v[b + 7] * s)};
  return r;
}
#define ZERO16 {0.f, 0.f, 0.f, 0.f, 0.f, 0.f, 0.f, 0.f, 0.f, 0.f, 0.f, 0.f, 0.f, 0.f, 0.f, 0.f}

__global__ __launch_bounds__(256, 1) void k_main(const u16* __restrict__ x, const u16* __restrict__ wq, const u16* __restrict__ wk, const u16* __restrict__ wvv,
                                                 const u16* __restrict__ wo, const u16* __restrict__ w1, const u16* __restrict__ b1, const u16* __restrict__ w2,
                                                 const u16* __restrict__ gw, char* __restrict__ ws) {
  extern __shared__ __attribute__((aligned(16))) char lds[];
  const int l = threadIdx.x & 63, wv = __builtin_amdgcn_readfirstlane(threadIdx.x >> 6);
  const int xcd = blockIdx.x & 7, idx = (blockIdx.x >> 4) | (~blockIdx.x & 8) << 1;
  const int h = l >> 5, q = l & 31;
  const unsigned lb = (unsigned)(uintptr_t)(lds_char*)lds;
  const int fs = (xcd & 3) * 2 + (idx & 1), tb = (xcd >> 2) * 8 + ((idx >> 1) & 7), tt = tb * 4 + wv;
  const unsigned xg = (l >> 2) * 1152 + 16 * (l & 3), xw = (l >> 2) * 64 + 16 * ((l & 3) ^ (l >> 4)), xr = q * 64 + 16 * (h ^ ((q >> 2) & 3));
  if (idx < 16) {
    const unsigned pc = 16 * ((l & 7) ^ (4 * ((l >> 4) & 1)));
    const unsigned w1a = (16 * wv + 8 * h + ((l >> 3) & 3)) * 9216 + pc, w1b = w1a + 4 * 9216 - 1024;
    const unsigned w2a = (16 * wv + (l >> 3)) * 1152 + pc, w2b = w2a + 8 * 1152 - 1024;
    const unsigned sw = (l >> 3) & 1, ra = lb + h * 512 + ((l >> 2) & 3) * 128 + 32 * ((l >> 4) & 1) + 8 * (l & 3);
    const unsigned rd0 = ra + 64 * sw, rd1 = ra + 64 * (1 - sw), sb = lb + 149760 + wv * 2048;
    const u32x4 srd1 = make_srd(w1 + fs * 576, 576 * 9216 - fs * 1152), srd2 = make_srd(w2 + fs * 576 * 576, 576 * 1152);
    const u32x4 srdy = make_srd(x + tt * 18432, 36864), srdb = make_srd(b1 + fs * 576, 1152);
    const u32x4 srdp = make_srd(ws + OFF_P + (fs * 64 + tt) * 36864, 36864), srdq = make_srd(ws + OFF_P + (fs * 64 + tt) * 36864, wv ? 36864 : 0);
    asm volatile(
    "s_cmp_eq_u32 %[wv], 0\n"
    "s_cselect_b64 vcc, -1, 0\n"
    "s_nop 4\n"
    "v_add_u32 v16, 49152, %[rd0]\n"
    "v_add_u32 v17, 49152, %[rd1]\n"
    "v_add_u32 v18, 98304, %[rd0]\n"
    "v_add_u32 v19, 98304, %[rd1]\n"
    "buffer_load_dwordx2 v[28:29], %[vb1], %[srdb], 0 offen\n"
    "s_mov_b32 s41, 18432\n"
    "s_mov_b32 s42, 0xffff0000\n"
    "s_mov_b32 s43, 0x3f803f80\n"
    "buffer_load_dwordx4 v[176:179], %[vx], %[srdy], 0 offen offset:0\n"
    "buffer_load_dwordx4 v[180:183], %[vx], %[srdy], s41 offen offset:0\n"
    "buffer_load_dwordx4 v[184:187], %[vx], %[srdy], 0 offen offset:64\n"
    "buffer_load_dwordx4 v[188:191], %[vx], %[srdy], s41 offen offset:64\n"
    "buffer_load_dwordx4 v[192:195], %[vx], %[srdy], 0 offen offset:128\n"
    "buffer_load_dwordx4 v[196:199], %[vx], %[srdy], s41 offen offset:128\n"
    "buffer_load_dwordx4 v[200:203], %[vx], %[srdy], 0 offen offset:192\n"
    "buffer_load_dwordx4 v[204:207], %[vx], %[srdy], s41 offen offset:192\n"
    "buffer_load_dwordx4 v[208:211], %[vx], %[srdy], 0 offen offset:256\n"
    "buffer_load_dwordx4 v[212:215], %[vx], %[srdy], s41 offen offset:256\n"
    "s_waitcnt vmcnt(10)\n"
    "v_lshlrev_b32 v20, 16, v28\n"
    "v_and_b32 v21, 0xffff0000, v28\n"
    "v_lshlrev_b32 v22, 16, v29\n"
    "v_and_b32 v23, 0xffff0000, v29\n"
    "v_mul_f32 v20, 0xbeeff558, v20\n"
    "v_mul_f32 v21, 0xbeeff558, v21\n"
    "v_mul_f32 v22, 0xbeeff558, v22\n"
    "v_mul_f32 v23, 0xbeeff558, v23\n"
    "ds_write_b128 %[vbw], v[20:23]\n"
    "v_mov_b32 v24, 0\n"
    "v_mov_b32 v25, 0\n"
    "v_mov_b32 v26, 0\n"
    "v_mov_b32 v27, 0\n"
    "s_waitcnt vmcnt(8)\n"
    "buffer_load_dwordx4 v[216:219], %[vx], %[srdy], 0 offen offset:320\n"
    "buffer_load_dwordx4 v[220:223], %[vx], %[srdy], s41 offen offset:320\n"
    "ds_write_b128 %[xw], v[176:179] offset:0\n"
    "ds_write_b128 %[xw], v[180:183] offset:1024\n"
    "ds_read_b128 v[168:171], %[xr0]\n"
    "ds_read_b128 v[172:175], %[xr1]\n"
    "s_waitcnt vmcnt(8)\n"
    "buffer_load_dwordx4 v[224:227], %[vx], %[srdy], 0 offen offset:384\n"
    "buffer_load_dwordx4 v[228:231], %[vx], %[srdy], s41 offen offset:384\n"
    "ds_write_b128 %[xw], v[184:187] offset:0\n"
    "ds_write_b128 %[xw], v[188:191] offset:1024\n"
    "ds_read_b128 v[160:163], %[xr0]\n"
    "ds_read_b128 v[164:167], %[xr1]\n"
    "s_waitcnt lgkmcnt(4)\n"
    "v_dot2c_f32_bf16 v24, s43, v168\n"
    "v_dot2c_f32_bf16 v25, v168, v168\n"
    "v_dot2c_f32_bf16 v26, s43, v172\n"
    "v_dot2c_f32_bf16 v27, v172, v172\n"
    "v_dot2c_f32_bf16 v24, s43, v169\n"
    "v_dot2c_f32_bf16 v25, v169, v169\n"
    "v_dot2c_f32_bf16 v26, s43, v173\n"
    "v_dot2c_f32_bf16 v27, v173, v173\n"
    "v_dot2c_f32_bf16 v24, s43, v170\n"
    "v_dot2c_f32_bf16 v25, v170, v170\n"
    "v_dot2c_f32_bf16 v26, s43, v174\n"
    "v_dot2c_f32_bf16 v27, v174, v174\n"
    "v_dot2c_f32_bf16 v24, s43, v171\n"
    "v_dot2c_f32_bf16 v25, v171, v171\n"
    "v_dot2c_f32_bf16 v26, s43, v175\n"
    "v_dot2c_f32_bf16 v27, v175, v175\n"
    "s_waitcnt vmcnt(8)\n"
    "buffer_load_dwordx4 v[232:235], %[vx], %[srdy], 0 offen offset:448\n"
    "buffer_load_dwordx4 v[236:239], %[vx], %[srdy], s41 offen offset:448\n"
    "ds_write_b128 %[xw], v[192:195] offset:0\n"
    "ds_write_b128 %[xw], v[196:199] offset:1024\n"
    "ds_read_b128 v[152:155], %[xr0]\n"
    "ds_read_b128 v[156:159], %[xr1]\n"
    "s_waitcnt lgkmcnt(4)\n"
    "v_dot2c_f32_bf16 v24, s43, v160\n"
    "v_dot2c_f32_bf16 v25, v160, v160\n"
    "v_dot2c_f32_bf16 v26, s43, v164\n"
    "v_dot2c_f32_bf16 v27, v164, v164\n"
    "v_dot2c_f32_bf16 v24, s43, v161\n"
    "v_dot2c_f32_bf16 v25, v161, v161\n"
    "v_dot2c_f32_bf16 v26, s43, v165\n"
    "v_dot2c_f32_bf16 v27, v165, v165\n"
    "v_dot2c_f32_bf16 v24, s43, v162\n"
    "v_dot2c_f32_bf16 v25, v162, v162\n"
    "v_dot2c_f32_bf16 v26, s43, v166\n"
    "v_dot2c_f32_bf16 v27, v166, v166\n"
    "v_dot2c_f32_bf16 v24, s43, v163\n"
    "v_dot2c_f32_bf16 v25, v163, v163\n"
    "v_dot2c_f32_bf16 v26, s43, v167\n"
    "v_dot2c_f32_bf16 v27, v167, v167\n"
    "s_waitcnt vmcnt(8)\n"
    "buffer_load_dwordx4 v[240:243], %[vx], %[srdy], 0 offen offset:512\n"
    "buffer_load_dwordx4 v[244:247], %[vx], %[srdy], s41 offen offset:512\n"
    "ds_write_b128 %[xw], v[200:203] offset:0\n"
    "ds_write_b128 %[xw], v[204:207] offset:1024\n"
    "ds_read_b128 v[144:147], %[xr0]\n"
    "ds_read_b128 v[148:151], %[xr1]\n"
    "s_waitcnt lgkmcnt(4)\n"
    "v_dot2c_f32_bf16 v24, s43, v152\n"
    "v_dot2c_f32_bf16 v25, v152, v152\n"
    "v_dot2c_f32_bf16 v26, s43, v156\n"
    "v_dot2c_f32_bf16 v27, v156, v156\n"
    "v_dot2c_f32_bf16 v24, s43, v153\n"
    "v_dot2c_f32_bf16 v25, v153, v153\n"
    "v_dot2c_f32_bf16 v26, s43, v157\n"
    "v_dot2c_f32_bf16 v27, v157, v157\n"
    "v_dot2c_f32_bf16 v24, s43, v154\n"
    "v_dot2c_f32_bf16 v25, v154, v154\n"
    "v_dot2c_f32_bf16 v26, s43, v158\n"
    "v_dot2c_f32_bf16 v27, v158, v158\n"
    "v_dot2c_f32_bf16 v24, s43, v155\n"
    "v_dot2c_f32_bf16 v25, v155, v155\n"
    "v_dot2c_f32_bf16 v26, s43, v159\n"
    "v_dot2c_f32_bf16 v27, v159, v159\n"
    "s_waitcnt vmcnt(8)\n"
    "buffer_load_dwordx4 v[248:251], %[vx], %[srdy], 0 offen offset:576\n"
    "buffer_load_dwordx4 v[252:255], %[vx], %[srdy], s41 offen offset:576\n"
    "ds_write_b128 %[xw], v[208:211] offset:0\n"
    "ds_write_b128 %[xw], v[212:215] offset:1024\n"
    "ds_read_b128 v[136:139], %[xr0]\n"
    "ds_read_b128 v[140:143], %[xr1]\n"
    "s_waitcnt lgkmcnt(4)\n"
    "v_dot2c_f32_bf16 v24, s43, v144\n"
    "v_dot2c_f32_bf16 v25, v144, v144\n"
    "v_dot2c_f32_bf16 v26, s43, v148\n"
    "v_dot2c_f32_bf16 v27, v148, v148\n"
    "v_dot2c_f32_bf16 v24, s43, v145\n"
    "v_dot2c_f32_bf16 v25, v145, v145\n"
    "v_dot2c_f32_bf16 v26, s43, v149\n"
    "v_dot2c_f32_bf16 v27, v149, v149\n"
    "v_dot2c_f32_bf16 v24, s43, v146\n"
    "v_dot2c_f32_bf16 v25, v146, v146\n"
    "v_dot2c_f32_bf16 v26, s43, v150\n"
    "v_dot2c_f32_bf16 v27, v150, v150\n"
    "v_dot2c_f32_bf16 v24, s43, v147\n"
    "v_dot2c_f32_bf16 v25, v147, v147\n"
    "v_dot2c_f32_bf16 v26, s43, v151\n"
    "v_dot2c_f32_bf16 v27, v151, v151\n"
    "s_waitcnt vmcnt(8)\n"
    "buffer_load_dwordx4 v[176:179], %[vx], %[srdy], 0 offen offset:640\n"
    "buffer_load_dwordx4 v[180:183], %[vx], %[srdy], s41 offen offset:640\n"
    "ds_write_b128 %[xw], v[216:219] offset:0\n"
    "ds_write_b128 %[xw], v[220:223] offset:1024\n"
    "ds_read_b128 v[128:131], %[xr0]\n"
    "ds_read_b128 v[132:135], %[xr1]\n"
    "s_waitcnt lgkmcnt(4)\n"
    "v_dot2c_f32_bf16 v24, s43, v136\n"
    "v_dot2c_f32_bf16 v25, v136, v136\n"
    "v_dot2c_f32_bf16 v26, s43, v140\n"
    "v_dot2c_f32_bf16 v27, v140, v140\n"
    "v_dot2c_f32_bf16 v24, s43, v137\n"
    "v_dot2c_f32_bf16 v25, v137, v137\n"
    "v_dot2c_f32_bf16 v26, s43, v141\n"
    "v_dot2c_f32_bf16 v27, v141, v141\n"
    "v_dot2c_f32_bf16 v24, s43, v138\n"
    "v_dot2c_f32_bf16 v25, v138, v138\n"
    "v_dot2c_f32_bf16 v26, s43, v142\n"
    "v_dot2c_f32_bf16 v27, v142, v142\n"
    "v_dot2c_f32_bf16 v24, s43, v139\n"
    "v_dot2c_f32_bf16 v25, v139, v139\n"
    "v_dot2c_f32_bf16 v26, s43, v143\n"
    "v_dot2c_f32_bf16 v27, v143, v143\n"
    "s_waitcnt vmcnt(8)\n"
    "buffer_load_dwordx4 v[184:187], %[vx], %[srdy], 0 offen offset:704\n"
    "buffer_load_dwordx4 v[188:191], %[vx], %[srdy], s41 offen offset:704\n"
    "ds_write_b128 %[xw], v[224:227] offset:0\n"
    "ds_write_b128 %[xw], v[228:231] offset:1024\n"
    "ds_read_b128 v[120:123], %[xr0]\n"
    "ds_read_b128 v[124:127], %[xr1]\n"
    "s_waitcnt lgkmcnt(4)\n"
    "v_dot2c_f32_bf16 v24, s43, v128\n"
    "v_dot2c_f32_bf16 v25, v128, v128\n"
    "v_dot2c_f32_bf16 v26, s43, v132\n"
    "v_dot2c_f32_bf16 v27, v132, v132\n"
    "v_dot2c_f32_bf16 v24, s43, v129\n"
    "v_dot2c_f32_bf16 v25, v129, v129\n"
    "v_dot2c_f32_bf16 v26, s43, v133\n"
    "v_dot2c_f32_bf16 v27, v133, v133\n"
    "v_dot2c_f32_bf16 v24, s43, v130\n"
    "v_dot2c_f32_bf16 v25, v130, v130\n"
    "v_dot2c_f32_bf16 v26, s43, v134\n"
    "v_dot2c_f32_bf16 v27, v134, v134\n"
    "v_dot2c_f32_bf16 v24, s43, v131\n"
    "v_dot2c_f32_bf16 v25, v131, v131\n"
    "v_dot2c_f32_bf16 v26, s43, v135\n"
    "v_dot2c_f32_bf16 v27, v135, v135\n"
    "s_waitcnt vmcnt(8)\n"
    "buffer_load_dwordx4 v[192:195], %[vx], %[srdy], 0 offen offset:768\n"
    "buffer_load_dwordx4 v[196:199], %[vx], %[srdy], s41 offen offset:768\n"
    "ds_write_b128 %[xw], v[232:235] offset:0\n"
    "ds_write_b128 %[xw], v[236:239] offset:1024\n"
    "ds_read_b128 v[112:115], %[xr0]\n"
    "ds_read_b128 v[116:119], %[xr1]\n"
    "s_waitcnt lgkmcnt(4)\n"
    "v_dot2c_f32_bf16 v24, s43, v120\n"
    "v_dot2c_f32_bf16 v25, v120, v120\n"
    "v_dot2c_f32_bf16 v26, s43, v124\n"
    "v_dot2c_f32_bf16 v27, v124, v124\n"
    "v_dot2c_f32_bf16 v24, s43, v121\n"
    "v_dot2c_f32_bf16 v25, v121, v121\n"
    "v_dot2c_f32_bf16 v26, s43, v125\n"
    "v_dot2c_f32_bf16 v27, v125, v125\n"
    "v_dot2c_f32_bf16 v24, s43, v122\n"
    "v_dot2c_f32_bf16 v25, v122, v122\n"
    "v_dot2c_f32_bf16 v26, s43, v126\n"
    "v_dot2c_f32_bf16 v27, v126, v126\n"
    "v_dot2c_f32_bf16 v24, s43, v123\n"
    "v_dot2c_f32_bf16 v25, v123, v123\n"
    "v_dot2c_f32_bf16 v26, s43, v127\n"
    "v_dot2c_f32_bf16 v27, v127, v127\n"
    "s_waitcnt vmcnt(8)\n"
    "buffer_load_dwordx4 v[200:203], %[vx], %[srdy], 0 offen offset:832\n"
    "buffer_load_dwordx4 v[204:207], %[vx], %[srdy], s41 offen offset:832\n"
    "ds_write_b128 %[xw], v[240:243] offset:0\n"
    "ds_write_b128 %[xw], v[244:247] offset:1024\n"
    "ds_read_b128 v[104:107], %[xr0]\n"
    "ds_read_b128 v[108:111], %[xr1]\n"
    "s_waitcnt lgkmcnt(4)\n"
    "v_dot2c_f32_bf16 v24, s43, v112\n"
    "v_dot2c_f32_bf16 v25, v112, v112\n"
    "v_dot2c_f32_bf16 v26, s43, v116\n"
    "v_dot2c_f32_bf16 v27, v116, v116\n"
    "v_dot2c_f32_bf16 v24, s43, v113\n"
    "v_dot2c_f32_bf16 v25, v113, v113\n"
    "v_dot2c_f32_bf16 v26, s43, v117\n"
    "v_dot2c_f32_bf16 v27, v117, v117\n"
    "v_dot2c_f32_bf16 v24, s43, v114\n"
    "v_dot2c_f32_bf16 v25, v114, v114\n"
    "v_dot2c_f32_bf16 v26, s43, v118\n"
    "v_dot2c_f32_bf16 v27, v118, v118\n"
    "v_dot2c_f32_bf16 v24, s43, v115\n"
    "v_dot2c_f32_bf16 v25, v115, v115\n"
    "v_dot2c_f32_bf16 v26, s43, v119\n"
    "v_dot2c_f32_bf16 v27, v119, v119\n"
    "s_waitcnt vmcnt(8)\n"
    "buffer_load_dwordx4 v[208:211], %[vx], %[srdy], 0 offen offset:896\n"
    "buffer_load_dwordx4 v[212:215], %[vx], %[srdy], s41 offen offset:896\n"
    "ds_write_b128 %[xw], v[248:251] offset:0\n"
    "ds_write_b128 %[xw], v[252:255] offset:1024\n"
    "ds_read_b128 v[96:99], %[xr0]\n"
    "ds_read_b128 v[100:103], %[xr1]\n"
    "s_waitcnt lgkmcnt(4)\n"
    "v_dot2c_f32_bf16 v24, s43, v104\n"
    "v_dot2c_f32_bf16 v25, v104, v104\n"
    "v_dot2c_f32_bf16 v26, s43, v108\n"
    "v_dot2c_f32_bf16 v27, v108, v108\n"
    "v_dot2c_f32_bf16 v24, s43, v105\n"
    "v_dot2c_f32_bf16 v25, v105, v105\n"
    "v_dot2c_f32_bf16 v26, s43, v109\n"
    "v_dot2c_f32_bf16 v27, v109, v109\n"
    "v_dot2c_f32_bf16 v24, s43, v106\n"
    "v_dot2c_f32_bf16 v25, v106, v106\n"
    "v_dot2c_f32_bf16 v26, s43, v110\n"
    "v_dot2c_f32_bf16 v27, v110, v110\n"
    "v_dot2c_f32_bf16 v24, s43, v107\n"
    "v_dot2c_f32_bf16 v25, v107, v107\n"
    "v_dot2c_f32_bf16 v26, s43, v111\n"
    "v_dot2c_f32_bf16 v27, v111, v111\n"
    "s_waitcnt vmcnt(8)\n"
    "buffer_load_dwordx4 v[216:219], %[vx], %[srdy], 0 offen offset:960\n"
    "buffer_load_dwordx4 v[220:223], %[vx], %[srdy], s41 offen offset:960\n"
    "ds_write_b128 %[xw], v[176:179] offset:0\n"
    "ds_write_b128 %[xw], v[180:183] offset:1024\n"
    "ds_read_b128 v[88:91], %[xr0]\n"
    "ds_read_b128 v[92:95], %[xr1]\n"
    "s_waitcnt lgkmcnt(4)\n"
    "v_dot2c_f32_bf16 v24, s43, v96\n"
    "v_dot2c_f32_bf16 v25, v96, v96\n"
    "v_dot2c_f32_bf16 v26, s43, v100\n"
    "v_dot2c_f32_bf16 v27, v100, v100\n"
    "v_dot2c_f32_bf16 v24, s43, v97\n"
    "v_dot2c_f32_bf16 v25, v97, v97\n"
    "v_dot2c_f32_bf16 v26, s43, v101\n"
    "v_dot2c_f32_bf16 v27, v101, v101\n"
    "v_dot2c_f32_bf16 v24, s43, v98\n"
    "v_dot2c_f32_bf16 v25, v98, v98\n"
    "v_dot2c_f32_bf16 v26, s43, v102\n"
    "v_dot2c_f32_bf16 v27, v102, v102\n"
    "v_dot2c_f32_bf16 v24, s43, v99\n"
    "v_dot2c_f32_bf16 v25, v99, v99\n"
    "v_dot2c_f32_bf16 v26, s43, v103\n"
    "v_dot2c_f32_bf16 v27, v103, v103\n"
    "s_waitcnt vmcnt(8)\n"
    "buffer_load_dwordx4 v[224:227], %[vx], %[srdy], 0 offen offset:1024\n"
    "buffer_load_dwordx4 v[228:231], %[vx], %[srdy], s41 offen offset:1024\n"
    "ds_write_b128 %[xw], v[184:187] offset:0\n"
    "ds_write_b128 %[xw], v[188:191] offset:1024\n"
    "ds_read_b128 v[80:83], %[xr0]\n"
    "ds_read_b128 v[84:87], %[xr1]\n"
    "s_waitcnt lgkmcnt(4)\n"
    "v_dot2c_f32_bf16 v24, s43, v88\n"
    "v_dot2c_f32_bf16 v25, v88, v88\n"
    "v_dot2c_f32_bf16 v26, s43, v92\n"
    "v_dot2c_f32_bf16 v27, v92, v92\n"
    "v_dot2c_f32_bf16 v24, s43, v89\n"
    "v_dot2c_f32_bf16 v25, v89, v89\n"
    "v_dot2c_f32_bf16 v26, s43, v93\n"
    "v_dot2c_f32_bf16 v27, v93, v93\n"
    "v_dot2c_f32_bf16 v24, s43, v90\n"
    "v_dot2c_f32_bf16 v25, v90, v90\n"
    "v_dot2c_f32_bf16 v26, s43, v94\n"
    "v_dot2c_f32_bf16 v27, v94, v94\n"
    "v_dot2c_f32_bf16 v24, s43, v91\n"
    "v_dot2c_f32_bf16 v25, v91, v91\n"
    "v_dot2c_f32_bf16 v26, s43, v95\n"
    "v_dot2c_f32_bf16 v27, v95, v95\n"
    "s_waitcnt vmcnt(8)\n"
    "buffer_load_dwordx4 v[232:235], %[vx], %[srdy], 0 offen offset:1088\n"
    "buffer_load_dwordx4 v[236:239], %[vx], %[srdy], s41 offen offset:1088\n"
    "ds_write_b128 %[xw], v[192:195] offset:0\n"
    "ds_write_b128 %[xw], v[196:199] offset:1024\n"
    "ds_read_b128 v[72:75], %[xr0]\n"
    "ds_read_b128 v[76:79], %[xr1]\n"
    "s_waitcnt lgkmcnt(4)\n"
    "v_dot2c_f32_bf16 v24, s43, v80\n"
    "v_dot2c_f32_bf16 v25, v80, v80\n"
    "v_dot2c_f32_bf16 v26, s43, v84\n"
    "v_dot2c_f32_bf16 v27, v84, v84\n"
    "v_dot2c_f32_bf16 v24, s43, v81\n"
    "v_dot2c_f32_bf16 v25, v81, v81\n"
    "v_dot2c_f32_bf16 v26, s43, v85\n"
    "v_dot2c_f32_bf16 v27, v85, v85\n"
    "v_dot2c_f32_bf16 v24, s43, v82\n"
    "v_dot2c_f32_bf16 v25, v82, v82\n"
    "v_dot2c_f32_bf16 v26, s43, v86\n"
    "v_dot2c_f32_bf16 v27, v86, v86\n"
    "v_dot2c_f32_bf16 v24, s43, v83\n"
    "v_dot2c_f32_bf16 v25, v83, v83\n"
    "v_dot2c_f32_bf16 v26, s43, v87\n"
    "v_dot2c_f32_bf16 v27, v87, v87\n"
    "s_waitcnt vmcnt(8)\n"
    "s_add_u32 m0, %[ldsw], 0\n"
    "s_mov_b32 s40, 0\n"
    "buffer_load_dwordx4 %[w1a], %[srd1], s40 offen lds\n"
    "buffer_load_dwordx4 %[w1b], %[srd1], s40 offen offset:1024 lds\n"
    "ds_write_b128 %[xw], v[200:203] offset:0\n"
    "ds_write_b128 %[xw], v[204:207] offset:1024\n"
    "ds_read_b128 v[64:67], %[xr0]\n"
    "ds_read_b128 v[68:71], %[xr1]\n"
    "s_waitcnt lgkmcnt(4)\n"
    "v_dot2c_f32_bf16 v24, s43, v72\n"
    "v_dot2c_f32_bf16 v25, v72, v72\n"
    "v_dot2c_f32_bf16 v26, s43, v76\n"
    "v_dot2c_f32_bf16 v27, v76, v76\n"
    "v_dot2c_f32_bf16 v24, s43, v73\n"
    "v_dot2c_f32_bf16 v25, v73, v73\n"
    "v_dot2c_f32_bf16 v26, s43, v77\n"
    "v_dot2c_f32_bf16 v27, v77, v77\n"
    "v_dot2c_f32_bf16 v24, s43, v74\n"
    "v_dot2c_f32_bf16 v25, v74, v74\n"
    "v_dot2c_f32_bf16 v26, s43, v78\n"
    "v_dot2c_f32_bf16 v27, v78, v78\n"
    "v_dot2c_f32_bf16 v24, s43, v75\n"
    "v_dot2c_f32_bf16 v25, v75, v75\n"
    "v_dot2c_f32_bf16 v26, s43, v79\n"
    "v_dot2c_f32_bf16 v27, v79, v79\n"
    "s_waitcnt vmcnt(8)\n"
    "s_add_u32 m0, %[ldsw], 8192\n"
    "s_mov_b32 s40, 589824\n"
    "buffer_load_dwordx4 %[w1a], %[srd1], s40 offen lds\n"
    "buffer_load_dwordx4 %[w1b], %[srd1], s40 offen offset:1024 lds\n"
    "ds_write_b128 %[xw], v[208:211] offset:0\n"
    "ds_write_b128 %[xw], v[212:215] offset:1024\n"
    "ds_read_b128 v[56:59], %[xr0]\n"
    "ds_read_b128 v[60:63], %[xr1]\n"
    "s_waitcnt lgkmcnt(4)\n"
    "v_dot2c_f32_bf16 v24, s43, v64\n"
    "v_dot2c_f32_bf16 v25, v64, v64\n"
    "v_dot2c_f32_bf16 v26, s43, v68\n"
    "v_dot2c_f32_bf16 v27, v68, v68\n"
    "v_dot2c_f32_bf16 v24, s43, v65\n"
    "v_dot2c_f32_bf16 v25, v65, v65\n"
    "v_dot2c_f32_bf16 v26, s43, v69\n"
    "v_dot2c_f32_bf16 v27, v69, v69\n"
    "v_dot2c_f32_bf16 v24, s43, v66\n"
    "v_dot2c_f32_bf16 v25, v66, v66\n"
    "v_dot2c_f32_bf16 v26, s43, v70\n"
    "v_dot2c_f32_bf16 v27, v70, v70\n"
    "v_dot2c_f32_bf16 v24, s43, v67\n"
    "v_dot2c_f32_bf16 v25, v67, v67\n"
    "v_dot2c_f32_bf16 v26, s43, v71\n"
    "v_dot2c_f32_bf16 v27, v71, v71\n"
    "s_waitcnt vmcnt(8)\n"
    "s_add_u32 m0, %[ldsw], 16384\n"
    "s_mov_b32 s40, 1179648\n"
    "buffer_load_dwordx4 %[w1a], %[srd1], s40 offen lds\n"
    "buffer_load_dwordx4 %[w1b], %[srd1], s40 offen offset:1024 lds\n"
    "ds_write_b128 %[xw], v[216:219] offset:0\n"
    "ds_write_b128 %[xw], v[220:223] offset:1024\n"
    "ds_read_b128 v[48:51], %[xr0]\n"
    "ds_read_b128 v[52:55], %[xr1]\n"
    "s_waitcnt lgkmcnt(4)\n"
    "v_dot2c_f32_bf16 v24, s43, v56\n"
    "v_dot2c_f32_bf16 v25, v56, v56\n"
    "v_dot2c_f32_bf16 v26, s43, v60\n"
    "v_dot2c_f32_bf16 v27, v60, v60\n"
    "v_dot2c_f32_bf16 v24, s43, v57\n"
    "v_dot2c_f32_bf16 v25, v57, v57\n"
    "v_dot2c_f32_bf16 v26, s43, v61\n"
    "v_dot2c_f32_bf16 v27, v61, v61\n"
    "v_dot2c_f32_bf16 v24, s43, v58\n"
    "v_dot2c_f32_bf16 v25, v58, v58\n"
    "v_dot2c_f32_bf16 v26, s43, v62\n"
    "v_dot2c_f32_bf16 v27, v62, v62\n"
    "v_dot2c_f32_bf16 v24, s43, v59\n"
    "v_dot2c_f32_bf16 v25, v59, v59\n"
    "v_dot2c_f32_bf16 v26, s43, v63\n"
    "v_dot2c_f32_bf16 v27, v63, v63\n"
    "s_waitcnt vmcnt(8)\n"
    "s_add_u32 m0, %[ldsw], 24576\n"
    "s_mov_b32 s40, 1769472\n"
    "buffer_load_dwordx4 %[w1a], %[srd1], s40 offen lds\n"
    "buffer_load_dwordx4 %[w1b], %[srd1], s40 offen offset:1024 lds\n"
    "ds_write_b128 %[xw], v[224:227] offset:0\n"
    "ds_write_b128 %[xw], v[228:231] offset:1024\n"
    "ds_read_b128 v[40:43], %[xr0]\n"
    "ds_read_b128 v[44:47], %[xr1]\n"
    "s_waitcnt lgkmcnt(4)\n"
    "v_dot2c_f32_bf16 v24, s43, v48\n"
    "v_dot2c_f32_bf16 v25, v48, v48\n"
    "v_dot2c_f32_bf16 v26, s43, v52\n"
    "v_dot2c_f32_bf16 v27, v52, v52\n"
    "v_dot2c_f32_bf16 v24, s43, v49\n"
    "v_dot2c_f32_bf16 v25, v49, v49\n"
    "v_dot2c_f32_bf16 v26, s43, v53\n"
    "v_dot2c_f32_bf16 v27, v53, v53\n"
    "v_dot2c_f32_bf16 v24, s43, v50\n"
    "v_dot2c_f32_bf16 v25, v50, v50\n"
    "v_dot2c_f32_bf16 v26, s43, v54\n"
    "v_dot2c_f32_bf16 v27, v54, v54\n"
    "v_dot2c_f32_bf16 v24, s43, v51\n"
    "v_dot2c_f32_bf16 v25, v51, v51\n"
    "v_dot2c_f32_bf16 v26, s43, v55\n"
    "v_dot2c_f32_bf16 v27, v55, v55\n"
    "s_waitcnt vmcnt(8)\n"
    "s_add_u32 m0, %[ldsw], 32768\n"
    "s_mov_b32 s40, 2359296\n"
    "buffer_load_dwordx4 %[w1a], %[srd1], s40 offen lds\n"
    "buffer_load_dwordx4 %[w1b], %[srd1], s40 offen offset:1024 lds\n"
    "ds_write_b128 %[xw], v[232:235] offset:0\n"
    "ds_write_b128 %[xw], v[236:239] offset:1024\n"
    "ds_read_b128 v[32:35], %[xr0]\n"
    "ds_read_b128 v[36:39], %[xr1]\n"
    "s_waitcnt lgkmcnt(4)\n"
    "v_dot2c_f32_bf16 v24, s43, v40\n"
    "v_dot2c_f32_bf16 v25, v40, v40\n"
    "v_dot2c_f32_bf16 v26, s43, v44\n"
    "v_dot2c_f32_bf16 v27, v44, v44\n"
    "v_dot2c_f32_bf16 v24, s43, v41\n"
    "v_dot2c_f32_bf16 v25, v41, v41\n"
    "v_dot2c_f32_bf16 v26, s43, v45\n"
    "v_dot2c_f32_bf16 v27, v45, v45\n"
    "v_dot2c_f32_bf16 v24, s43, v42\n"
    "v_dot2c_f32_bf16 v25, v42, v42\n"
    "v_dot2c_f32_bf16 v26, s43, v46\n"
    "v_dot2c_f32_bf16 v27, v46, v46\n"
    "v_dot2c_f32_bf16 v24, s43, v43\n"
    "v_dot2c_f32_bf16 v25, v43, v43\n"
    "v_dot2c_f32_bf16 v26, s43, v47\n"
    "v_dot2c_f32_bf16 v27, v47, v47\n"
    "s_waitcnt lgkmcnt(0)\n"
    "v_dot2c_f32_bf16 v24, s43, v32\n"
    "v_dot2c_f32_bf16 v25, v32, v32\n"
    "v_dot2c_f32_bf16 v26, s43, v36\n"
    "v_dot2c_f32_bf16 v27, v36, v36\n"
    "v_dot2c_f32_bf16 v24, s43, v33\n"
    "v_dot2c_f32_bf16 v25, v33, v33\n"
    "v_dot2c_f32_bf16 v26, s43, v37\n"
    "v_dot2c_f32_bf16 v27, v37, v37\n"
    "v_dot2c_f32_bf16 v24, s43, v34\n"
    "v_dot2c_f32_bf16 v25, v34, v34\n"
    "v_dot2c_f32_bf16 v26, s43, v38\n"
    "v_dot2c_f32_bf16 v27, v38, v38\n"
    "v_dot2c_f32_bf16 v24, s43, v35\n"
    "v_dot2c_f32_bf16 v25, v35, v35\n"
    "v_dot2c_f32_bf16 v26, s43, v39\n"
    "v_dot2c_f32_bf16 v27, v39, v39\n"
    "s_nop 3\n"
    "v_add_f32 v24, v24, v26\n"
    "v_add_f32 v25, v25, v27\n"
    "v_mov_b32 v26, v24\n"
    "v_mov_b32 v27, v25\n"
    "s_nop 1\n"
    "v_permlane32_swap_b32 v24, v26\n"
    "v_permlane32_swap_b32 v25, v27\n"
    "s_nop 1\n"
    "v_add_f32 v24, v24, v26\n"
    "v_add_f32 v25, v25, v27\n"
    "v_mul_f32 v24, 0x3ae38e39, v24\n"
    "v_mul_f32 v25, 0x3ae38e39, v25\n"
    "v_mul_f32 v26, 0x3ae38e39, v26\n"
    "v_mul_f32 v27, 0x3ae38e39, v27\n"
    "v_fma_f32 v25, -v24, v24, v25\n"
    "v_fma_f32 v27, -v26, v26, v27\n"
    "v_max_f32 v25, 0, v25\n"
    "v_max_f32 v27, 0, v27\n"
    "v_add_f32 v25, 0x3727c5ac, v25\n"
    "v_add_f32 v27, 0x3727c5ac, v27\n"
    "v_rsq_f32 v25, v25\n"
    "v_rsq_f32 v27, v27\n"
    "s_nop 1\n"
    "v_mul_f32 v28, 0xbeeff558, v25\n"
    "v_mul_f32 v30, 0xbeeff558, v27\n"
    "v_mul_f32 v29, v24, v28\n"
    "v_mul_f32 v31, v26, v30\n"
    "v_sub_f32 v29, 0, v29\n"
    "v_sub_f32 v31, 0, v31\n"
    "s_waitcnt lgkmcnt(0)\n"
    "v_lshlrev_b32 v20, 16, v168\n"
    "s_add_u32 m0, %[ldsw], 40960\n"
    "s_mov_b32 s40, 2949120\n"
    "buffer_load_dwordx4 %[w1a], %[srd1], s40 offen lds\n"
    "v_and_b32 v21, s42, v168\n"
    "v_lshlrev_b32 v22, 16, v169\n"
    "v_and_b32 v23, s42, v169\n"
    "v_lshlrev_b32 v24, 16, v170\n"
    "v_and_b32 v25, s42, v170\n"
    "buffer_load_dwordx4 %[w1b], %[srd1], s40 offen offset:1024 lds\n"
    "v_lshlrev_b32 v26, 16, v171\n"
    "v_and_b32 v27, s42, v171\n"
    "v_pk_fma_f32 v[20:21], v[20:21], v[28:29], v[28:29] op_sel:[0,0,1] op_sel_hi:[1,0,1]\n"
    "v_pk_fma_f32 v[22:23], v[22:23], v[28:29], v[28:29] op_sel:[0,0,1] op_sel_hi:[1,0,1]\n"
    "v_pk_fma_f32 v[24:25], v[24:25], v[28:29], v[28:29] op_sel:[0,0,1] op_sel_hi:[1,0,1]\n"
    "s_add_u32 m0, %[ldsw], 49152\n"
    "s_mov_b32 s40, 3538944\n"
    "buffer_load_dwordx4 %[w1a], %[srd1], s40 offen lds\n"
    "v_pk_fma_f32 v[26:27], v[26:27], v[28:29], v[28:29] op_sel:[0,0,1] op_sel_hi:[1,0,1]\n"
    "v_cvt_pk_bf16_f32 v168, v20, v21\n"
    "v_cvt_pk_bf16_f32 v169, v22, v23\n"
    "v_cvt_pk_bf16_f32 v170, v24, v25\n"
    "v_cvt_pk_bf16_f32 v171, v26, v27\n"
    "buffer_load_dwordx4 %[w1b], %[srd1], s40 offen offset:1024 lds\n"
    "v_accvgpr_write_b32 a0, v168\n"
    "v_accvgpr_write_b32 a1, v169\n"
    "v_accvgpr_write_b32 a2, v170\n"
    "v_accvgpr_write_b32 a3, v171\n"
    "v_lshlrev_b32 v20, 16, v172\n"
    "s_add_u32 m0, %[ldsw], 57344\n"
    "s_mov_b32 s40, 4128768\n"
    "buffer_load_dwordx4 %[w1a], %[srd1], s40 offen lds\n"
    "v_and_b32 v21, s42, v172\n"
    "v_lshlrev_b32 v22, 16, v173\n"
    "v_and_b32 v23, s42, v173\n"
    "v_lshlrev_b32 v24, 16, v174\n"
    "v_and_b32 v25, s42, v174\n"
    "buffer_load_dwordx4 %[w1b], %[srd1], s40 offen offset:1024 lds\n"
    "v_lshlrev_b32 v26, 16, v175\n"
    "v_and_b32 v27, s42, v175\n"
    "v_pk_fma_f32 v[20:21], v[20:21], v[28:29], v[28:29] op_sel:[0,0,1] op_sel_hi:[1,0,1]\n"
    "v_pk_fma_f32 v[22:23], v[22:23], v[28:29], v[28:29] op_sel:[0,0,1] op_sel_hi:[1,0,1]\n"
    "v_pk_fma_f32 v[24:25], v[24:25], v[28:29], v[28:29] op_sel:[0,0,1] op_sel_hi:[1,0,1]\n"
    "s_add_u32 m0, %[ldsw], 65536\n"
    "s_mov_b32 s40, 4718592\n"
    "buffer_load_dwordx4 %[w1a], %[srd1], s40 offen lds\n"
    "v_pk_fma_f32 v[26:27], v[26:27], v[28:29], v[28:29] op_sel:[0,0,1] op_sel_hi:[1,0,1]\n"
    "v_cvt_pk_bf16_f32 v172, v20, v21\n"
    "v_cvt_pk_bf16_f32 v173, v22, v23\n"
    "v_cvt_pk_bf16_f32 v174, v24, v25\n"
    "v_cvt_pk_bf16_f32 v175, v26, v27\n"
    "buffer_load_dwordx4 %[w1b], %[srd1], s40 offen offset:1024 lds\n"
    "v_accvgpr_write_b32 a4, v172\n"
    "v_accvgpr_write_b32 a5, v173\n"
    "v_accvgpr_write_b32 a6, v174\n"
    "v_accvgpr_write_b32 a7, v175\n"
    "v_lshlrev_b32 v20, 16, v160\n"
    "s_add_u32 m0, %[ldsw], 73728\n"
    "s_mov_b32 s40, 128\n"
    "buffer_load_dwordx4 %[w1a], %[srd1], s40 offen lds\n"
    "v_and_b32 v21, s42, v160\n"
    "v_lshlrev_b32 v22, 16, v161\n"
    "v_and_b32 v23, s42, v161\n"
    "v_lshlrev_b32 v24, 16, v162\n"
    "v_and_b32 v25, s42, v162\n"
    "buffer_load_dwordx4 %[w1b], %[srd1], s40 offen offset:1024 lds\n"
    "v_lshlrev_b32 v26, 16, v163\n"
    "v_and_b32 v27, s42, v163\n"
    "v_pk_fma_f32 v[20:21], v[20:21], v[28:29], v[28:29] op_sel:[0,0,1] op_sel_hi:[1,0,1]\n"
    "v_pk_fma_f32 v[22:23], v[22:23], v[28:29], v[28:29] op_sel:[0,0,1] op_sel_hi:[1,0,1]\n"
    "v_pk_fma_f32 v[24:25], v[24:25], v[28:29], v[28:29] op_sel:[0,0,1] op_sel_hi:[1,0,1]\n"
    "s_add_u32 m0, %[ldsw], 81920\n"
    "s_mov_b32 s40, 589952\n"
    "buffer_load_dwordx4 %[w1a], %[srd1], s40 offen lds\n"
    "v_pk_fma_f32 v[26:27], v[26:27], v[28:29], v[28:29] op_sel:[0,0,1] op_sel_hi:[1,0,1]\n"
    "v_cvt_pk_bf16_f32 v160, v20, v21\n"
    "v_cvt_pk_bf16_f32 v161, v22, v23\n"
    "v_cvt_pk_bf16_f32 v162, v24, v25\n"
    "v_cvt_pk_bf16_f32 v163, v26, v27\n"
    "buffer_load_dwordx4 %[w1b], %[srd1], s40 offen offset:1024 lds\n"
    "v_accvgpr_write_b32 a8, v160\n"
    "v_accvgpr_write_b32 a9, v161\n"
    "v_accvgpr_write_b32 a10, v162\n"
    "v_accvgpr_write_b32 a11, v163\n"
    "v_lshlrev_b32 v20, 16, v164\n"
    "s_add_u32 m0, %[ldsw], 90112\n"
    "s_mov_b32 s40, 1179776\n"
    "buffer_load_dwordx4 %[w1a], %[srd1], s40 offen lds\n"
    "v_and_b32 v21, s42, v164\n"
    "v_lshlrev_b32 v22, 16, v165\n"
    "v_and_b32 v23, s42, v165\n"
    "v_lshlrev_b32 v24, 16, v166\n"
    "v_and_b32 v25, s42, v166\n"
    "buffer_load_dwordx4 %[w1b], %[srd1], s40 offen offset:1024 lds\n"
    "v_lshlrev_b32 v26, 16, v167\n"
    "v_and_b32 v27, s42, v167\n"
    "v_pk_fma_f32 v[20:21], v[20:21], v[28:29], v[28:29] op_sel:[0,0,1] op_sel_hi:[1,0,1]\n"
    "v_pk_fma_f32 v[22:23], v[22:23], v[28:29], v[28:29] op_sel:[0,0,1] op_sel_hi:[1,0,1]\n"
    "v_pk_fma_f32 v[24:25], v[24:25], v[28:29], v[28:29] op_sel:[0,0,1] op_sel_hi:[1,0,1]\n"
    "v_pk_fma_f32 v[26:27], v[26:27], v[28:29], v[28:29] op_sel:[0,0,1] op_sel_hi:[1,0,1]\n"
    "v_cvt_pk_bf16_f32 v164, v20, v21\n"
    "v_cvt_pk_bf16_f32 v165, v22, v23\n"
    "v_cvt_pk_bf16_f32 v166, v24, v25\n"
    "v_cvt_pk_bf16_f32 v167, v26, v27\n"
    "v_accvgpr_write_b32 a12, v164\n"
    "v_accvgpr_write_b32 a13, v165\n"
    "v_accvgpr_write_b32 a14, v166\n"
    "v_accvgpr_write_b32 a15, v167\n"
    "s_waitcnt vmcnt(18)\n"
    "s_barrier\n"
    "ds_read_b128 v[176:179], %[vb] offset:0\n"
    "ds_read_b128 v[180:183], %[vb] offset:32\n"
    "ds_read_b128 v[184:187], %[vb] offset:64\n"
    "ds_read_b128 v[188:191], %[vb] offset:96\n"
    "ds_read_b128 v[192:195], %[vb] offset:128\n"
    "ds_read_b128 v[196:199], %[vb] offset:160\n"
    "ds_read_b128 v[200:203], %[vb] offset:192\n"
    "ds_read_b128 v[204:207], %[vb] offset:224\n"
    "ds_read_b64_tr_b16 a[224:225], %[rd0] offset:0\n"
    "ds_read_b64_tr_b16 a[226:227], %[rd0] offset:1024\n"
    "ds_read_b64_tr_b16 a[228:229], %[rd1] offset:0\n"
    "ds_read_b64_tr_b16 a[230:231], %[rd1] offset:1024\n"
    "ds_read_b64_tr_b16 a[232:233], %[rd0] offset:2048\n"
    "ds_read_b64_tr_b16 a[234:235], %[rd0] offset:3072\n"
    "ds_read_b64_tr_b16 a[236:237], %[rd1] offset:2048\n"
    "s_waitcnt lgkmcnt(11)\n"
    "ds_read_b64_tr_b16 a[238:239], %[rd1] offset:3072\n"
    "ds_read_b64_tr_b16 a[240:241], %[rd0] offset:4096\n"
    "ds_read_b64_tr_b16 a[242:243], %[rd0] offset:5120\n"
    "ds_read_b64_tr_b16 a[244:245], %[rd1] offset:4096\n"
    "s_waitcnt lgkmcnt(11)\n"
    "ds_read_b64_tr_b16 a[246:247], %[rd1] offset:5120\n"
    "ds_read_b64_tr_b16 a[248:249], %[rd0] offset:6144\n"
    "ds_read_b64_tr_b16 a[250:251], %[rd0] offset:7168\n"
    "s_waitcnt lgkmcnt(6)\n"
    "v_mfma_f32_32x32x16_bf16 v[176:191], a[224:227], a[0:3], v[176:191]\n"
    "ds_read_b64_tr_b16 a[252:253], %[rd1] offset:6144\n"
    "ds_read_b64_tr_b16 a[254:255], %[rd1] offset:7168\n"
    "s_add_u32 m0, %[ldsw], 98304\n"
    "s_mov_b32 s40, 1769600\n"
    "v_lshlrev_b32 v20, 16, v152\n"
    "v_and_b32 v21, s42, v152\n"
    "v_lshlrev_b32 v22, 16, v153\n"
    "v_and_b32 v23, s42, v153\n"
    "v_lshlrev_b32 v24, 16, v154\n"
    "v_and_b32 v25, s42, v154\n"
    "v_lshlrev_b32 v26, 16, v155\n"
    "v_and_b32 v27, s42, v155\n"
    "v_pk_fma_f32 v[20:21], v[20:21], v[28:29], v[28:29] op_sel:[0,0,1] op_sel_hi:[1,0,1]\n"
    "v_pk_fma_f32 v[22:23], v[22:23], v[28:29], v[28:29] op_sel:[0,0,1] op_sel_hi:[1,0,1]\n"
    "v_mfma_f32_32x32x16_bf16 v[192:207], a[228:231], a[0:3], v[192:207]\n"
    "ds_read_b64_tr_b16 a[224:225], %[rd0] offset:8192\n"
    "ds_read_b64_tr_b16 a[226:227], %[rd0] offset:9216\n"
    "buffer_load_dwordx4 %[w1a], %[srd1], s40 offen lds\n"
    "v_pk_fma_f32 v[24:25], v[24:25], v[28:29], v[28:29] op_sel:[0,0,1] op_sel_hi:[1,0,1]\n"
    "v_pk_fma_f32 v[26:27], v[26:27], v[28:29], v[28:29] op_sel:[0,0,1] op_sel_hi:[1,0,1]\n"
    "v_cvt_pk_bf16_f32 v152, v20, v21\n"
    "v_cvt_pk_bf16_f32 v153, v22, v23\n"
    "v_cvt_pk_bf16_f32 v154, v24, v25\n"
    "v_cvt_pk_bf16_f32 v155, v26, v27\n"
    "v_accvgpr_write_b32 a16, v152\n"
    "v_accvgpr_write_b32 a17, v153\n"
    "v_accvgpr_write_b32 a18, v154\n"
    "v_accvgpr_write_b32 a19, v155\n"
    "v_mfma_f32_32x32x16_bf16 v[176:191], a[232:235], a[4:7], v[176:191]\n"
    "ds_read_b64_tr_b16 a[228:229], %[rd1] offset:8192\n"
    "ds_read_b64_tr_b16 a[230:231], %[rd1] offset:9216\n"
    "v_lshlrev_b32 v20, 16, v156\n"
    "v_and_b32 v21, s42, v156\n"
    "v_lshlrev_b32 v22, 16, v157\n"
    "v_and_b32 v23, s42, v157\n"
    "v_lshlrev_b32 v24, 16, v158\n"
    "v_and_b32 v25, s42, v158\n"
    "v_lshlrev_b32 v26, 16, v159\n"
    "v_and_b32 v27, s42, v159\n"
    "v_pk_fma_f32 v[20:21], v[20:21], v[28:29], v[28:29] op_sel:[0,0,1] op_sel_hi:[1,0,1]\n"
    "v_pk_fma_f32 v[22:23], v[22:23], v[28:29], v[28:29] op_sel:[0,0,1] op_sel_hi:[1,0,1]\n"
    "v_mfma_f32_32x32x16_bf16 v[192:207], a[236:239], a[4:7], v[192:207]\n"
    "s_waitcnt lgkmcnt(4)\n"
    "ds_read_b64_tr_b16 a[232:233], %[rd0] offset:10240\n"
    "ds_read_b64_tr_b16 a[234:235], %[rd0] offset:11264\n"
    "buffer_load_dwordx4 %[w1b], %[srd1], s40 offen offset:1024 lds\n"
    "v_pk_fma_f32 v[24:25], v[24:25], v[28:29], v[28:29] op_sel:[0,0,1] op_sel_hi:[1,0,1]\n"
    "v_pk_fma_f32 v[26:27], v[26:27], v[28:29], v[28:29] op_sel:[0,0,1] op_sel_hi:[1,0,1]\n"
    "v_cvt_pk_bf16_f32 v156, v20, v21\n"
    "v_cvt_pk_bf16_f32 v157, v22, v23\n"
    "v_cvt_pk_bf16_f32 v158, v24, v25\n"
    "v_cvt_pk_bf16_f32 v159, v26, v27\n"
    "v_accvgpr_write_b32 a20, v156\n"
    "v_accvgpr_write_b32 a21, v157\n"
    "v_accvgpr_write_b32 a22, v158\n"
    "v_accvgpr_write_b32 a23, v159\n"
    "v_mfma_f32_32x32x16_bf16 v[176:191], a[240:243], a[8:11], v[176:191]\n"
    "ds_read_b64_tr_b16 a[236:237], %[rd1] offset:10240\n"
    "ds_read_b64_tr_b16 a[238:239], %[rd1] offset:11264\n"
    "v_lshlrev_b32 v20, 16, v144\n"
    "v_and_b32 v21, s42, v144\n"
    "v_lshlrev_b32 v22, 16, v145\n"
    "v_and_b32 v23, s42, v145\n"
    "v_lshlrev_b32 v24, 16, v146\n"
    "v_and_b32 v25, s42, v146\n"
    "v_lshlrev_b32 v26, 16, v147\n"
    "v_and_b32 v27, s42, v147\n"
    "v_pk_fma_f32 v[20:21], v[20:21], v[28:29], v[28:29] op_sel:[0,0,1] op_sel_hi:[1,0,1]\n"
    "v_pk_fma_f32 v[22:23], v[22:23], v[28:29], v[28:29] op_sel:[0,0,1] op_sel_hi:[1,0,1]\n"
    "v_mfma_f32_32x32x16_bf16 v[192:207], a[244:247], a[8:11], v[192:207]\n"
    "ds_read_b64_tr_b16 a[240:241], %[rd0] offset:12288\n"
    "ds_read_b64_tr_b16 a[242:243], %[rd0] offset:13312\n"
    "s_add_u32 m0, %[ldsw], 106496\n"
    "s_mov_b32 s40, 2359424\n"
    "v_pk_fma_f32 v[24:25], v[24:25], v[28:29], v[28:29] op_sel:[0,0,1] op_sel_hi:[1,0,1]\n"
    "v_pk_fma_f32 v[26:27], v[26:27], v[28:29], v[28:29] op_sel:[0,0,1] op_sel_hi:[1,0,1]\n"
    "v_cvt_pk_bf16_f32 v144, v20, v21\n"
    "v_cvt_pk_bf16_f32 v145, v22, v23\n"
    "v_cvt_pk_bf16_f32 v146, v24, v25\n"
    "v_cvt_pk_bf16_f32 v147, v26, v27\n"
    "v_accvgpr_write_b32 a24, v144\n"
    "v_accvgpr_write_b32 a25, v145\n"
    "v_accvgpr_write_b32 a26, v146\n"
    "v_accvgpr_write_b32 a27, v147\n"
    "v_mfma_f32_32x32x16_bf16 v[176:191], a[248:251], a[12:15], v[176:191]\n"
    "ds_read_b64_tr_b16 a[244:245], %[rd1] offset:12288\n"
    "ds_read_b64_tr_b16 a[246:247], %[rd1] offset:13312\n"
    "buffer_load_dwordx4 %[w1a], %[srd1], s40 offen lds\n"
    "v_lshlrev_b32 v20, 16, v148\n"
    "v_and_b32 v21, s42, v148\n"
    "v_lshlrev_b32 v22, 16, v149\n"
    "v_and_b32 v23, s42, v149\n"
    "v_lshlrev_b32 v24, 16, v150\n"
    "v_and_b32 v25, s42, v150\n"
    "v_lshlrev_b32 v26, 16, v151\n"
    "v_and_b32 v27, s42, v151\n"
    "v_pk_fma_f32 v[20:21], v[20:21], v[28:29], v[28:29] op_sel:[0,0,1] op_sel_hi:[1,0,1]\n"
    "v_pk_fma_f32 v[22:23], v[22:23], v[28:29], v[28:29] op_sel:[0,0,1] op_sel_hi:[1,0,1]\n"
    "v_mfma_f32_32x32x16_bf16 v[192:207], a[252:255], a[12:15], v[192:207]\n"
    "s_waitcnt lgkmcnt(4)\n"
    "ds_read_b64_tr_b16 a[248:249], %[rd0] offset:14336\n"
    "ds_read_b64_tr_b16 a[250:251], %[rd0] offset:15360\n"
    "v_pk_fma_f32 v[24:25], v[24:25], v[28:29], v[28:29] op_sel:[0,0,1] op_sel_hi:[1,0,1]\n"
    "v_pk_fma_f32 v[26:27], v[26:27], v[28:29], v[28:29] op_sel:[0,0,1] op_sel_hi:[1,0,1]\n"
    "v_cvt_pk_bf16_f32 v148, v20, v21\n"
    "v_cvt_pk_bf16_f32 v149, v22, v23\n"
    "v_cvt_pk_bf16_f32 v150, v24, v25\n"
    "v_cvt_pk_bf16_f32 v151, v26, v27\n"
    "v_accvgpr_write_b32 a28, v148\n"
    "v_accvgpr_write_b32 a29, v149\n"
    "v_accvgpr_write_b32 a30, v150\n"
    "v_accvgpr_write_b32 a31, v151\n"
    "v_mfma_f32_32x32x16_bf16 v[176:191], a[224:227], a[16:19], v[176:191]\n"
    "ds_read_b64_tr_b16 a[252:253], %[rd1] offset:14336\n"
    "ds_read_b64_tr_b16 a[254:255], %[rd1] offset:15360\n"
    "buffer_load_dwordx4 %[w1b], %[srd1], s40 offen offset:1024 lds\n"
    "v_lshlrev_b32 v20, 16, v136\n"
    "v_and_b32 v21, s42, v136\n"
    "v_lshlrev_b32 v22, 16, v137\n"
    "v_and_b32 v23, s42, v137\n"
    "v_lshlrev_b32 v24, 16, v138\n"
    "v_and_b32 v25, s42, v138\n"
    "v_lshlrev_b32 v26, 16, v139\n"
    "v_and_b32 v27, s42, v139\n"
    "v_pk_fma_f32 v[20:21], v[20:21], v[28:29], v[28:29] op_sel:[0,0,1] op_sel_hi:[1,0,1]\n"
    "v_pk_fma_f32 v[22:23], v[22:23], v[28:29], v[28:29] op_sel:[0,0,1] op_sel_hi:[1,0,1]\n"
    "v_mfma_f32_32x32x16_bf16 v[192:207], a[228:231], a[16:19], v[192:207]\n"
    "ds_read_b64_tr_b16 a[224:225], %[rd0] offset:16384\n"
    "ds_read_b64_tr_b16 a[226:227], %[rd0] offset:17408\n"
    "v_pk_fma_f32 v[24:25], v[24:25], v[28:29], v[28:29] op_sel:[0,0,1] op_sel_hi:[1,0,1]\n"
    "v_pk_fma_f32 v[26:27], v[26:27], v[28:29], v[28:29] op_sel:[0,0,1] op_sel_hi:[1,0,1]\n"
    "v_cvt_pk_bf16_f32 v136, v20, v21\n"
    "v_cvt_pk_bf16_f32 v137, v22, v23\n"
    "v_cvt_pk_bf16_f32 v138, v24, v25\n"
    "v_cvt_pk_bf16_f32 v139, v26, v27\n"
    "v_accvgpr_write_b32 a32, v136\n"
    "v_accvgpr_write_b32 a33, v137\n"
    "v_accvgpr_write_b32 a34, v138\n"
    "v_accvgpr_write_b32 a35, v139\n"
    "v_mfma_f32_32x32x16_bf16 v[176:191], a[232:235], a[20:23], v[176:191]\n"
    "ds_read_b64_tr_b16 a[228:229], %[rd1] offset:16384\n"
    "ds_read_b64_tr_b16 a[230:231], %[rd1] offset:17408\n"
    "s_add_u32 m0, %[ldsw], 114688\n"
    "s_mov_b32 s40, 2949248\n"
    "v_lshlrev_b32 v20, 16, v140\n"
    "v_and_b32 v21, s42, v140\n"
    "v_lshlrev_b32 v22, 16, v141\n"
    "v_and_b32 v23, s42, v141\n"
    "v_lshlrev_b32 v24, 16, v142\n"
    "v_and_b32 v25, s42, v142\n"
    "v_lshlrev_b32 v26, 16, v143\n"
    "v_and_b32 v27, s42, v143\n"
    "v_pk_fma_f32 v[20:21], v[20:21], v[28:29], v[28:29] op_sel:[0,0,1] op_sel_hi:[1,0,1]\n"
    "v_pk_fma_f32 v[22:23], v[22:23], v[28:29], v[28:29] op_sel:[0,0,1] op_sel_hi:[1,0,1]\n"
    "v_mfma_f32_32x32x16_bf16 v[192:207], a[236:239], a[20:23], v[192:207]\n"
    "s_waitcnt lgkmcnt(4)\n"
    "ds_read_b64_tr_b16 a[232:233], %[rd0] offset:18432\n"
    "ds_read_b64_tr_b16 a[234:235], %[rd0] offset:19456\n"
    "buffer_load_dwordx4 %[w1a], %[srd1], s40 offen lds\n"
    "v_pk_fma_f32 v[24:25], v[24:25], v[28:29], v[28:29] op_sel:[0,0,1] op_sel_hi:[1,0,1]\n"
    "v_pk_fma_f32 v[26:27], v[26:27], v[28:29], v[28:29] op_sel:[0,0,1] op_sel_hi:[1,0,1]\n"
    "v_cvt_pk_bf16_f32 v140, v20, v21\n"
    "v_cvt_pk_bf16_f32 v141, v22, v23\n"
    "v_cvt_pk_bf16_f32 v142, v24, v25\n"
    "v_cvt_pk_bf16_f32 v143, v26, v27\n"
    "v_accvgpr_write_b32 a36, v140\n"
    "v_accvgpr_write_b32 a37, v141\n"
    "v_accvgpr_write_b32 a38, v142\n"
    "v_accvgpr_write_b32 a39, v143\n"
    "v_mfma_f32_32x32x16_bf16 v[176:191], a[240:243], a[24:27], v[176:191]\n"
    "ds_read_b64_tr_b16 a[236:237], %[rd1] offset:18432\n"
    "ds_read_b64_tr_b16 a[238:239], %[rd1] offset:19456\n"
    "v_lshlrev_b32 v20, 16, v128\n"
    "v_and_b32 v21, s42, v128\n"
    "v_lshlrev_b32 v22, 16, v129\n"
    "v_and_b32 v23, s42, v129\n"
    "v_lshlrev_b32 v24, 16, v130\n"
    "v_and_b32 v25, s42, v130\n"
    "v_lshlrev_b32 v26, 16, v131\n"
    "v_and_b32 v27, s42, v131\n"
    "v_pk_fma_f32 v[20:21], v[20:21], v[28:29], v[28:29] op_sel:[0,0,1] op_sel_hi:[1,0,1]\n"
    "v_pk_fma_f32 v[22:23], v[22:23], v[28:29], v[28:29] op_sel:[0,0,1] op_sel_hi:[1,0,1]\n"
    "v_mfma_f32_32x32x16_bf16 v[192:207], a[244:247], a[24:27], v[192:207]\n"
    "ds_read_b64_tr_b16 a[240:241], %[rd0] offset:20480\n"
    "ds_read_b64_tr_b16 a[242:243], %[rd0] offset:21504\n"
    "buffer_load_dwordx4 %[w1b], %[srd1], s40 offen offset:1024 lds\n"
    "v_pk_fma_f32 v[24:25], v[24:25], v[28:29], v[28:29] op_sel:[0,0,1] op_sel_hi:[1,0,1]\n"
    "v_pk_fma_f32 v[26:27], v[26:27], v[28:29], v[28:29] op_sel:[0,0,1] op_sel_hi:[1,0,1]\n"
    "v_cvt_pk_bf16_f32 v128, v20, v21\n"
    "v_cvt_pk_bf16_f32 v129, v22, v23\n"
    "v_cvt_pk_bf16_f32 v130, v24, v25\n"
    "v_cvt_pk_bf16_f32 v131, v26, v27\n"
    "v_accvgpr_write_b32 a40, v128\n"
    "v_accvgpr_write_b32 a41, v129\n"
    "v_accvgpr_write_b32 a42, v130\n"
    "v_accvgpr_write_b32 a43, v131\n"
    "v_mfma_f32_32x32x16_bf16 v[176:191], a[248:251], a[28:31], v[176:191]\n"
    "ds_read_b64_tr_b16 a[244:245], %[rd1] offset:20480\n"
    "ds_read_b64_tr_b16 a[246:247], %[rd1] offset:21504\n"
    "v_lshlrev_b32 v20, 16, v132\n"
    "v_and_b32 v21, s42, v132\n"
    "v_lshlrev_b32 v22, 16, v133\n"
    "v_and_b32 v23, s42, v133\n"
    "v_lshlrev_b32 v24, 16, v134\n"
    "v_and_b32 v25, s42, v134\n"
    "v_lshlrev_b32 v26, 16, v135\n"
    "v_and_b32 v27, s42, v135\n"
    "v_pk_fma_f32 v[20:21], v[20:21], v[28:29], v[28:29] op_sel:[0,0,1] op_sel_hi:[1,0,1]\n"
    "v_pk_fma_f32 v[22:23], v[22:23], v[28:29], v[28:29] op_sel:[0,0,1] op_sel_hi:[1,0,1]\n"
    "v_mfma_f32_32x32x16_bf16 v[192:207], a[252:255], a[28:31], v[192:207]\n"
    "s_waitcnt lgkmcnt(4)\n"
    "ds_read_b64_tr_b16 a[248:249], %[rd0] offset:22528\n"
    "ds_read_b64_tr_b16 a[250:251], %[rd0] offset:23552\n"
    "v_pk_fma_f32 v[24:25], v[24:25], v[28:29], v[28:29] op_sel:[0,0,1] op_sel_hi:[1,0,1]\n"
    "v_pk_fma_f32 v[26:27], v[26:27], v[28:29], v[28:29] op_sel:[0,0,1] op_sel_hi:[1,0,1]\n"
    "v_cvt_pk_bf16_f32 v132, v20, v21\n"
    "v_cvt_pk_bf16_f32 v133, v22, v23\n"
    "v_cvt_pk_bf16_f32 v134, v24, v25\n"
    "v_cvt_pk_bf16_f32 v135, v26, v27\n"
    "v_accvgpr_write_b32 a44, v132\n"
    "v_accvgpr_write_b32 a45, v133\n"
    "v_accvgpr_write_b32 a46, v134\n"
    "v_accvgpr_write_b32 a47, v135\n"
    "v_mfma_f32_32x32x16_bf16 v[176:191], a[224:227], a[32:35], v[176:191]\n"
    "ds_read_b64_tr_b16 a[252:253], %[rd1] offset:22528\n"
    "ds_read_b64_tr_b16 a[254:255], %[rd1] offset:23552\n"
    "v_lshlrev_b32 v20, 16, v120\n"
    "v_and_b32 v21, s42, v120\n"
    "v_lshlrev_b32 v22, 16, v121\n"
    "v_and_b32 v23, s42, v121\n"
    "v_lshlrev_b32 v24, 16, v122\n"
    "v_and_b32 v25, s42, v122\n"
    "v_lshlrev_b32 v26, 16, v123\n"
    "v_and_b32 v27, s42, v123\n"
    "v_pk_fma_f32 v[20:21], v[20:21], v[28:29], v[28:29] op_sel:[0,0,1] op_sel_hi:[1,0,1]\n"
    "v_pk_fma_f32 v[22:23], v[22:23], v[28:29], v[28:29] op_sel:[0,0,1] op_sel_hi:[1,0,1]\n"
    "v_mfma_f32_32x32x16_bf16 v[192:207], a[228:231], a[32:35], v[192:207]\n"
    "s_waitcnt vmcnt(18)\n"
    "s_barrier\n"
    "ds_read_b64_tr_b16 a[224:225], %[rd0] offset:24576\n"
    "ds_read_b64_tr_b16 a[226:227], %[rd0] offset:25600\n"
    "v_pk_fma_f32 v[24:25], v[24:25], v[28:29], v[28:29] op_sel:[0,0,1] op_sel_hi:[1,0,1]\n"
    "v_pk_fma_f32 v[26:27], v[26:27], v[28:29], v[28:29] op_sel:[0,0,1] op_sel_hi:[1,0,1]\n"
    "v_cvt_pk_bf16_f32 v120, v20, v21\n"
    "v_cvt_pk_bf16_f32 v121, v22, v23\n"
    "v_cvt_pk_bf16_f32 v122, v24, v25\n"
    "v_cvt_pk_bf16_f32 v123, v26, v27\n"
    "v_accvgpr_write_b32 a48, v120\n"
    "v_accvgpr_write_b32 a49, v121\n"
    "v_accvgpr_write_b32 a50, v122\n"
    "v_accvgpr_write_b32 a51, v123\n"
    "v_mfma_f32_32x32x16_bf16 v[176:191], a[232:235], a[36:39], v[176:191]\n"
    "ds_read_b64_tr_b16 a[228:229], %[rd1] offset:24576\n"
    "ds_read_b64_tr_b16 a[230:231], %[rd1] offset:25600\n"
    "s_add_u32 m0, %[ldsw], 122880\n"
    "s_mov_b32 s40, 3539072\n"
    "v_lshlrev_b32 v20, 16, v124\n"
    "v_and_b32 v21, s42, v124\n"
    "v_lshlrev_b32 v22, 16, v125\n"
    "v_and_b32 v23, s42, v125\n"
    "v_lshlrev_b32 v24, 16, v126\n"
    "v_and_b32 v25, s42, v126\n"
    "v_lshlrev_b32 v26, 16, v127\n"
    "v_and_b32 v27, s42, v127\n"
    "v_pk_fma_f32 v[20:21], v[20:21], v[28:29], v[28:29] op_sel:[0,0,1] op_sel_hi:[1,0,1]\n"
    "v_pk_fma_f32 v[22:23], v[22:23], v[28:29], v[28:29] op_sel:[0,0,1] op_sel_hi:[1,0,1]\n"
    "v_mfma_f32_32x32x16_bf16 v[192:207], a[236:239], a[36:39], v[192:207]\n"
    "s_waitcnt lgkmcnt(4)\n"
    "ds_read_b64_tr_b16 a[232:233], %[rd0] offset:26624\n"
    "ds_read_b64_tr_b16 a[234:235], %[rd0] offset:27648\n"
    "buffer_load_dwordx4 %[w1a], %[srd1], s40 offen lds\n"
    "v_pk_fma_f32 v[24:25], v[24:25], v[28:29], v[28:29] op_sel:[0,0,1] op_sel_hi:[1,0,1]\n"
    "v_pk_fma_f32 v[26:27], v[26:27], v[28:29], v[28:29] op_sel:[0,0,1] op_sel_hi:[1,0,1]\n"
    "v_cvt_pk_bf16_f32 v124, v20, v21\n"
    "v_cvt_pk_bf16_f32 v125, v22, v23\n"
    "v_cvt_pk_bf16_f32 v126, v24, v25\n"
    "v_cvt_pk_bf16_f32 v127, v26, v27\n"
    "v_accvgpr_write_b32 a52, v124\n"
    "v_accvgpr_write_b32 a53, v125\n"
    "v_accvgpr_write_b32 a54, v126\n"
    "v_accvgpr_write_b32 a55, v127\n"
    "v_mfma_f32_32x32x16_bf16 v[176:191], a[240:243], a[40:43], v[176:191]\n"
    "ds_read_b64_tr_b16 a[236:237], %[rd1] offset:26624\n"
    "ds_read_b64_tr_b16 a[238:239], %[rd1] offset:27648\n"
    "v_lshlrev_b32 v20, 16, v112\n"
    "v_and_b32 v21, s42, v112\n"
    "v_lshlrev_b32 v22, 16, v113\n"
    "v_and_b32 v23, s42, v113\n"
    "v_lshlrev_b32 v24, 16, v114\n"
    "v_and_b32 v25, s42, v114\n"
    "v_lshlrev_b32 v26, 16, v115\n"
    "v_and_b32 v27, s42, v115\n"
    "v_pk_fma_f32 v[20:21], v[20:21], v[28:29], v[28:29] op_sel:[0,0,1] op_sel_hi:[1,0,1]\n"
    "v_pk_fma_f32 v[22:23], v[22:23], v[28:29], v[28:29] op_sel:[0,0,1] op_sel_hi:[1,0,1]\n"
    "v_mfma_f32_32x32x16_bf16 v[192:207], a[244:247], a[40:43], v[192:207]\n"
    "ds_read_b64_tr_b16 a[240:241], %[rd0] offset:28672\n"
    "ds_read_b64_tr_b16 a[242:243], %[rd0] offset:29696\n"
    "v_pk_fma_f32 v[24:25], v[24:25], v[28:29], v[28:29] op_sel:[0,0,1] op_sel_hi:[1,0,1]\n"
    "v_pk_fma_f32 v[26:27], v[26:27], v[28:29], v[28:29] op_sel:[0,0,1] op_sel_hi:[1,0,1]\n"
    "v_cvt_pk_bf16_f32 v112, v20, v21\n"
    "v_cvt_pk_bf16_f32 v113, v22, v23\n"
    "v_cvt_pk_bf16_f32 v114, v24, v25\n"
    "v_cvt_pk_bf16_f32 v115, v26, v27\n"
    "v_accvgpr_write_b32 a56, v112\n"
    "v_accvgpr_write_b32 a57, v113\n"
    "v_accvgpr_write_b32 a58, v114\n"
    "v_accvgpr_write_b32 a59, v115\n"
    "v_mfma_f32_32x32x16_bf16 v[176:191], a[248:251], a[44:47], v[176:191]\n"
    "ds_read_b64_tr_b16 a[244:245], %[rd1] offset:28672\n"
    "ds_read_b64_tr_b16 a[246:247], %[rd1] offset:29696\n"
    "v_lshlrev_b32 v20, 16, v116\n"
    "v_and_b32 v21, s42, v116\n"
    "v_lshlrev_b32 v22, 16, v117\n"
    "v_and_b32 v23, s42, v117\n"
    "v_lshlrev_b32 v24, 16, v118\n"
    "v_and_b32 v25, s42, v118\n"
    "v_lshlrev_b32 v26, 16, v119\n"
    "v_and_b32 v27, s42, v119\n"
    "v_pk_fma_f32 v[20:21], v[20:21], v[28:29], v[28:29] op_sel:[0,0,1] op_sel_hi:[1,0,1]\n"
    "v_pk_fma_f32 v[22:23], v[22:23], v[28:29], v[28:29] op_sel:[0,0,1] op_sel_hi:[1,0,1]\n"
    "v_mfma_f32_32x32x16_bf16 v[192:207], a[252:255], a[44:47], v[192:207]\n"
    "s_waitcnt lgkmcnt(4)\n"
    "ds_read_b64_tr_b16 a[248:249], %[rd0] offset:30720\n"
    "ds_read_b64_tr_b16 a[250:251], %[rd0] offset:31744\n"
    "buffer_load_dwordx4 %[w1b], %[srd1], s40 offen offset:1024 lds\n"
    "v_pk_fma_f32 v[24:25], v[24:25], v[28:29], v[28:29] op_sel:[0,0,1] op_sel_hi:[1,0,1]\n"
    "v_pk_fma_f32 v[26:27], v[26:27], v[28:29], v[28:29] op_sel:[0,0,1] op_sel_hi:[1,0,1]\n"
    "v_cvt_pk_bf16_f32 v116, v20, v21\n"
    "v_cvt_pk_bf16_f32 v117, v22, v23\n"
    "v_cvt_pk_bf16_f32 v118, v24, v25\n"
    "v_cvt_pk_bf16_f32 v119, v26, v27\n"
    "v_accvgpr_write_b32 a60, v116\n"
    "v_accvgpr_write_b32 a61, v117\n"
    "v_accvgpr_write_b32 a62, v118\n"
    "v_accvgpr_write_b32 a63, v119\n"
    "v_mfma_f32_32x32x16_bf16 v[176:191], a[224:227], a[48:51], v[176:191]\n"
    "ds_read_b64_tr_b16 a[252:253], %[rd1] offset:30720\n"
    "ds_read_b64_tr_b16 a[254:255], %[rd1] offset:31744\n"
    "v_lshlrev_b32 v20, 16, v104\n"
    "v_and_b32 v21, s42, v104\n"
    "v_lshlrev_b32 v22, 16, v105\n"
    "v_and_b32 v23, s42, v105\n"
    "v_lshlrev_b32 v24, 16, v106\n"
    "v_and_b32 v25, s42, v106\n"
    "v_lshlrev_b32 v26, 16, v107\n"
    "v_and_b32 v27, s42, v107\n"
    "v_pk_fma_f32 v[20:21], v[20:21], v[28:29], v[28:29] op_sel:[0,0,1] op_sel_hi:[1,0,1]\n"
    "v_pk_fma_f32 v[22:23], v[22:23], v[28:29], v[28:29] op_sel:[0,0,1] op_sel_hi:[1,0,1]\n"
    "ds_read_b128 v[144:147], %[vb] offset:256\n"
    "v_mfma_f32_32x32x16_bf16 v[192:207], a[228:231], a[48:51], v[192:207]\n"
    "ds_read_b64_tr_b16 a[224:225], %[rd0] offset:32768\n"
    "ds_read_b64_tr_b16 a[226:227], %[rd0] offset:33792\n"
    "v_pk_fma_f32 v[24:25], v[24:25], v[28:29], v[28:29] op_sel:[0,0,1] op_sel_hi:[1,0,1]\n"
    "v_pk_fma_f32 v[26:27], v[26:27], v[28:29], v[28:29] op_sel:[0,0,1] op_sel_hi:[1,0,1]\n"
    "v_cvt_pk_bf16_f32 v104, v20, v21\n"
    "v_cvt_pk_bf16_f32 v105, v22, v23\n"
    "v_cvt_pk_bf16_f32 v106, v24, v25\n"
    "v_cvt_pk_bf16_f32 v107, v26, v27\n"
    "v_accvgpr_write_b32 a64, v104\n"
    "v_accvgpr_write_b32 a65, v105\n"
    "v_accvgpr_write_b32 a66, v106\n"
    "v_accvgpr_write_b32 a67, v107\n"
    "v_mfma_f32_32x32x16_bf16 v[176:191], a[232:235], a[52:55], v[176:191]\n"
    "ds_read_b64_tr_b16 a[228:229], %[rd1] offset:32768\n"
    "ds_read_b64_tr_b16 a[230:231], %[rd1] offset:33792\n"
    "s_add_u32 m0, %[ldsw], 131072\n"
    "s_mov_b32 s40, 4128896\n"
    "v_lshlrev_b32 v20, 16, v108\n"
    "v_and_b32 v21, s42, v108\n"
    "v_lshlrev_b32 v22, 16, v109\n"
    "v_and_b32 v23, s42, v109\n"
    "v_lshlrev_b32 v24, 16, v110\n"
    "v_and_b32 v25, s42, v110\n"
    "v_lshlrev_b32 v26, 16, v111\n"
    "v_and_b32 v27, s42, v111\n"
    "v_pk_fma_f32 v[20:21], v[20:21], v[28:29], v[28:29] op_sel:[0,0,1] op_sel_hi:[1,0,1]\n"
    "v_pk_fma_f32 v[22:23], v[22:23], v[28:29], v[28:29] op_sel:[0,0,1] op_sel_hi:[1,0,1]\n"
    "v_mfma_f32_32x32x16_bf16 v[192:207], a[236:239], a[52:55], v[192:207]\n"
    "s_waitcnt lgkmcnt(5)\n"
    "ds_read_b64_tr_b16 a[232:233], %[rd0] offset:34816\n"
    "ds_read_b64_tr_b16 a[234:235], %[rd0] offset:35840\n"
    "buffer_load_dwordx4 %[w1a], %[srd1], s40 offen lds\n"
    "v_pk_fma_f32 v[24:25], v[24:25], v[28:29], v[28:29] op_sel:[0,0,1] op_sel_hi:[1,0,1]\n"
    "v_pk_fma_f32 v[26:27], v[26:27], v[28:29], v[28:29] op_sel:[0,0,1] op_sel_hi:[1,0,1]\n"
    "v_cvt_pk_bf16_f32 v108, v20, v21\n"
    "v_cvt_pk_bf16_f32 v109, v22, v23\n"
    "v_cvt_pk_bf16_f32 v110, v24, v25\n"
    "v_cvt_pk_bf16_f32 v111, v26, v27\n"
    "v_accvgpr_write_b32 a68, v108\n"
    "v_accvgpr_write_b32 a69, v109\n"
    "v_accvgpr_write_b32 a70, v110\n"
    "v_accvgpr_write_b32 a71, v111\n"
    "v_mfma_f32_32x32x16_bf16 v[176:191], a[240:243], a[56:59], v[176:191]\n"
    "ds_read_b64_tr_b16 a[236:237], %[rd1] offset:34816\n"
    "ds_read_b64_tr_b16 a[238:239], %[rd1] offset:35840\n"
    "v_lshlrev_b32 v20, 16, v96\n"
    "v_and_b32 v21, s42, v96\n"
    "v_lshlrev_b32 v22, 16, v97\n"
    "v_and_b32 v23, s42, v97\n"
    "v_lshlrev_b32 v24, 16, v98\n"
    "v_and_b32 v25, s42, v98\n"
    "v_lshlrev_b32 v26, 16, v99\n"
    "v_and_b32 v27, s42, v99\n"
    "v_pk_fma_f32 v[20:21], v[20:21], v[28:29], v[28:29] op_sel:[0,0,1] op_sel_hi:[1,0,1]\n"
    "v_pk_fma_f32 v[22:23], v[22:23], v[28:29], v[28:29] op_sel:[0,0,1] op_sel_hi:[1,0,1]\n"
    "ds_read_b128 v[148:151], %[vb] offset:288\n"
    "v_mfma_f32_32x32x16_bf16 v[192:207], a[244:247], a[56:59], v[192:207]\n"
    "ds_read_b64_tr_b16 a[240:241], %[rd0] offset:36864\n"
    "ds_read_b64_tr_b16 a[242:243], %[rd0] offset:37888\n"
    "v_pk_fma_f32 v[24:25], v[24:25], v[28:29], v[28:29] op_sel:[0,0,1] op_sel_hi:[1,0,1]\n"
    "v_pk_fma_f32 v[26:27], v[26:27], v[28:29], v[28:29] op_sel:[0,0,1] op_sel_hi:[1,0,1]\n"
    "v_cvt_pk_bf16_f32 v96, v20, v21\n"
    "v_cvt_pk_bf16_f32 v97, v22, v23\n"
    "v_cvt_pk_bf16_f32 v98, v24, v25\n"
    "v_cvt_pk_bf16_f32 v99, v26, v27\n"
    "v_accvgpr_write_b32 a72, v96\n"
    "v_accvgpr_write_b32 a73, v97\n"
    "v_accvgpr_write_b32 a74, v98\n"
    "v_accvgpr_write_b32 a75, v99\n"
    "v_mfma_f32_32x32x16_bf16 v[176:191], a[248:251], a[60:63], v[176:191]\n"
    "ds_read_b64_tr_b16 a[244:245], %[rd1] offset:36864\n"
    "ds_read_b64_tr_b16 a[246:247], %[rd1] offset:37888\n"
    "v_lshlrev_b32 v20, 16, v100\n"
    "v_and_b32 v21, s42, v100\n"
    "v_lshlrev_b32 v22, 16, v101\n"
    "v_and_b32 v23, s42, v101\n"
    "v_lshlrev_b32 v24, 16, v102\n"
    "v_and_b32 v25, s42, v102\n"
    "v_lshlrev_b32 v26, 16, v103\n"
    "v_and_b32 v27, s42, v103\n"
    "v_pk_fma_f32 v[20:21], v[20:21], v[28:29], v[28:29] op_sel:[0,0,1] op_sel_hi:[1,0,1]\n"
    "v_pk_fma_f32 v[22:23], v[22:23], v[28:29], v[28:29] op_sel:[0,0,1] op_sel_hi:[1,0,1]\n"
    "v_mfma_f32_32x32x16_bf16 v[192:207], a[252:255], a[60:63], v[192:207]\n"
    "s_waitcnt lgkmcnt(5)\n"
    "ds_read_b64_tr_b16 a[248:249], %[rd0] offset:38912\n"
    "ds_read_b64_tr_b16 a[250:251], %[rd0] offset:39936\n"
    "buffer_load_dwordx4 %[w1b], %[srd1], s40 offen offset:1024 lds\n"
    "v_pk_fma_f32 v[24:25], v[24:25], v[28:29], v[28:29] op_sel:[0,0,1] op_sel_hi:[1,0,1]\n"
    "v_pk_fma_f32 v[26:27], v[26:27], v[28:29], v[28:29] op_sel:[0,0,1] op_sel_hi:[1,0,1]\n"
    "v_cvt_pk_bf16_f32 v100, v20, v21\n"
    "v_cvt_pk_bf16_f32 v101, v22, v23\n"
    "v_cvt_pk_bf16_f32 v102, v24, v25\n"
    "v_cvt_pk_bf16_f32 v103, v26, v27\n"
    "v_accvgpr_write_b32 a76, v100\n"
    "v_accvgpr_write_b32 a77, v101\n"
    "v_accvgpr_write_b32 a78, v102\n"
    "v_accvgpr_write_b32 a79, v103\n"
    "v_mfma_f32_32x32x16_bf16 v[176:191], a[224:227], a[64:67], v[176:191]\n"
    "ds_read_b64_tr_b16 a[252:253], %[rd1] offset:38912\n"
    "ds_read_b64_tr_b16 a[254:255], %[rd1] offset:39936\n"
    "v_lshlrev_b32 v20, 16, v88\n"
    "v_and_b32 v21, s42, v88\n"
    "v_lshlrev_b32 v22, 16, v89\n"
    "v_and_b32 v23, s42, v89\n"
    "v_lshlrev_b32 v24, 16, v90\n"
    "v_and_b32 v25, s42, v90\n"
    "v_lshlrev_b32 v26, 16, v91\n"
    "v_and_b32 v27, s42, v91\n"
    "v_pk_fma_f32 v[20:21], v[20:21], v[28:29], v[28:29] op_sel:[0,0,1] op_sel_hi:[1,0,1]\n"
    "v_pk_fma_f32 v[22:23], v[22:23], v[28:29], v[28:29] op_sel:[0,0,1] op_sel_hi:[1,0,1]\n"
    "v_mfma_f32_32x32x16_bf16 v[192:207], a[228:231], a[64:67], v[192:207]\n"
    "ds_read_b64_tr_b16 a[224:225], %[rd0] offset:40960\n"
    "ds_read_b64_tr_b16 a[226:227], %[rd0] offset:41984\n"
    "v_pk_fma_f32 v[24:25], v[24:25], v[28:29], v[28:29] op_sel:[0,0,1] op_sel_hi:[1,0,1]\n"
    "v_pk_fma_f32 v[26:27], v[26:27], v[28:29], v[28:29] op_sel:[0,0,1] op_sel_hi:[1,0,1]\n"
    "v_cvt_pk_bf16_f32 v88, v20, v21\n"
    "v_cvt_pk_bf16_f32 v89, v22, v23\n"
    "v_cvt_pk_bf16_f32 v90, v24, v25\n"
    "v_cvt_pk_bf16_f32 v91, v26, v27\n"
    "v_accvgpr_write_b32 a80, v88\n"
    "v_accvgpr_write_b32 a81, v89\n"
    "v_accvgpr_write_b32 a82, v90\n"
    "v_accvgpr_write_b32 a83, v91\n"
    "ds_read_b128 v[152:155], %[vb] offset:320\n"
    "v_mfma_f32_32x32x16_bf16 v[176:191], a[232:235], a[68:71], v[176:191]\n"
    "ds_read_b64_tr_b16 a[228:229], %[rd1] offset:40960\n"
    "ds_read_b64_tr_b16 a[230:231], %[rd1] offset:41984\n"
    "s_add_u32 m0, %[ldsw], 139264\n"
    "s_mov_b32 s40, 4718720\n"
    "v_lshlrev_b32 v20, 16, v92\n"
    "v_and_b32 v21, s42, v92\n"
    "v_lshlrev_b32 v22, 16, v93\n"
    "v_and_b32 v23, s42, v93\n"
    "v_lshlrev_b32 v24, 16, v94\n"
    "v_and_b32 v25, s42, v94\n"
    "v_lshlrev_b32 v26, 16, v95\n"
    "v_and_b32 v27, s42, v95\n"
    "v_pk_fma_f32 v[20:21], v[20:21], v[28:29], v[28:29] op_sel:[0,0,1] op_sel_hi:[1,0,1]\n"
    "v_pk_fma_f32 v[22:23], v[22:23], v[28:29], v[28:29] op_sel:[0,0,1] op_sel_hi:[1,0,1]\n"
    "v_mfma_f32_32x32x16_bf16 v[192:207], a[236:239], a[68:71], v[192:207]\n"
    "s_waitcnt lgkmcnt(5)\n"
    "ds_read_b64_tr_b16 a[232:233], %[rd0] offset:43008\n"
    "ds_read_b64_tr_b16 a[234:235], %[rd0] offset:44032\n"
    "buffer_load_dwordx4 %[w1a], %[srd1], s40 offen lds\n"
    "v_pk_fma_f32 v[24:25], v[24:25], v[28:29], v[28:29] op_sel:[0,0,1] op_sel_hi:[1,0,1]\n"
    "v_pk_fma_f32 v[26:27], v[26:27], v[28:29], v[28:29] op_sel:[0,0,1] op_sel_hi:[1,0,1]\n"
    "v_cvt_pk_bf16_f32 v92, v20, v21\n"
    "v_cvt_pk_bf16_f32 v93, v22, v23\n"
    "v_cvt_pk_bf16_f32 v94, v24, v25\n"
    "v_cvt_pk_bf16_f32 v95, v26, v27\n"
    "v_accvgpr_write_b32 a84, v92\n"
    "v_accvgpr_write_b32 a85, v93\n"
    "v_accvgpr_write_b32 a86, v94\n"
    "v_accvgpr_write_b32 a87, v95\n"
    "v_mfma_f32_32x32x16_bf16 v[176:191], a[240:243], a[72:75], v[176:191]\n"
    "ds_read_b64_tr_b16 a[236:237], %[rd1] offset:43008\n"
    "ds_read_b64_tr_b16 a[238:239], %[rd1] offset:44032\n"
    "v_lshlrev_b32 v20, 16, v80\n"
    "v_and_b32 v21, s42, v80\n"
    "v_lshlrev_b32 v22, 16, v81\n"
    "v_and_b32 v23, s42, v81\n"
    "v_lshlrev_b32 v24, 16, v82\n"
    "v_and_b32 v25, s42, v82\n"
    "v_lshlrev_b32 v26, 16, v83\n"
    "v_and_b32 v27, s42, v83\n"
    "v_pk_fma_f32 v[20:21], v[20:21], v[28:29], v[28:29] op_sel:[0,0,1] op_sel_hi:[1,0,1]\n"
    "v_pk_fma_f32 v[22:23], v[22:23], v[28:29], v[28:29] op_sel:[0,0,1] op_sel_hi:[1,0,1]\n"
    "v_mfma_f32_32x32x16_bf16 v[192:207], a[244:247], a[72:75], v[192:207]\n"
    "ds_read_b64_tr_b16 a[240:241], %[rd0] offset:45056\n"
    "ds_read_b64_tr_b16 a[242:243], %[rd0] offset:46080\n"
    "v_pk_fma_f32 v[24:25], v[24:25], v[28:29], v[28:29] op_sel:[0,0,1] op_sel_hi:[1,0,1]\n"
    "v_pk_fma_f32 v[26:27], v[26:27], v[28:29], v[28:29] op_sel:[0,0,1] op_sel_hi:[1,0,1]\n"
    "v_cvt_pk_bf16_f32 v80, v20, v21\n"
    "v_cvt_pk_bf16_f32 v81, v22, v23\n"
    "v_cvt_pk_bf16_f32 v82, v24, v25\n"
    "v_cvt_pk_bf16_f32 v83, v26, v27\n"
    "v_accvgpr_write_b32 a88, v80\n"
    "v_accvgpr_write_b32 a89, v81\n"
    "v_accvgpr_write_b32 a90, v82\n"
    "v_accvgpr_write_b32 a91, v83\n"
    "v_mfma_f32_32x32x16_bf16 v[176:191], a[248:251], a[76:79], v[176:191]\n"
    "ds_read_b64_tr_b16 a[244:245], %[rd1] offset:45056\n"
    "ds_read_b64_tr_b16 a[246:247], %[rd1] offset:46080\n"
    "v_lshlrev_b32 v20, 16, v84\n"
    "v_and_b32 v21, s42, v84\n"
    "v_lshlrev_b32 v22, 16, v85\n"
    "v_and_b32 v23, s42, v85\n"
    "v_lshlrev_b32 v24, 16, v86\n"
    "v_and_b32 v25, s42, v86\n"
    "v_lshlrev_b32 v26, 16, v87\n"
    "v_and_b32 v27, s42, v87\n"
    "v_pk_fma_f32 v[20:21], v[20:21], v[28:29], v[28:29] op_sel:[0,0,1] op_sel_hi:[1,0,1]\n"
    "v_pk_fma_f32 v[22:23], v[22:23], v[28:29], v[28:29] op_sel:[0,0,1] op_sel_hi:[1,0,1]\n"
    "ds_read_b128 v[156:159], %[vb] offset:352\n"
    "v_mfma_f32_32x32x16_bf16 v[192:207], a[252:255], a[76:79], v[192:207]\n"
    "s_waitcnt lgkmcnt(5)\n"
    "ds_read_b64_tr_b16 a[248:249], %[rd0] offset:47104\n"
    "ds_read_b64_tr_b16 a[250:251], %[rd0] offset:48128\n"
    "buffer_load_dwordx4 %[w1b], %[srd1], s40 offen offset:1024 lds\n"
    "v_pk_fma_f32 v[24:25], v[24:25], v[28:29], v[28:29] op_sel:[0,0,1] op_sel_hi:[1,0,1]\n"
    "v_pk_fma_f32 v[26:27], v[26:27], v[28:29], v[28:29] op_sel:[0,0,1] op_sel_hi:[1,0,1]\n"
    "v_cvt_pk_bf16_f32 v84, v20, v21\n"
    "v_cvt_pk_bf16_f32 v85, v22, v23\n"
    "v_cvt_pk_bf16_f32 v86, v24, v25\n"
    "v_cvt_pk_bf16_f32 v87, v26, v27\n"
    "v_accvgpr_write_b32 a92, v84\n"
    "v_accvgpr_write_b32 a93, v85\n"
    "v_accvgpr_write_b32 a94, v86\n"
    "v_accvgpr_write_b32 a95, v87\n"
    "v_mfma_f32_32x32x16_bf16 v[176:191], a[224:227], a[80:83], v[176:191]\n"
    "ds_read_b64_tr_b16 a[252:253], %[rd1] offset:47104\n"
    "ds_read_b64_tr_b16 a[254:255], %[rd1] offset:48128\n"
    "v_lshlrev_b32 v20, 16, v72\n"
    "v_and_b32 v21, s42, v72\n"
    "v_lshlrev_b32 v22, 16, v73\n"
    "v_and_b32 v23, s42, v73\n"
    "v_lshlrev_b32 v24, 16, v74\n"
    "v_and_b32 v25, s42, v74\n"
    "v_lshlrev_b32 v26, 16, v75\n"
    "v_and_b32 v27, s42, v75\n"
    "v_pk_fma_f32 v[20:21], v[20:21], v[28:29], v[28:29] op_sel:[0,0,1] op_sel_hi:[1,0,1]\n"
    "v_pk_fma_f32 v[22:23], v[22:23], v[28:29], v[28:29] op_sel:[0,0,1] op_sel_hi:[1,0,1]\n"
    "v_mfma_f32_32x32x16_bf16 v[192:207], a[228:231], a[80:83], v[192:207]\n"
    "s_waitcnt vmcnt(18)\n"
    "s_barrier\n"
    "ds_read_b64_tr_b16 a[224:225], v16 offset:0\n"
    "ds_read_b64_tr_b16 a[226:227], v16 offset:1024\n"
    "v_pk_fma_f32 v[24:25], v[24:25], v[28:29], v[28:29] op_sel:[0,0,1] op_sel_hi:[1,0,1]\n"
    "v_pk_fma_f32 v[26:27], v[26:27], v[28:29], v[28:29] op_sel:[0,0,1] op_sel_hi:[1,0,1]\n"
    "v_cvt_pk_bf16_f32 v72, v20, v21\n"
    "v_cvt_pk_bf16_f32 v73, v22, v23\n"
    "v_cvt_pk_bf16_f32 v74, v24, v25\n"
    "v_cvt_pk_bf16_f32 v75, v26, v27\n"
    "v_accvgpr_write_b32 a96, v72\n"
    "v_accvgpr_write_b32 a97, v73\n"
    "v_accvgpr_write_b32 a98, v74\n"
    "v_accvgpr_write_b32 a99, v75\n"
    "v_mfma_f32_32x32x16_bf16 v[176:191], a[232:235], a[84:87], v[176:191]\n"
    "ds_read_b64_tr_b16 a[228:229], v17 offset:0\n"
    "ds_read_b64_tr_b16 a[230:231], v17 offset:1024\n"
    "s_add_u32 m0, %[ldsw], 0\n"
    "s_mov_b32 s40, 0\n"
    "v_lshlrev_b32 v20, 16, v76\n"
    "v_and_b32 v21, s42, v76\n"
    "v_lshlrev_b32 v22, 16, v77\n"
    "v_and_b32 v23, s42, v77\n"
    "v_lshlrev_b32 v24, 16, v78\n"
    "v_and_b32 v25, s42, v78\n"
    "v_lshlrev_b32 v26, 16, v79\n"
    "v_and_b32 v27, s42, v79\n"
    "v_pk_fma_f32 v[20:21], v[20:21], v[28:29], v[28:29] op_sel:[0,0,1] op_sel_hi:[1,0,1]\n"
    "v_pk_fma_f32 v[22:23], v[22:23], v[28:29], v[28:29] op_sel:[0,0,1] op_sel_hi:[1,0,1]\n"
    "v_mfma_f32_32x32x16_bf16 v[192:207], a[236:239], a[84:87], v[192:207]\n"
    "s_waitcnt lgkmcnt(4)\n"
    "ds_read_b64_tr_b16 a[232:233], v16 offset:2048\n"
    "ds_read_b64_tr_b16 a[234:235], v16 offset:3072\n"
    "buffer_load_dwordx4 %[w2a], %[srd2], s40 offen lds\n"
    "v_pk_fma_f32 v[24:25], v[24:25], v[28:29], v[28:29] op_sel:[0,0,1] op_sel_hi:[1,0,1]\n"
    "v_pk_fma_f32 v[26:27], v[26:27], v[28:29], v[28:29] op_sel:[0,0,1] op_sel_hi:[1,0,1]\n"
    "v_cvt_pk_bf16_f32 v76, v20, v21\n"
    "v_cvt_pk_bf16_f32 v77, v22, v23\n"
    "v_cvt_pk_bf16_f32 v78, v24, v25\n"
    "v_cvt_pk_bf16_f32 v79, v26, v27\n"
    "v_accvgpr_write_b32 a100, v76\n"
    "v_accvgpr_write_b32 a101, v77\n"
    "v_accvgpr_write_b32 a102, v78\n"
    "v_accvgpr_write_b32 a103, v79\n"
    "v_mfma_f32_32x32x16_bf16 v[176:191], a[240:243], a[88:91], v[176:191]\n"
    "ds_read_b64_tr_b16 a[236:237], v17 offset:2048\n"
    "ds_read_b64_tr_b16 a[238:239], v17 offset:3072\n"
    "v_lshlrev_b32 v20, 16, v64\n"
    "v_and_b32 v21, s42, v64\n"
    "v_lshlrev_b32 v22, 16, v65\n"
    "v_and_b32 v23, s42, v65\n"
    "v_lshlrev_b32 v24, 16, v66\n"
    "v_and_b32 v25, s42, v66\n"
    "v_lshlrev_b32 v26, 16, v67\n"
    "v_and_b32 v27, s42, v67\n"
    "v_pk_fma_f32 v[20:21], v[20:21], v[28:29], v[28:29] op_sel:[0,0,1] op_sel_hi:[1,0,1]\n"
    "v_pk_fma_f32 v[22:23], v[22:23], v[28:29], v[28:29] op_sel:[0,0,1] op_sel_hi:[1,0,1]\n"
    "ds_read_b128 v[160:163], %[vb] offset:384\n"
    "v_mfma_f32_32x32x16_bf16 v[192:207], a[244:247], a[88:91], v[192:207]\n"
    "ds_read_b64_tr_b16 a[240:241], v16 offset:4096\n"
    "ds_read_b64_tr_b16 a[242:243], v16 offset:5120\n"
    "v_pk_fma_f32 v[24:25], v[24:25], v[28:29], v[28:29] op_sel:[0,0,1] op_sel_hi:[1,0,1]\n"
    "v_pk_fma_f32 v[26:27], v[26:27], v[28:29], v[28:29] op_sel:[0,0,1] op_sel_hi:[1,0,1]\n"
    "v_cvt_pk_bf16_f32 v64, v20, v21\n"
    "v_cvt_pk_bf16_f32 v65, v22, v23\n"
    "v_cvt_pk_bf16_f32 v66, v24, v25\n"
    "v_cvt_pk_bf16_f32 v67, v26, v27\n"
    "v_accvgpr_write_b32 a104, v64\n"
    "v_accvgpr_write_b32 a105, v65\n"
    "v_accvgpr_write_b32 a106, v66\n"
    "v_accvgpr_write_b32 a107, v67\n"
    "v_mfma_f32_32x32x16_bf16 v[176:191], a[248:251], a[92:95], v[176:191]\n"
    "ds_read_b64_tr_b16 a[244:245], v17 offset:4096\n"
    "ds_read_b64_tr_b16 a[246:247], v17 offset:5120\n"
    "v_lshlrev_b32 v20, 16, v68\n"
    "v_and_b32 v21, s42, v68\n"
    "v_lshlrev_b32 v22, 16, v69\n"
    "v_and_b32 v23, s42, v69\n"
    "v_lshlrev_b32 v24, 16, v70\n"
    "v_and_b32 v25, s42, v70\n"
    "v_lshlrev_b32 v26, 16, v71\n"
    "v_and_b32 v27, s42, v71\n"
    "v_pk_fma_f32 v[20:21], v[20:21], v[28:29], v[28:29] op_sel:[0,0,1] op_sel_hi:[1,0,1]\n"
    "v_pk_fma_f32 v[22:23], v[22:23], v[28:29], v[28:29] op_sel:[0,0,1] op_sel_hi:[1,0,1]\n"
    "v_mfma_f32_32x32x16_bf16 v[192:207], a[252:255], a[92:95], v[192:207]\n"
    "s_waitcnt lgkmcnt(5)\n"
    "ds_read_b64_tr_b16 a[248:249], v16 offset:6144\n"
    "ds_read_b64_tr_b16 a[250:251], v16 offset:7168\n"
    "buffer_load_dwordx4 %[w2b], %[srd2], s40 offen offset:1024 lds\n"
    "v_pk_fma_f32 v[24:25], v[24:25], v[28:29], v[28:29] op_sel:[0,0,1] op_sel_hi:[1,0,1]\n"
    "v_pk_fma_f32 v[26:27], v[26:27], v[28:29], v[28:29] op_sel:[0,0,1] op_sel_hi:[1,0,1]\n"
    "v_cvt_pk_bf16_f32 v68, v20, v21\n"
    "v_cvt_pk_bf16_f32 v69, v22, v23\n"
    "v_cvt_pk_bf16_f32 v70, v24, v25\n"
    "v_cvt_pk_bf16_f32 v71, v26, v27\n"
    "v_accvgpr_write_b32 a108, v68\n"
    "v_accvgpr_write_b32 a109, v69\n"
    "v_accvgpr_write_b32 a110, v70\n"
    "v_accvgpr_write_b32 a111, v71\n"
    "v_mfma_f32_32x32x16_bf16 v[176:191], a[224:227], a[96:99], v[176:191]\n"
    "ds_read_b64_tr_b16 a[252:253], v17 offset:6144\n"
    "ds_read_b64_tr_b16 a[254:255], v17 offset:7168\n"
    "v_lshlrev_b32 v20, 16, v56\n"
    "v_and_b32 v21, s42, v56\n"
    "v_lshlrev_b32 v22, 16, v57\n"
    "v_and_b32 v23, s42, v57\n"
    "v_lshlrev_b32 v24, 16, v58\n"
    "v_and_b32 v25, s42, v58\n"
    "v_lshlrev_b32 v26, 16, v59\n"
    "v_and_b32 v27, s42, v59\n"
    "v_pk_fma_f32 v[20:21], v[20:21], v[28:29], v[28:29] op_sel:[0,0,1] op_sel_hi:[1,0,1]\n"
    "v_pk_fma_f32 v[22:23], v[22:23], v[28:29], v[28:29] op_sel:[0,0,1] op_sel_hi:[1,0,1]\n"
    "v_mfma_f32_32x32x16_bf16 v[192:207], a[228:231], a[96:99], v[192:207]\n"
    "ds_read_b64_tr_b16 a[224:225], v16 offset:8192\n"
    "ds_read_b64_tr_b16 a[226:227], v16 offset:9216\n"
    "v_pk_fma_f32 v[24:25], v[24:25], v[28:29], v[28:29] op_sel:[0,0,1] op_sel_hi:[1,0,1]\n"
    "v_pk_fma_f32 v[26:27], v[26:27], v[28:29], v[28:29] op_sel:[0,0,1] op_sel_hi:[1,0,1]\n"
    "v_cvt_pk_bf16_f32 v56, v20, v21\n"
    "v_cvt_pk_bf16_f32 v57, v22, v23\n"
    "v_cvt_pk_bf16_f32 v58, v24, v25\n"
    "v_cvt_pk_bf16_f32 v59, v26, v27\n"
    "v_accvgpr_write_b32 a112, v56\n"
    "v_accvgpr_write_b32 a113, v57\n"
    "v_accvgpr_write_b32 a114, v58\n"
    "v_accvgpr_write_b32 a115, v59\n"
    "ds_read_b128 v[164:167], %[vb] offset:416\n"
    "v_mfma_f32_32x32x16_bf16 v[176:191], a[232:235], a[100:103], v[176:191]\n"
    "ds_read_b64_tr_b16 a[228:229], v17 offset:8192\n"
    "ds_read_b64_tr_b16 a[230:231], v17 offset:9216\n"
    "s_add_u32 m0, %[ldsw], 8192\n"
    "s_mov_b32 s40, 128\n"
    "v_lshlrev_b32 v20, 16, v60\n"
    "v_and_b32 v21, s42, v60\n"
    "v_lshlrev_b32 v22, 16, v61\n"
    "v_and_b32 v23, s42, v61\n"
    "v_lshlrev_b32 v24, 16, v62\n"
    "v_and_b32 v25, s42, v62\n"
    "v_lshlrev_b32 v26, 16, v63\n"
    "v_and_b32 v27, s42, v63\n"
    "v_pk_fma_f32 v[20:21], v[20:21], v[28:29], v[28:29] op_sel:[0,0,1] op_sel_hi:[1,0,1]\n"
    "v_pk_fma_f32 v[22:23], v[22:23], v[28:29], v[28:29] op_sel:[0,0,1] op_sel_hi:[1,0,1]\n"
    "v_mfma_f32_32x32x16_bf16 v[192:207], a[236:239], a[100:103], v[192:207]\n"
    "s_waitcnt lgkmcnt(5)\n"
    "ds_read_b64_tr_b16 a[232:233], v16 offset:10240\n"
    "ds_read_b64_tr_b16 a[234:235], v16 offset:11264\n"
    "buffer_load_dwordx4 %[w2a], %[srd2], s40 offen lds\n"
    "v_pk_fma_f32 v[24:25], v[24:25], v[28:29], v[28:29] op_sel:[0,0,1] op_sel_hi:[1,0,1]\n"
    "v_pk_fma_f32 v[26:27], v[26:27], v[28:29], v[28:29] op_sel:[0,0,1] op_sel_hi:[1,0,1]\n"
    "v_cvt_pk_bf16_f32 v60, v20, v21\n"
    "v_cvt_pk_bf16_f32 v61, v22, v23\n"
    "v_cvt_pk_bf16_f32 v62, v24, v25\n"
    "v_cvt_pk_bf16_f32 v63, v26, v27\n"
    "v_accvgpr_write_b32 a116, v60\n"
    "v_accvgpr_write_b32 a117, v61\n"
    "v_accvgpr_write_b32 a118, v62\n"
    "v_accvgpr_write_b32 a119, v63\n"
    "v_mfma_f32_32x32x16_bf16 v[176:191], a[240:243], a[104:107], v[176:191]\n"
    "ds_read_b64_tr_b16 a[236:237], v17 offset:10240\n"
    "ds_read_b64_tr_b16 a[238:239], v17 offset:11264\n"
    "v_lshlrev_b32 v20, 16, v48\n"
    "v_and_b32 v21, s42, v48\n"
    "v_lshlrev_b32 v22, 16, v49\n"
    "v_and_b32 v23, s42, v49\n"
    "v_lshlrev_b32 v24, 16, v50\n"
    "v_and_b32 v25, s42, v50\n"
    "v_lshlrev_b32 v26, 16, v51\n"
    "v_and_b32 v27, s42, v51\n"
    "v_pk_fma_f32 v[20:21], v[20:21], v[28:29], v[28:29] op_sel:[0,0,1] op_sel_hi:[1,0,1]\n"
    "v_pk_fma_f32 v[22:23], v[22:23], v[28:29], v[28:29] op_sel:[0,0,1] op_sel_hi:[1,0,1]\n"
    "v_mfma_f32_32x32x16_bf16 v[192:207], a[244:247], a[104:107], v[192:207]\n"
    "ds_read_b64_tr_b16 a[240:241], v16 offset:12288\n"
    "ds_read_b64_tr_b16 a[242:243], v16 offset:13312\n"
    "v_pk_fma_f32 v[24:25], v[24:25], v[28:29], v[28:29] op_sel:[0,0,1] op_sel_hi:[1,0,1]\n"
    "v_pk_fma_f32 v[26:27], v[26:27], v[28:29], v[28:29] op_sel:[0,0,1] op_sel_hi:[1,0,1]\n"
    "v_cvt_pk_bf16_f32 v48, v20, v21\n"
    "v_cvt_pk_bf16_f32 v49, v22, v23\n"
    "v_cvt_pk_bf16_f32 v50, v24, v25\n"
    "v_cvt_pk_bf16_f32 v51, v26, v27\n"
    "v_accvgpr_write_b32 a120, v48\n"
    "v_accvgpr_write_b32 a121, v49\n"
    "v_accvgpr_write_b32 a122, v50\n"
    "v_accvgpr_write_b32 a123, v51\n"
    "v_mfma_f32_32x32x16_bf16 v[176:191], a[248:251], a[108:111], v[176:191]\n"
    "ds_read_b64_tr_b16 a[244:245], v17 offset:12288\n"
    "ds_read_b64_tr_b16 a[246:247], v17 offset:13312\n"
    "v_lshlrev_b32 v20, 16, v52\n"
    "v_and_b32 v21, s42, v52\n"
    "v_lshlrev_b32 v22, 16, v53\n"
    "v_and_b32 v23, s42, v53\n"
    "v_lshlrev_b32 v24, 16, v54\n"
    "v_and_b32 v25, s42, v54\n"
    "v_lshlrev_b32 v26, 16, v55\n"
    "v_and_b32 v27, s42, v55\n"
    "v_pk_fma_f32 v[20:21], v[20:21], v[28:29], v[28:29] op_sel:[0,0,1] op_sel_hi:[1,0,1]\n"
    "v_pk_fma_f32 v[22:23], v[22:23], v[28:29], v[28:29] op_sel:[0,0,1] op_sel_hi:[1,0,1]\n"
    "ds_read_b128 v[168:171], %[vb] offset:448\n"
    "v_mfma_f32_32x32x16_bf16 v[192:207], a[252:255], a[108:111], v[192:207]\n"
    "s_waitcnt lgkmcnt(5)\n"
    "ds_read_b64_tr_b16 a[248:249], v16 offset:14336\n"
    "ds_read_b64_tr_b16 a[250:251], v16 offset:15360\n"
    "buffer_load_dwordx4 %[w2b], %[srd2], s40 offen offset:1024 lds\n"
    "v_pk_fma_f32 v[24:25], v[24:25], v[28:29], v[28:29] op_sel:[0,0,1] op_sel_hi:[1,0,1]\n"
    "v_pk_fma_f32 v[26:27], v[26:27], v[28:29], v[28:29] op_sel:[0,0,1] op_sel_hi:[1,0,1]\n"
    "v_cvt_pk_bf16_f32 v52, v20, v21\n"
    "v_cvt_pk_bf16_f32 v53, v22, v23\n"
    "v_cvt_pk_bf16_f32 v54, v24, v25\n"
    "v_cvt_pk_bf16_f32 v55, v26, v27\n"
    "v_accvgpr_write_b32 a124, v52\n"
    "v_accvgpr_write_b32 a125, v53\n"
    "v_accvgpr_write_b32 a126, v54\n"
    "v_accvgpr_write_b32 a127, v55\n"
    "v_mfma_f32_32x32x16_bf16 v[176:191], a[224:227], a[112:115], v[176:191]\n"
    "ds_read_b64_tr_b16 a[252:253], v17 offset:14336\n"
    "ds_read_b64_tr_b16 a[254:255], v17 offset:15360\n"
    "v_lshlrev_b32 v20, 16, v40\n"
    "v_and_b32 v21, s42, v40\n"
    "v_lshlrev_b32 v22, 16, v41\n"
    "v_and_b32 v23, s42, v41\n"
    "v_lshlrev_b32 v24, 16, v42\n"
    "v_and_b32 v25, s42, v42\n"
    "v_lshlrev_b32 v26, 16, v43\n"
    "v_and_b32 v27, s42, v43\n"
    "v_pk_fma_f32 v[20:21], v[20:21], v[28:29], v[28:29] op_sel:[0,0,1] op_sel_hi:[1,0,1]\n"
    "v_pk_fma_f32 v[22:23], v[22:23], v[28:29], v[28:29] op_sel:[0,0,1] op_sel_hi:[1,0,1]\n"
    "v_mfma_f32_32x32x16_bf16 v[192:207], a[228:231], a[112:115], v[192:207]\n"
    "ds_read_b64_tr_b16 a[224:225], v16 offset:16384\n"
    "ds_read_b64_tr_b16 a[226:227], v16 offset:17408\n"
    "v_pk_fma_f32 v[24:25], v[24:25], v[28:29], v[28:29] op_sel:[0,0,1] op_sel_hi:[1,0,1]\n"
    "v_pk_fma_f32 v[26:27], v[26:27], v[28:29], v[28:29] op_sel:[0,0,1] op_sel_hi:[1,0,1]\n"
    "v_cvt_pk_bf16_f32 v40, v20, v21\n"
    "v_cvt_pk_bf16_f32 v41, v22, v23\n"
    "v_cvt_pk_bf16_f32 v42, v24, v25\n"
    "v_cvt_pk_bf16_f32 v43, v26, v27\n"
    "v_accvgpr_write_b32 a128, v40\n"
    "v_accvgpr_write_b32 a129, v41\n"
    "v_accvgpr_write_b32 a130, v42\n"
    "v_accvgpr_write_b32 a131, v43\n"
    "v_mfma_f32_32x32x16_bf16 v[176:191], a[232:235], a[116:119], v[176:191]\n"
    "ds_read_b64_tr_b16 a[228:229], v17 offset:16384\n"
    "ds_read_b64_tr_b16 a[230:231], v17 offset:17408\n"
    "s_add_u32 m0, %[ldsw], 16384\n"
    "s_mov_b32 s40, 256\n"
    "v_lshlrev_b32 v20, 16, v44\n"
    "v_and_b32 v21, s42, v44\n"
    "v_lshlrev_b32 v22, 16, v45\n"
    "v_and_b32 v23, s42, v45\n"
    "v_lshlrev_b32 v24, 16, v46\n"
    "v_and_b32 v25, s42, v46\n"
    "v_lshlrev_b32 v26, 16, v47\n"
    "v_and_b32 v27, s42, v47\n"
    "v_pk_fma_f32 v[20:21], v[20:21], v[28:29], v[28:29] op_sel:[0,0,1] op_sel_hi:[1,0,1]\n"
    "v_pk_fma_f32 v[22:23], v[22:23], v[28:29], v[28:29] op_sel:[0,0,1] op_sel_hi:[1,0,1]\n"
    "v_mfma_f32_32x32x16_bf16 v[192:207], a[236:239], a[116:119], v[192:207]\n"
    "s_waitcnt lgkmcnt(4)\n"
    "ds_read_b64_tr_b16 a[232:233], v16 offset:18432\n"
    "ds_read_b64_tr_b16 a[234:235], v16 offset:19456\n"
    "buffer_load_dwordx4 %[w2a], %[srd2], s40 offen lds\n"
    "v_pk_fma_f32 v[24:25], v[24:25], v[28:29], v[28:29] op_sel:[0,0,1] op_sel_hi:[1,0,1]\n"
    "v_pk_fma_f32 v[26:27], v[26:27], v[28:29], v[28:29] op_sel:[0,0,1] op_sel_hi:[1,0,1]\n"
    "v_cvt_pk_bf16_f32 v44, v20, v21\n"
    "v_cvt_pk_bf16_f32 v45, v22, v23\n"
    "v_cvt_pk_bf16_f32 v46, v24, v25\n"
    "v_cvt_pk_bf16_f32 v47, v26, v27\n"
    "v_accvgpr_write_b32 a132, v44\n"
    "v_accvgpr_write_b32 a133, v45\n"
    "v_accvgpr_write_b32 a134, v46\n"
    "v_accvgpr_write_b32 a135, v47\n"
    "ds_read_b128 v[172:175], %[vb] offset:480\n"
    "v_mfma_f32_32x32x16_bf16 v[176:191], a[240:243], a[120:123], v[176:191]\n"
    "ds_read_b64_tr_b16 a[236:237], v17 offset:18432\n"
    "ds_read_b64_tr_b16 a[238:239], v17 offset:19456\n"
    "v_lshlrev_b32 v20, 16, v32\n"
    "v_and_b32 v21, s42, v32\n"
    "v_lshlrev_b32 v22, 16, v33\n"
    "v_and_b32 v23, s42, v33\n"
    "v_lshlrev_b32 v24, 16, v34\n"
    "v_and_b32 v25, s42, v34\n"
    "v_lshlrev_b32 v26, 16, v35\n"
    "v_and_b32 v27, s42, v35\n"
    "v_pk_fma_f32 v[20:21], v[20:21], v[28:29], v[28:29] op_sel:[0,0,1] op_sel_hi:[1,0,1]\n"
    "v_pk_fma_f32 v[22:23], v[22:23], v[28:29], v[28:29] op_sel:[0,0,1] op_sel_hi:[1,0,1]\n"
    "v_mfma_f32_32x32x16_bf16 v[192:207], a[244:247], a[120:123], v[192:207]\n"
    "ds_read_b64_tr_b16 a[240:241], v16 offset:20480\n"
    "ds_read_b64_tr_b16 a[242:243], v16 offset:21504\n"
    "v_pk_fma_f32 v[24:25], v[24:25], v[28:29], v[28:29] op_sel:[0,0,1] op_sel_hi:[1,0,1]\n"
    "v_pk_fma_f32 v[26:27], v[26:27], v[28:29], v[28:29] op_sel:[0,0,1] op_sel_hi:[1,0,1]\n"
    "v_cvt_pk_bf16_f32 v32, v20, v21\n"
    "v_cvt_pk_bf16_f32 v33, v22, v23\n"
    "v_cvt_pk_bf16_f32 v34, v24, v25\n"
    "v_cvt_pk_bf16_f32 v35, v26, v27\n"
    "v_accvgpr_write_b32 a136, v32\n"
    "v_accvgpr_write_b32 a137, v33\n"
    "v_accvgpr_write_b32 a138, v34\n"
    "v_accvgpr_write_b32 a139, v35\n"
    "v_mfma_f32_32x32x16_bf16 v[176:191], a[248:251], a[124:127], v[176:191]\n"
    "ds_read_b64_tr_b16 a[244:245], v17 offset:20480\n"
    "ds_read_b64_tr_b16 a[246:247], v17 offset:21504\n"
    "v_lshlrev_b32 v20, 16, v36\n"
    "v_and_b32 v21, s42, v36\n"
    "v_lshlrev_b32 v22, 16, v37\n"
    "v_and_b32 v23, s42, v37\n"
    "v_lshlrev_b32 v24, 16, v38\n"
    "v_and_b32 v25, s42, v38\n"
    "v_lshlrev_b32 v26, 16, v39\n"
    "v_and_b32 v27, s42, v39\n"
    "v_pk_fma_f32 v[20:21], v[20:21], v[28:29], v[28:29] op_sel:[0,0,1] op_sel_hi:[1,0,1]\n"
    "v_pk_fma_f32 v[22:23], v[22:23], v[28:29], v[28:29] op_sel:[0,0,1] op_sel_hi:[1,0,1]\n"
    "v_mfma_f32_32x32x16_bf16 v[192:207], a[252:255], a[124:127], v[192:207]\n"
    "s_waitcnt lgkmcnt(4)\n"
    "ds_read_b64_tr_b16 a[248:249], v16 offset:22528\n"
    "ds_read_b64_tr_b16 a[250:251], v16 offset:23552\n"
    "buffer_load_dwordx4 %[w2b], %[srd2], s40 offen offset:1024 lds\n"
    "v_pk_fma_f32 v[24:25], v[24:25], v[28:29], v[28:29] op_sel:[0,0,1] op_sel_hi:[1,0,1]\n"
    "v_pk_fma_f32 v[26:27], v[26:27], v[28:29], v[28:29] op_sel:[0,0,1] op_sel_hi:[1,0,1]\n"
    "v_cvt_pk_bf16_f32 v36, v20, v21\n"
    "v_cvt_pk_bf16_f32 v37, v22, v23\n"
    "v_cvt_pk_bf16_f32 v38, v24, v25\n"
    "v_cvt_pk_bf16_f32 v39, v26, v27\n"
    "v_accvgpr_write_b32 a140, v36\n"
    "v_accvgpr_write_b32 a141, v37\n"
    "v_accvgpr_write_b32 a142, v38\n"
    "v_accvgpr_write_b32 a143, v39\n"
    "v_mfma_f32_32x32x16_bf16 v[176:191], a[224:227], a[128:131], v[176:191]\n"
    "ds_read_b64_tr_b16 a[252:253], v17 offset:22528\n"
    "ds_read_b64_tr_b16 a[254:255], v17 offset:23552\n"
    "v_mfma_f32_32x32x16_bf16 v[192:207], a[228:231], a[128:131], v[192:207]\n"
    "s_waitcnt vmcnt(18)\n"
    "s_barrier\n"
    "ds_read_b64_tr_b16 a[224:225], v16 offset:24576\n"
    "ds_read_b64_tr_b16 a[226:227], v16 offset:25600\n"
    "v_mfma_f32_32x32x16_bf16 v[176:191], a[232:235], a[132:135], v[176:191]\n"
    "ds_read_b64_tr_b16 a[228:229], v17 offset:24576\n"
    "ds_read_b64_tr_b16 a[230:231], v17 offset:25600\n"
    "s_add_u32 m0, %[ldsw], 24576\n"
    "s_mov_b32 s40, 384\n"
    "v_mfma_f32_32x32x16_bf16 v[192:207], a[236:239], a[132:135], v[192:207]\n"
    "s_waitcnt lgkmcnt(4)\n"
    "ds_read_b64_tr_b16 a[232:233], v16 offset:26624\n"
    "ds_read_b64_tr_b16 a[234:235], v16 offset:27648\n"
    "buffer_load_dwordx4 %[w2a], %[srd2], s40 offen lds\n"
    "v_mfma_f32_32x32x16_bf16 v[176:191], a[240:243], a[136:139], v[176:191]\n"
    "ds_read_b64_tr_b16 a[236:237], v17 offset:26624\n"
    "ds_read_b64_tr_b16 a[238:239], v17 offset:27648\n"
    "v_mfma_f32_32x32x16_bf16 v[192:207], a[244:247], a[136:139], v[192:207]\n"
    "ds_read_b64_tr_b16 a[240:241], v16 offset:28672\n"
    "ds_read_b64_tr_b16 a[242:243], v16 offset:29696\n"
    "v_mfma_f32_32x32x16_bf16 v[176:191], a[248:251], a[140:143], v[176:191]\n"
    "ds_read_b64_tr_b16 a[244:245], v17 offset:28672\n"
    "ds_read_b64_tr_b16 a[246:247], v17 offset:29696\n"
    "v_mfma_f32_32x32x16_bf16 v[192:207], a[252:255], a[140:143], v[192:207]\n"
    "s_waitcnt lgkmcnt(4)\n"
    "ds_read_b64_tr_b16 a[248:249], v16 offset:30720\n"
    "ds_read_b64_tr_b16 a[250:251], v16 offset:31744\n"
    "buffer_load_dwordx4 %[w2b], %[srd2], s40 offen offset:1024 lds\n"
    "v_mfma_f32_32x32x16_bf16 v[144:159], a[224:227], a[0:3], v[144:159]\n"
    "ds_read_b64_tr_b16 a[252:253], v17 offset:30720\n"
    "ds_read_b64_tr_b16 a[254:255], v17 offset:31744\n"
    "v_mfma_f32_32x32x16_bf16 v[160:175], a[228:231], a[0:3], v[160:175]\n"
    "ds_read_b64_tr_b16 a[224:225], v16 offset:32768\n"
    "ds_read_b64_tr_b16 a[226:227], v16 offset:33792\n"
    "v_mfma_f32_32x32x16_bf16 v[144:159], a[232:235], a[4:7], v[144:159]\n"
    "ds_read_b64_tr_b16 a[228:229], v17 offset:32768\n"
    "ds_read_b64_tr_b16 a[230:231], v17 offset:33792\n"
    "s_add_u32 m0, %[ldsw], 32768\n"
    "s_mov_b32 s40, 256\n"
    "v_mfma_f32_32x32x16_bf16 v[160:175], a[236:239], a[4:7], v[160:175]\n"
    "s_waitcnt lgkmcnt(4)\n"
    "ds_read_b64_tr_b16 a[232:233], v16 offset:34816\n"
    "ds_read_b64_tr_b16 a[234:235], v16 offset:35840\n"
    "buffer_load_dwordx4 %[w1a], %[srd1], s40 offen lds\n"
    "v_fmaak_f32 v20, v176, v176, 0x409d30fc\n"
    "v_mul_f32 v20, v20, v176\n"
    "v_exp_f32 v20, v20\n"
    "v_fmaak_f32 v21, v177, v177, 0x409d30fc\n"
    "v_mfma_f32_32x32x16_bf16 v[144:159], a[240:243], a[8:11], v[144:159]\n"
    "ds_read_b64_tr_b16 a[236:237], v17 offset:34816\n"
    "ds_read_b64_tr_b16 a[238:239], v17 offset:35840\n"
    "v_add_f32 v20, 1.0, v20\n"
    "v_rcp_f32 v20, v20\n"
    "v_mul_f32 v21, v21, v177\n"
    "v_mfma_f32_32x32x16_bf16 v[160:175], a[244:247], a[8:11], v[160:175]\n"
    "ds_read_b64_tr_b16 a[240:241], v16 offset:36864\n"
    "ds_read_b64_tr_b16 a[242:243], v16 offset:37888\n"
    "v_mul_f32 v20, v20, v176\n"
    "v_exp_f32 v21, v21\n"
    "v_fmaak_f32 v22, v178, v178, 0x409d30fc\n"
    "v_mfma_f32_32x32x16_bf16 v[144:159], a[248:251], a[12:15], v[144:159]\n"
    "ds_read_b64_tr_b16 a[244:245], v17 offset:36864\n"
    "ds_read_b64_tr_b16 a[246:247], v17 offset:37888\n"
    "v_add_f32 v21, 1.0, v21\n"
    "v_rcp_f32 v21, v21\n"
    "v_mul_f32 v22, v22, v178\n"
    "v_mul_f32 v21, v21, v177\n"
    "v_mfma_f32_32x32x16_bf16 v[160:175], a[252:255], a[12:15], v[160:175]\n"
    "s_waitcnt lgkmcnt(4)\n"
    "ds_read_b64_tr_b16 a[248:249], v16 offset:38912\n"
    "ds_read_b64_tr_b16 a[250:251], v16 offset:39936\n"
    "buffer_load_dwordx4 %[w1b], %[srd1], s40 offen offset:1024 lds\n"
    "v_cvt_pk_bf16_f32 v32, v20, v21\n"
    "v_exp_f32 v22, v22\n"
    "v_fmaak_f32 v20, v179, v179, 0x409d30fc\n"
    "v_mfma_f32_32x32x16_bf16 v[144:159], a[224:227], a[16:19], v[144:159]\n"
    "ds_read_b64_tr_b16 a[252:253], v17 offset:38912\n"
    "ds_read_b64_tr_b16 a[254:255], v17 offset:39936\n"
    "v_add_f32 v22, 1.0, v22\n"
    "v_rcp_f32 v22, v22\n"
    "v_mul_f32 v20, v20, v179\n"
    "v_mfma_f32_32x32x16_bf16 v[160:175], a[228:231], a[16:19], v[160:175]\n"
    "ds_read_b64_tr_b16 a[224:225], v16 offset:40960\n"
    "ds_read_b64_tr_b16 a[226:227], v16 offset:41984\n"
    "v_mul_f32 v22, v22, v178\n"
    "v_exp_f32 v20, v20\n"
    "v_fmaak_f32 v21, v180, v180, 0x409d30fc\n"
    "v_mfma_f32_32x32x16_bf16 v[144:159], a[232:235], a[20:23], v[144:159]\n"
    "ds_read_b64_tr_b16 a[228:229], v17 offset:40960\n"
    "ds_read_b64_tr_b16 a[230:231], v17 offset:41984\n"
    "s_add_u32 m0, %[ldsw], 40960\n"
    "s_mov_b32 s40, 590080\n"
    "v_add_f32 v20, 1.0, v20\n"
    "v_rcp_f32 v20, v20\n"
    "v_mul_f32 v21, v21, v180\n"
    "v_mul_f32 v20, v20, v179\n"
    "v_mfma_f32_32x32x16_bf16 v[160:175], a[236:239], a[20:23], v[160:175]\n"
    "s_waitcnt lgkmcnt(4)\n"
    "ds_read_b64_tr_b16 a[232:233], v16 offset:43008\n"
    "ds_read_b64_tr_b16 a[234:235], v16 offset:44032\n"
    "buffer_load_dwordx4 %[w1a], %[srd1], s40 offen lds\n"
    "v_cvt_pk_bf16_f32 v33, v22, v20\n"
    "v_exp_f32 v21, v21\n"
    "v_fmaak_f32 v22, v181, v181, 0x409d30fc\n"
    "v_mfma_f32_32x32x16_bf16 v[144:159], a[240:243], a[24:27], v[144:159]\n"
    "ds_read_b64_tr_b16 a[236:237], v17 offset:43008\n"
    "ds_read_b64_tr_b16 a[238:239], v17 offset:44032\n"
    "v_add_f32 v21, 1.0, v21\n"
    "v_rcp_f32 v21, v21\n"
    "v_mul_f32 v22, v22, v181\n"
    "v_mfma_f32_32x32x16_bf16 v[160:175], a[244:247], a[24:27], v[160:175]\n"
    "ds_read_b64_tr_b16 a[240:241], v16 offset:45056\n"
    "ds_read_b64_tr_b16 a[242:243], v16 offset:46080\n"
    "v_mul_f32 v21, v21, v180\n"
    "v_exp_f32 v22, v22\n"
    "v_fmaak_f32 v20, v182, v182, 0x409d30fc\n"
    "v_add_f32 v22, 1.0, v22\n"
    "v_mfma_f32_32x32x16_bf16 v[144:159], a[248:251], a[28:31], v[144:159]\n"
    "ds_read_b64_tr_b16 a[244:245], v17 offset:45056\n"
    "ds_read_b64_tr_b16 a[246:247], v17 offset:46080\n"
    "v_rcp_f32 v22, v22\n"
    "v_mul_f32 v20, v20, v182\n"
    "v_mul_f32 v22, v22, v181\n"
    "v_mfma_f32_32x32x16_bf16 v[160:175], a[252:255], a[28:31], v[160:175]\n"
    "s_waitcnt lgkmcnt(4)\n"
    "ds_read_b64_tr_b16 a[248:249], v16 offset:47104\n"
    "ds_read_b64_tr_b16 a[250:251], v16 offset:48128\n"
    "buffer_load_dwordx4 %[w1b], %[srd1], s40 offen offset:1024 lds\n"
    "v_cvt_pk_bf16_f32 v34, v21, v22\n"
    "v_exp_f32 v20, v20\n"
    "v_fmaak_f32 v21, v183, v183, 0x409d30fc\n"
    "v_mfma_f32_32x32x16_bf16 v[144:159], a[224:227], a[32:35], v[144:159]\n"
    "ds_read_b64_tr_b16 a[252:253], v17 offset:47104\n"
    "ds_read_b64_tr_b16 a[254:255], v17 offset:48128\n"
    "v_add_f32 v20, 1.0, v20\n"
    "v_rcp_f32 v20, v20\n"
    "v_mul_f32 v21, v21, v183\n"
    "v_mfma_f32_32x32x16_bf16 v[160:175], a[228:231], a[32:35], v[160:175]\n"
    "s_waitcnt vmcnt(18)\n"
    "s_barrier\n"
    "ds_read_b64_tr_b16 a[224:225], v18 offset:0\n"
    "ds_read_b64_tr_b16 a[226:227], v18 offset:1024\n"
    "v_mfma_f32_32x32x16_bf16 v[144:159], a[232:235], a[36:39], v[144:159]\n"
    "ds_read_b64_tr_b16 a[228:229], v19 offset:0\n"
    "ds_read_b64_tr_b16 a[230:231], v19 offset:1024\n"
    "s_add_u32 m0, %[ldsw], 49152\n"
    "s_mov_b32 s40, 1179904\n"
    "v_mul_f32 v20, v20, v182\n"
    "v_exp_f32 v21, v21\n"
    "v_fmaak_f32 v22, v184, v184, 0x409d30fc\n"
    "v_add_f32 v21, 1.0, v21\n"
    "v_mfma_f32_32x32x16_bf16 v[160:175], a[236:239], a[36:39], v[160:175]\n"
    "s_waitcnt lgkmcnt(4)\n"
    "ds_read_b64_tr_b16 a[232:233], v18 offset:2048\n"
    "ds_read_b64_tr_b16 a[234:235], v18 offset:3072\n"
    "buffer_load_dwordx4 %[w1a], %[srd1], s40 offen lds\n"
    "v_rcp_f32 v21, v21\n"
    "v_mul_f32 v22, v22, v184\n"
    "v_mul_f32 v21, v21, v183\n"
    "v_mfma_f32_32x32x16_bf16 v[144:159], a[240:243], a[40:43], v[144:159]\n"
    "ds_read_b64_tr_b16 a[236:237], v19 offset:2048\n"
    "ds_read_b64_tr_b16 a[238:239], v19 offset:3072\n"
    "v_cvt_pk_bf16_f32 v35, v20, v21\n"
    "v_exp_f32 v22, v22\n"
    "v_fmaak_f32 v20, v185, v185, 0x409d30fc\n"
    "v_mfma_f32_32x32x16_bf16 v[160:175], a[244:247], a[40:43], v[160:175]\n"
    "ds_read_b64_tr_b16 a[240:241], v18 offset:4096\n"
    "ds_read_b64_tr_b16 a[242:243], v18 offset:5120\n"
    "v_add_f32 v22, 1.0, v22\n"
    "v_rcp_f32 v22, v22\n"
    "v_mul_f32 v20, v20, v185\n"
    "v_mul_f32 v22, v22, v184\n"
    "v_mfma_f32_32x32x16_bf16 v[144:159], a[248:251], a[44:47], v[144:159]\n"
    "ds_read_b64_tr_b16 a[244:245], v19 offset:4096\n"
    "ds_read_b64_tr_b16 a[246:247], v19 offset:5120\n"
    "v_exp_f32 v20, v20\n"
    "v_fmaak_f32 v21, v186, v186, 0x409d30fc\n"
    "v_add_f32 v20, 1.0, v20\n"
    "v_mfma_f32_32x32x16_bf16 v[160:175], a[252:255], a[44:47], v[160:175]\n"
    "s_waitcnt lgkmcnt(4)\n"
    "ds_read_b64_tr_b16 a[248:249], v18 offset:6144\n"
    "ds_read_b64_tr_b16 a[250:251], v18 offset:7168\n"
    "buffer_load_dwordx4 %[w1b], %[srd1], s40 offen offset:1024 lds\n"
    "v_rcp_f32 v20, v20\n"
    "v_mul_f32 v21, v21, v186\n"
    "v_mul_f32 v20, v20, v185\n"
    "v_mfma_f32_32x32x16_bf16 v[144:159], a[224:227], a[48:51], v[144:159]\n"
    "ds_read_b64_tr_b16 a[252:253], v19 offset:6144\n"
    "ds_read_b64_tr_b16 a[254:255], v19 offset:7168\n"
    "v_cvt_pk_bf16_f32 v36, v22, v20\n"
    "v_exp_f32 v21, v21\n"
    "v_fmaak_f32 v22, v187, v187, 0x409d30fc\n"
    "v_mfma_f32_32x32x16_bf16 v[160:175], a[228:231], a[48:51], v[160:175]\n"
    "ds_read_b64_tr_b16 a[224:225], v18 offset:8192\n"
    "ds_read_b64_tr_b16 a[226:227], v18 offset:9216\n"
    "v_add_f32 v21, 1.0, v21\n"
    "v_rcp_f32 v21, v21\n"
    "v_mul_f32 v22, v22, v187\n"
    "v_mul_f32 v21, v21, v186\n"
    "v_mfma_f32_32x32x16_bf16 v[144:159], a[232:235], a[52:55], v[144:159]\n"
    "ds_read_b64_tr_b16 a[228:229], v19 offset:8192\n"
    "ds_read_b64_tr_b16 a[230:231], v19 offset:9216\n"
    "s_add_u32 m0, %[ldsw], 57344\n"
    "s_mov_b32 s40, 1769728\n"
    "v_exp_f32 v22, v22\n"
    "v_fmaak_f32 v20, v188, v188, 0x409d30fc\n"
    "v_add_f32 v22, 1.0, v22\n"
    "v_mfma_f32_32x32x16_bf16 v[160:175], a[236:239], a[52:55], v[160:175]\n"
    "s_waitcnt lgkmcnt(4)\n"
    "ds_read_b64_tr_b16 a[232:233], v18 offset:10240\n"
    "ds_read_b64_tr_b16 a[234:235], v18 offset:11264\n"
    "buffer_load_dwordx4 %[w1a], %[srd1], s40 offen lds\n"
    "v_rcp_f32 v22, v22\n"
    "v_mul_f32 v20, v20, v188\n"
    "v_mul_f32 v22, v22, v187\n"
    "v_mfma_f32_32x32x16_bf16 v[144:159], a[240:243], a[56:59], v[144:159]\n"
    "ds_read_b64_tr_b16 a[236:237], v19 offset:10240\n"
    "ds_read_b64_tr_b16 a[238:239], v19 offset:11264\n"
    "v_cvt_pk_bf16_f32 v37, v21, v22\n"
    "v_exp_f32 v20, v20\n"
    "v_fmaak_f32 v21, v189, v189, 0x409d30fc\n"
    "v_add_f32 v20, 1.0, v20\n"
    "v_mfma_f32_32x32x16_bf16 v[160:175], a[244:247], a[56:59], v[160:175]\n"
    "ds_read_b64_tr_b16 a[240:241], v18 offset:12288\n"
    "ds_read_b64_tr_b16 a[242:243], v18 offset:13312\n"
    "v_rcp_f32 v20, v20\n"
    "v_mul_f32 v21, v21, v189\n"
    "v_mul_f32 v20, v20, v188\n"
    "v_mfma_f32_32x32x16_bf16 v[144:159], a[248:251], a[60:63], v[144:159]\n"
    "ds_read_b64_tr_b16 a[244:245], v19 offset:12288\n"
    "ds_read_b64_tr_b16 a[246:247], v19 offset:13312\n"
    "v_exp_f32 v21, v21\n"
    "v_fmaak_f32 v22, v190, v190, 0x409d30fc\n"
    "v_add_f32 v21, 1.0, v21\n"
    "v_mfma_f32_32x32x16_bf16 v[160:175], a[252:255], a[60:63], v[160:175]\n"
    "s_waitcnt lgkmcnt(4)\n"
    "ds_read_b64_tr_b16 a[248:249], v18 offset:14336\n"
    "ds_read_b64_tr_b16 a[250:251], v18 offset:15360\n"
    "buffer_load_dwordx4 %[w1b], %[srd1], s40 offen offset:1024 lds\n"
    "v_rcp_f32 v21, v21\n"
    "v_mul_f32 v22, v22, v190\n"
    "v_mul_f32 v21, v21, v189\n"
    "v_mfma_f32_32x32x16_bf16 v[144:159], a[224:227], a[64:67], v[144:159]\n"
    "ds_read_b64_tr_b16 a[252:253], v19 offset:14336\n"
    "ds_read_b64_tr_b16 a[254:255], v19 offset:15360\n"
    "v_cvt_pk_bf16_f32 v38, v20, v21\n"
    "v_exp_f32 v22, v22\n"
    "v_fmaak_f32 v20, v191, v191, 0x409d30fc\n"
    "v_add_f32 v22, 1.0, v22\n"
    "v_mfma_f32_32x32x16_bf16 v[160:175], a[228:231], a[64:67], v[160:175]\n"
    "ds_read_b64_tr_b16 a[224:225], v18 offset:16384\n"
    "ds_read_b64_tr_b16 a[226:227], v18 offset:17408\n"
    "v_rcp_f32 v22, v22\n"
    "v_mul_f32 v20, v20, v191\n"
    "v_mul_f32 v22, v22, v190\n"
    "v_mfma_f32_32x32x16_bf16 v[144:159], a[232:235], a[68:71], v[144:159]\n"
    "ds_read_b64_tr_b16 a[228:229], v19 offset:16384\n"
    "ds_read_b64_tr_b16 a[230:231], v19 offset:17408\n"
    "s_add_u32 m0, %[ldsw], 65536\n"
    "s_mov_b32 s40, 2359552\n"
    "v_exp_f32 v20, v20\n"
    "v_fmaak_f32 v21, v192, v192, 0x409d30fc\n"
    "v_add_f32 v20, 1.0, v20\n"
    "v_mfma_f32_32x32x16_bf16 v[160:175], a[236:239], a[68:71], v[160:175]\n"
    "s_waitcnt lgkmcnt(4)\n"
    "ds_read_b64_tr_b16 a[232:233], v18 offset:18432\n"
    "ds_read_b64_tr_b16 a[234:235], v18 offset:19456\n"
    "buffer_load_dwordx4 %[w1a], %[srd1], s40 offen lds\n"
    "v_rcp_f32 v20, v20\n"
    "v_mul_f32 v21, v21, v192\n"
    "v_mul_f32 v20, v20, v191\n"
    "v_mfma_f32_32x32x16_bf16 v[144:159], a[240:243], a[72:75], v[144:159]\n"
    "ds_read_b64_tr_b16 a[236:237], v19 offset:18432\n"
    "ds_read_b64_tr_b16 a[238:239], v19 offset:19456\n"
    "v_cvt_pk_bf16_f32 v39, v22, v20\n"
    "v_exp_f32 v21, v21\n"
    "v_fmaak_f32 v22, v193, v193, 0x409d30fc\n"
    "v_add_f32 v21, 1.0, v21\n"
    "v_mfma_f32_32x32x16_bf16 v[160:175], a[244:247], a[72:75], v[160:175]\n"
    "ds_read_b64_tr_b16 a[240:241], v18 offset:20480\n"
    "ds_read_b64_tr_b16 a[242:243], v18 offset:21504\n"
    "v_rcp_f32 v21, v21\n"
    "v_mul_f32 v22, v22, v193\n"
    "v_mul_f32 v21, v21, v192\n"
    "v_mfma_f32_32x32x16_bf16 v[144:159], a[248:251], a[76:79], v[144:159]\n"
    "ds_read_b64_tr_b16 a[244:245], v19 offset:20480\n"
    "ds_read_b64_tr_b16 a[246:247], v19 offset:21504\n"
    "v_exp_f32 v22, v22\n"
    "v_fmaak_f32 v20, v194, v194, 0x409d30fc\n"
    "v_add_f32 v22, 1.0, v22\n"
    "v_mfma_f32_32x32x16_bf16 v[160:175], a[252:255], a[76:79], v[160:175]\n"
    "s_waitcnt lgkmcnt(4)\n"
    "ds_read_b64_tr_b16 a[248:249], v18 offset:22528\n"
    "ds_read_b64_tr_b16 a[250:251], v18 offset:23552\n"
    "buffer_load_dwordx4 %[w1b], %[srd1], s40 offen offset:1024 lds\n"
    "v_rcp_f32 v22, v22\n"
    "v_mul_f32 v20, v20, v194\n"
    "v_mul_f32 v22, v22, v193\n"
    "v_cvt_pk_bf16_f32 v40, v21, v22\n"
    "v_mfma_f32_32x32x16_bf16 v[144:159], a[224:227], a[80:83], v[144:159]\n"
    "ds_read_b64_tr_b16 a[252:253], v19 offset:22528\n"
    "ds_read_b64_tr_b16 a[254:255], v19 offset:23552\n"
    "v_exp_f32 v20, v20\n"
    "v_fmaak_f32 v21, v195, v195, 0x409d30fc\n"
    "v_add_f32 v20, 1.0, v20\n"
    "v_mfma_f32_32x32x16_bf16 v[160:175], a[228:231], a[80:83], v[160:175]\n"
    "s_waitcnt vmcnt(18)\n"
    "s_barrier\n"
    "ds_read_b64_tr_b16 a[224:225], v18 offset:24576\n"
    "ds_read_b64_tr_b16 a[226:227], v18 offset:25600\n"
    "v_mfma_f32_32x32x16_bf16 v[144:159], a[232:235], a[84:87], v[144:159]\n"
    "ds_read_b64_tr_b16 a[228:229], v19 offset:24576\n"
    "ds_read_b64_tr_b16 a[230:231], v19 offset:25600\n"
    "s_add_u32 m0, %[ldsw], 73728\n"
    "s_mov_b32 s40, 2949376\n"
    "v_rcp_f32 v20, v20\n"
    "v_mul_f32 v21, v21, v195\n"
    "v_mul_f32 v20, v20, v194\n"
    "v_mfma_f32_32x32x16_bf16 v[160:175], a[236:239], a[84:87], v[160:175]\n"
    "s_waitcnt lgkmcnt(4)\n"
    "ds_read_b64_tr_b16 a[232:233], v18 offset:26624\n"
    "ds_read_b64_tr_b16 a[234:235], v18 offset:27648\n"
    "buffer_load_dwordx4 %[w1a], %[srd1], s40 offen lds\n"
    "v_exp_f32 v21, v21\n"
    "v_fmaak_f32 v22, v196, v196, 0x409d30fc\n"
    "v_add_f32 v21, 1.0, v21\n"
    "v_mfma_f32_32x32x16_bf16 v[144:159], a[240:243], a[88:91], v[144:159]\n"
    "ds_read_b64_tr_b16 a[236:237], v19 offset:26624\n"
    "ds_read_b64_tr_b16 a[238:239], v19 offset:27648\n"
    "v_rcp_f32 v21, v21\n"
    "v_mul_f32 v22, v22, v196\n"
    "v_mul_f32 v21, v21, v195\n"
    "v_cvt_pk_bf16_f32 v41, v20, v21\n"
    "v_mfma_f32_32x32x16_bf16 v[160:175], a[244:247], a[88:91], v[160:175]\n"
    "ds_read_b64_tr_b16 a[240:241], v18 offset:28672\n"
    "ds_read_b64_tr_b16 a[242:243], v18 offset:29696\n"
    "v_exp_f32 v22, v22\n"
    "v_fmaak_f32 v20, v197, v197, 0x409d30fc\n"
    "v_add_f32 v22, 1.0, v22\n"
    "v_mfma_f32_32x32x16_bf16 v[144:159], a[248:251], a[92:95], v[144:159]\n"
    "ds_read_b64_tr_b16 a[244:245], v19 offset:28672\n"
    "ds_read_b64_tr_b16 a[246:247], v19 offset:29696\n"
    "v_rcp_f32 v22, v22\n"
    "v_mul_f32 v20, v20, v197\n"
    "v_mul_f32 v22, v22, v196\n"
    "v_mfma_f32_32x32x16_bf16 v[160:175], a[252:255], a[92:95], v[160:175]\n"
    "s_waitcnt lgkmcnt(4)\n"
    "ds_read_b64_tr_b16 a[248:249], v18 offset:30720\n"
    "ds_read_b64_tr_b16 a[250:251], v18 offset:31744\n"
    "buffer_load_dwordx4 %[w1b], %[srd1], s40 offen offset:1024 lds\n"
    "v_exp_f32 v20, v20\n"
    "v_fmaak_f32 v21, v198, v198, 0x409d30fc\n"
    "v_add_f32 v20, 1.0, v20\n"
    "v_rcp_f32 v20, v20\n"
    "v_mfma_f32_32x32x16_bf16 v[144:159], a[224:227], a[96:99], v[144:159]\n"
    "ds_read_b64_tr_b16 a[252:253], v19 offset:30720\n"
    "ds_read_b64_tr_b16 a[254:255], v19 offset:31744\n"
    "v_mul_f32 v21, v21, v198\n"
    "v_mul_f32 v20, v20, v197\n"
    "v_cvt_pk_bf16_f32 v42, v22, v20\n"
    "v_mfma_f32_32x32x16_bf16 v[160:175], a[228:231], a[96:99], v[160:175]\n"
    "ds_read_b64_tr_b16 a[224:225], v18 offset:32768\n"
    "ds_read_b64_tr_b16 a[226:227], v18 offset:33792\n"
    "v_exp_f32 v21, v21\n"
    "v_fmaak_f32 v22, v199, v199, 0x409d30fc\n"
    "v_add_f32 v21, 1.0, v21\n"
    "v_mfma_f32_32x32x16_bf16 v[144:159], a[232:235], a[100:103], v[144:159]\n"
    "ds_read_b64_tr_b16 a[228:229], v19 offset:32768\n"
    "ds_read_b64_tr_b16 a[230:231], v19 offset:33792\n"
    "s_add_u32 m0, %[ldsw], 81920\n"
    "s_mov_b32 s40, 3539200\n"
    "v_rcp_f32 v21, v21\n"
    "v_mul_f32 v22, v22, v199\n"
    "v_mul_f32 v21, v21, v198\n"
    "v_mfma_f32_32x32x16_bf16 v[160:175], a[236:239], a[100:103], v[160:175]\n"
    "s_waitcnt lgkmcnt(4)\n"
    "ds_read_b64_tr_b16 a[232:233], v18 offset:34816\n"
    "ds_read_b64_tr_b16 a[234:235], v18 offset:35840\n"
    "buffer_load_dwordx4 %[w1a], %[srd1], s40 offen lds\n"
    "v_exp_f32 v22, v22\n"
    "v_fmaak_f32 v20, v200, v200, 0x409d30fc\n"
    "v_add_f32 v22, 1.0, v22\n"
    "v_rcp_f32 v22, v22\n"
    "v_mfma_f32_32x32x16_bf16 v[144:159], a[240:243], a[104:107], v[144:159]\n"
    "ds_read_b64_tr_b16 a[236:237], v19 offset:34816\n"
    "ds_read_b64_tr_b16 a[238:239], v19 offset:35840\n"
    "v_mul_f32 v20, v20, v200\n"
    "v_mul_f32 v22, v22, v199\n"
    "v_cvt_pk_bf16_f32 v43, v21, v22\n"
    "v_mfma_f32_32x32x16_bf16 v[160:175], a[244:247], a[104:107], v[160:175]\n"
    "ds_read_b64_tr_b16 a[240:241], v18 offset:36864\n"
    "ds_read_b64_tr_b16 a[242:243], v18 offset:37888\n"
    "v_exp_f32 v20, v20\n"
    "v_fmaak_f32 v21, v201, v201, 0x409d30fc\n"
    "v_add_f32 v20, 1.0, v20\n"
    "v_mfma_f32_32x32x16_bf16 v[144:159], a[248:251], a[108:111], v[144:159]\n"
    "ds_read_b64_tr_b16 a[244:245], v19 offset:36864\n"
    "ds_read_b64_tr_b16 a[246:247], v19 offset:37888\n"
    "v_rcp_f32 v20, v20\n"
    "v_mul_f32 v21, v21, v201\n"
    "v_mul_f32 v20, v20, v200\n"
    "v_exp_f32 v21, v21\n"
    "v_mfma_f32_32x32x16_bf16 v[160:175], a[252:255], a[108:111], v[160:175]\n"
    "s_waitcnt lgkmcnt(4)\n"
    "ds_read_b64_tr_b16 a[248:249], v18 offset:38912\n"
    "ds_read_b64_tr_b16 a[250:251], v18 offset:39936\n"
    "buffer_load_dwordx4 %[w1b], %[srd1], s40 offen offset:1024 lds\n"
    "v_fmaak_f32 v22, v202, v202, 0x409d30fc\n"
    "v_add_f32 v21, 1.0, v21\n"
    "v_rcp_f32 v21, v21\n"
    "v_mfma_f32_32x32x16_bf16 v[144:159], a[224:227], a[112:115], v[144:159]\n"
    "ds_read_b64_tr_b16 a[252:253], v19 offset:38912\n"
    "ds_read_b64_tr_b16 a[254:255], v19 offset:39936\n"
    "v_mul_f32 v22, v22, v202\n"
    "v_mul_f32 v21, v21, v201\n"
    "v_cvt_pk_bf16_f32 v44, v20, v21\n"
    "v_mfma_f32_32x32x16_bf16 v[160:175], a[228:231], a[112:115], v[160:175]\n"
    "ds_read_b64_tr_b16 a[224:225], v18 offset:40960\n"
    "ds_read_b64_tr_b16 a[226:227], v18 offset:41984\n"
    "v_exp_f32 v22, v22\n"
    "v_fmaak_f32 v20, v203, v203, 0x409d30fc\n"
    "v_add_f32 v22, 1.0, v22\n"
    "v_mfma_f32_32x32x16_bf16 v[144:159], a[232:235], a[116:119], v[144:159]\n"
    "ds_read_b64_tr_b16 a[228:229], v19 offset:40960\n"
    "ds_read_b64_tr_b16 a[230:231], v19 offset:41984\n"
    "s_add_u32 m0, %[ldsw], 90112\n"
    "s_mov_b32 s40, 4129024\n"
    "v_rcp_f32 v22, v22\n"
    "v_mul_f32 v20, v20, v203\n"
    "v_mul_f32 v22, v22, v202\n"
    "v_exp_f32 v20, v20\n"
    "v_mfma_f32_32x32x16_bf16 v[160:175], a[236:239], a[116:119], v[160:175]\n"
    "s_waitcnt lgkmcnt(4)\n"
    "ds_read_b64_tr_b16 a[232:233], v18 offset:43008\n"
    "ds_read_b64_tr_b16 a[234:235], v18 offset:44032\n"
    "buffer_load_dwordx4 %[w1a], %[srd1], s40 offen lds\n"
    "v_fmaak_f32 v21, v204, v204, 0x409d30fc\n"
    "v_add_f32 v20, 1.0, v20\n"
    "v_rcp_f32 v20, v20\n"
    "v_mfma_f32_32x32x16_bf16 v[144:159], a[240:243], a[120:123], v[144:159]\n"
    "ds_read_b64_tr_b16 a[236:237], v19 offset:43008\n"
    "ds_read_b64_tr_b16 a[238:239], v19 offset:44032\n"
    "v_mul_f32 v21, v21, v204\n"
    "v_mul_f32 v20, v20, v203\n"
    "v_cvt_pk_bf16_f32 v45, v22, v20\n"
    "v_mfma_f32_32x32x16_bf16 v[160:175], a[244:247], a[120:123], v[160:175]\n"
    "ds_read_b64_tr_b16 a[240:241], v18 offset:45056\n"
    "ds_read_b64_tr_b16 a[242:243], v18 offset:46080\n"
    "v_exp_f32 v21, v21\n"
    "v_fmaak_f32 v22, v205, v205, 0x409d30fc\n"
    "v_add_f32 v21, 1.0, v21\n"
    "v_rcp_f32 v21, v21\n"
    "v_mfma_f32_32x32x16_bf16 v[144:159], a[248:251], a[124:127], v[144:159]\n"
    "ds_read_b64_tr_b16 a[244:245], v19 offset:45056\n"
    "ds_read_b64_tr_b16 a[246:247], v19 offset:46080\n"
    "v_mul_f32 v22, v22, v205\n"
    "v_mul_f32 v21, v21, v204\n"
    "v_exp_f32 v22, v22\n"
    "v_mfma_f32_32x32x16_bf16 v[160:175], a[252:255], a[124:127], v[160:175]\n"
    "s_waitcnt lgkmcnt(4)\n"
    "ds_read_b64_tr_b16 a[248:249], v18 offset:47104\n"
    "ds_read_b64_tr_b16 a[250:251], v18 offset:48128\n"
    "buffer_load_dwordx4 %[w1b], %[srd1], s40 offen offset:1024 lds\n"
    "v_fmaak_f32 v20, v206, v206, 0x409d30fc\n"
    "v_add_f32 v22, 1.0, v22\n"
    "v_rcp_f32 v22, v22\n"
    "v_mfma_f32_32x32x16_bf16 v[144:159], a[224:227], a[128:131], v[144:159]\n"
    "ds_read_b64_tr_b16 a[252:253], v19 offset:47104\n"
    "ds_read_b64_tr_b16 a[254:255], v19 offset:48128\n"
    "v_mul_f32 v20, v20, v206\n"
    "v_mul_f32 v22, v22, v205\n"
    "v_cvt_pk_bf16_f32 v46, v21, v22\n"
    "v_mfma_f32_32x32x16_bf16 v[160:175], a[228:231], a[128:131], v[160:175]\n"
    "s_waitcnt vmcnt(18)\n"
    "s_barrier\n"
    "ds_read_b64_tr_b16 a[224:225], %[rd0] offset:0\n"
    "ds_read_b64_tr_b16 a[226:227], %[rd0] offset:1024\n"
    "v_mfma_f32_32x32x16_bf16 v[144:159], a[232:235], a[132:135], v[144:159]\n"
    "ds_read_b64_tr_b16 a[228:229], %[rd1] offset:0\n"
    "ds_read_b64_tr_b16 a[230:231], %[rd1] offset:1024\n"
    "s_add_u32 m0, %[ldsw], 98304\n"
    "s_mov_b32 s40, 4718848\n"
    "v_exp_f32 v20, v20\n"
    "v_fmaak_f32 v21, v207, v207, 0x409d30fc\n"
    "v_add_f32 v20, 1.0, v20\n"
    "v_rcp_f32 v20, v20\n"
    "v_mfma_f32_32x32x16_bf16 v[160:175], a[236:239], a[132:135], v[160:175]\n"
    "s_waitcnt lgkmcnt(4)\n"
    "ds_read_b64_tr_b16 a[232:233], %[rd0] offset:2048\n"
    "ds_read_b64_tr_b16 a[234:235], %[rd0] offset:3072\n"
    "buffer_load_dwordx4 %[w1a], %[srd1], s40 offen lds\n"
    "v_mul_f32 v21, v21, v207\n"
    "v_mul_f32 v20, v20, v206\n"
    "v_exp_f32 v21, v21\n"
    "v_mfma_f32_32x32x16_bf16 v[144:159], a[240:243], a[136:139], v[144:159]\n"
    "ds_read_b64_tr_b16 a[236:237], %[rd1] offset:2048\n"
    "ds_read_b64_tr_b16 a[238:239], %[rd1] offset:3072\n"
    "s_nop 0\n"
    "v_add_f32 v21, 1.0, v21\n"
    "v_rcp_f32 v21, v21\n"
    "v_mfma_f32_32x32x16_bf16 v[160:175], a[244:247], a[136:139], v[160:175]\n"
    "ds_read_b64_tr_b16 a[240:241], %[rd0] offset:4096\n"
    "ds_read_b64_tr_b16 a[242:243], %[rd0] offset:5120\n"
    "s_nop 0\n"
    "v_mul_f32 v21, v21, v207\n"
    "v_cvt_pk_bf16_f32 v47, v20, v21\n"
    "v_mfma_f32_32x32x16_bf16 v[144:159], a[248:251], a[140:143], v[144:159]\n"
    "ds_read_b64_tr_b16 a[244:245], %[rd1] offset:4096\n"
    "ds_read_b64_tr_b16 a[246:247], %[rd1] offset:5120\n"
    "v_mfma_f32_32x32x16_bf16 v[160:175], a[252:255], a[140:143], v[160:175]\n"
    "s_waitcnt lgkmcnt(4)\n"
    "ds_read_b64_tr_b16 a[248:249], %[rd0] offset:6144\n"
    "ds_read_b64_tr_b16 a[250:251], %[rd0] offset:7168\n"
    "buffer_load_dwordx4 %[w1b], %[srd1], s40 offen offset:1024 lds\n"
    "v_mfma_f32_32x32x16_bf16 v[208:223], a[224:227], v[32:35], 0\n"
    "ds_read_b64_tr_b16 a[252:253], %[rd1] offset:6144\n"
    "ds_read_b64_tr_b16 a[254:255], %[rd1] offset:7168\n"
    "v_mfma_f32_32x32x16_bf16 v[224:239], a[228:231], v[32:35], 0\n"
    "ds_read_b64_tr_b16 a[224:225], %[rd0] offset:8192\n"
    "ds_read_b64_tr_b16 a[226:227], %[rd0] offset:9216\n"
    "v_mfma_f32_32x32x16_bf16 v[208:223], a[232:235], v[36:39], v[208:223]\n"
    "ds_read_b64_tr_b16 a[228:229], %[rd1] offset:8192\n"
    "ds_read_b64_tr_b16 a[230:231], %[rd1] offset:9216\n"
    "s_add_u32 m0, %[ldsw], 106496\n"
    "s_mov_b32 s40, 73728\n"
    "ds_read_b128 v[176:179], %[vb] offset:512\n"
    "v_mfma_f32_32x32x16_bf16 v[224:239], a[236:239], v[36:39], v[224:239]\n"
    "s_waitcnt lgkmcnt(5)\n"
    "ds_read_b64_tr_b16 a[232:233], %[rd0] offset:10240\n"
    "ds_read_b64_tr_b16 a[234:235], %[rd0] offset:11264\n"
    "buffer_load_dwordx4 %[w2a], %[srd2], s40 offen lds\n"
    "v_fmaak_f32 v20, v144, v144, 0x409d30fc\n"
    "v_mul_f32 v20, v20, v144\n"
    "v_exp_f32 v20, v20\n"
    "v_mfma_f32_32x32x16_bf16 v[208:223], a[240:243], v[40:43], v[208:223]\n"
    "ds_read_b64_tr_b16 a[236:237], %[rd1] offset:10240\n"
    "ds_read_b64_tr_b16 a[238:239], %[rd1] offset:11264\n"
    "v_fmaak_f32 v21, v145, v145, 0x409d30fc\n"
    "v_add_f32 v20, 1.0, v20\n"
    "v_mfma_f32_32x32x16_bf16 v[224:239], a[244:247], v[40:43], v[224:239]\n"
    "ds_read_b64_tr_b16 a[240:241], %[rd0] offset:12288\n"
    "ds_read_b64_tr_b16 a[242:243], %[rd0] offset:13312\n"
    "ds_read_b128 v[180:183], %[vb] offset:544\n"
    "v_rcp_f32 v20, v20\n"
    "v_mul_f32 v21, v21, v145\n"
    "v_mfma_f32_32x32x16_bf16 v[208:223], a[248:251], v[44:47], v[208:223]\n"
    "ds_read_b64_tr_b16 a[244:245], %[rd1] offset:12288\n"
    "ds_read_b64_tr_b16 a[246:247], %[rd1] offset:13312\n"
    "v_mul_f32 v20, v20, v144\n"
    "v_exp_f32 v21, v21\n"
    "v_mfma_f32_32x32x16_bf16 v[224:239], a[252:255], v[44:47], v[224:239]\n"
    "s_waitcnt lgkmcnt(5)\n"
    "ds_read_b64_tr_b16 a[248:249], %[rd0] offset:14336\n"
    "ds_read_b64_tr_b16 a[250:251], %[rd0] offset:15360\n"
    "buffer_load_dwordx4 %[w2b], %[srd2], s40 offen offset:1024 lds\n"
    "v_fmaak_f32 v22, v146, v146, 0x409d30fc\n"
    "v_add_f32 v21, 1.0, v21\n"
    "v_rcp_f32 v21, v21\n"
    "v_mfma_f32_32x32x16_bf16 v[240:255], a[224:227], v[32:35], 0\n"
    "ds_read_b64_tr_b16 a[252:253], %[rd1] offset:14336\n"
    "ds_read_b64_tr_b16 a[254:255], %[rd1] offset:15360\n"
    "ds_read_b128 v[184:187], %[vb] offset:576\n"
    "v_mul_f32 v22, v22, v146\n"
    "v_mul_f32 v21, v21, v145\n"
    "v_mfma_f32_32x32x16_bf16 a[144:159], a[228:231], v[32:35], 0\n"
    "ds_read_b64_tr_b16 a[224:225], %[rd0] offset:16384\n"
    "ds_read_b64_tr_b16 a[226:227], %[rd0] offset:17408\n"
    "v_cvt_pk_bf16_f32 v48, v20, v21\n"
    "v_exp_f32 v22, v22\n"
    "v_mfma_f32_32x32x16_bf16 v[240:255], a[232:235], v[36:39], v[240:255]\n"
    "ds_read_b64_tr_b16 a[228:229], %[rd1] offset:16384\n"
    "ds_read_b64_tr_b16 a[230:231], %[rd1] offset:17408\n"
    "s_add_u32 m0, %[ldsw], 114688\n"
    "s_mov_b32 s40, 73856\n"
    "v_fmaak_f32 v20, v147, v147, 0x409d30fc\n"
    "v_add_f32 v22, 1.0, v22\n"
    "v_mfma_f32_32x32x16_bf16 a[144:159], a[236:239], v[36:39], a[144:159]\n"
    "s_waitcnt lgkmcnt(5)\n"
    "ds_read_b64_tr_b16 a[232:233], %[rd0] offset:18432\n"
    "ds_read_b64_tr_b16 a[234:235], %[rd0] offset:19456\n"
    "buffer_load_dwordx4 %[w2a], %[srd2], s40 offen lds\n"
    "ds_read_b128 v[188:191], %[vb] offset:608\n"
    "v_rcp_f32 v22, v22\n"
    "v_mul_f32 v20, v20, v147\n"
    "v_mfma_f32_32x32x16_bf16 v[240:255], a[240:243], v[40:43], v[240:255]\n"
    "ds_read_b64_tr_b16 a[236:237], %[rd1] offset:18432\n"
    "ds_read_b64_tr_b16 a[238:239], %[rd1] offset:19456\n"
    "v_mul_f32 v22, v22, v146\n"
    "v_exp_f32 v20, v20\n"
    "v_fmaak_f32 v21, v148, v148, 0x409d30fc\n"
    "v_mfma_f32_32x32x16_bf16 a[144:159], a[244:247], v[40:43], a[144:159]\n"
    "ds_read_b64_tr_b16 a[240:241], %[rd0] offset:20480\n"
    "ds_read_b64_tr_b16 a[242:243], %[rd0] offset:21504\n"
    "v_add_f32 v20, 1.0, v20\n"
    "v_rcp_f32 v20, v20\n"
    "v_mfma_f32_32x32x16_bf16 v[240:255], a[248:251], v[44:47], v[240:255]\n"
    "ds_read_b64_tr_b16 a[244:245], %[rd1] offset:20480\n"
    "ds_read_b64_tr_b16 a[246:247], %[rd1] offset:21504\n"
    "ds_read_b128 v[192:195], %[vb] offset:640\n"
    "v_mul_f32 v21, v21, v148\n"
    "v_mul_f32 v20, v20, v147\n"
    "v_mfma_f32_32x32x16_bf16 a[144:159], a[252:255], v[44:47], a[144:159]\n"
    "s_waitcnt lgkmcnt(5)\n"
    "ds_read_b64_tr_b16 a[248:249], %[rd0] offset:22528\n"
    "ds_read_b64_tr_b16 a[250:251], %[rd0] offset:23552\n"
    "buffer_load_dwordx4 %[w2b], %[srd2], s40 offen offset:1024 lds\n"
    "v_cvt_pk_bf16_f32 v49, v22, v20\n"
    "v_exp_f32 v21, v21\n"
    "v_mfma_f32_32x32x16_bf16 a[160:175], a[224:227], v[32:35], 0\n"
    "ds_read_b64_tr_b16 a[252:253], %[rd1] offset:22528\n"
    "ds_read_b64_tr_b16 a[254:255], %[rd1] offset:23552\n"
    "v_fmaak_f32 v22, v149, v149, 0x409d30fc\n"
    "v_add_f32 v21, 1.0, v21\n"
    "v_mfma_f32_32x32x16_bf16 a[176:191], a[228:231], v[32:35], 0\n"
    "s_waitcnt vmcnt(18)\n"
    "s_barrier\n"
    "ds_read_b64_tr_b16 a[224:225], %[rd0] offset:24576\n"
    "ds_read_b64_tr_b16 a[226:227], %[rd0] offset:25600\n"
    "v_mfma_f32_32x32x16_bf16 a[160:175], a[232:235], v[36:39], a[160:175]\n"
    "ds_read_b64_tr_b16 a[228:229], %[rd1] offset:24576\n"
    "ds_read_b64_tr_b16 a[230:231], %[rd1] offset:25600\n"
    "s_add_u32 m0, %[ldsw], 122880\n"
    "s_mov_b32 s40, 73984\n"
    "ds_read_b128 v[196:199], %[vb] offset:672\n"
    "v_rcp_f32 v21, v21\n"
    "v_mul_f32 v22, v22, v149\n"
    "v_mul_f32 v21, v21, v148\n"
    "v_mfma_f32_32x32x16_bf16 a[176:191], a[236:239], v[36:39], a[176:191]\n"
    "s_waitcnt lgkmcnt(5)\n"
    "ds_read_b64_tr_b16 a[232:233], %[rd0] offset:26624\n"
    "ds_read_b64_tr_b16 a[234:235], %[rd0] offset:27648\n"
    "buffer_load_dwordx4 %[w2a], %[srd2], s40 offen lds\n"
    "v_exp_f32 v22, v22\n"
    "v_fmaak_f32 v20, v150, v150, 0x409d30fc\n"
    "v_mfma_f32_32x32x16_bf16 a[160:175], a[240:243], v[40:43], a[160:175]\n"
    "ds_read_b64_tr_b16 a[236:237], %[rd1] offset:26624\n"
    "ds_read_b64_tr_b16 a[238:239], %[rd1] offset:27648\n"
    "v_add_f32 v22, 1.0, v22\n"
    "v_rcp_f32 v22, v22\n"
    "v_mfma_f32_32x32x16_bf16 a[176:191], a[244:247], v[40:43], a[176:191]\n"
    "ds_read_b64_tr_b16 a[240:241], %[rd0] offset:28672\n"
    "ds_read_b64_tr_b16 a[242:243], %[rd0] offset:29696\n"
    "ds_read_b128 v[200:203], %[vb] offset:704\n"
    "v_mul_f32 v20, v20, v150\n"
    "v_mul_f32 v22, v22, v149\n"
    "v_mfma_f32_32x32x16_bf16 a[160:175], a[248:251], v[44:47], a[160:175]\n"
    "ds_read_b64_tr_b16 a[244:245], %[rd1] offset:28672\n"
    "ds_read_b64_tr_b16 a[246:247], %[rd1] offset:29696\n"
    "v_cvt_pk_bf16_f32 v50, v21, v22\n"
    "v_exp_f32 v20, v20\n"
    "v_mfma_f32_32x32x16_bf16 a[176:191], a[252:255], v[44:47], a[176:191]\n"
    "s_waitcnt lgkmcnt(5)\n"
    "ds_read_b64_tr_b16 a[248:249], %[rd0] offset:30720\n"
    "ds_read_b64_tr_b16 a[250:251], %[rd0] offset:31744\n"
    "buffer_load_dwordx4 %[w2b], %[srd2], s40 offen offset:1024 lds\n"
    "v_fmaak_f32 v21, v151, v151, 0x409d30fc\n"
    "v_add_f32 v20, 1.0, v20\n"
    "v_rcp_f32 v20, v20\n"
    "v_mfma_f32_32x32x16_bf16 a[192:207], a[224:227], v[32:35], 0\n"
    "ds_read_b64_tr_b16 a[252:253], %[rd1] offset:30720\n"
    "ds_read_b64_tr_b16 a[254:255], %[rd1] offset:31744\n"
    "ds_read_b128 v[204:207], %[vb] offset:736\n"
    "v_mul_f32 v21, v21, v151\n"
    "v_mul_f32 v20, v20, v150\n"
    "v_mfma_f32_32x32x16_bf16 a[208:223], a[228:231], v[32:35], 0\n"
    "ds_read_b64_tr_b16 a[224:225], %[rd0] offset:32768\n"
    "ds_read_b64_tr_b16 a[226:227], %[rd0] offset:33792\n"
    "v_exp_f32 v21, v21\n"
    "v_fmaak_f32 v22, v152, v152, 0x409d30fc\n"
    "v_mfma_f32_32x32x16_bf16 a[192:207], a[232:235], v[36:39], a[192:207]\n"
    "ds_read_b64_tr_b16 a[228:229], %[rd1] offset:32768\n"
    "ds_read_b64_tr_b16 a[230:231], %[rd1] offset:33792\n"
    "s_add_u32 m0, %[ldsw], 131072\n"
    "s_mov_b32 s40, 74112\n"
    "v_add_f32 v21, 1.0, v21\n"
    "v_rcp_f32 v21, v21\n"
    "v_mfma_f32_32x32x16_bf16 a[208:223], a[236:239], v[36:39], a[208:223]\n"
    "s_waitcnt lgkmcnt(5)\n"
    "ds_read_b64_tr_b16 a[232:233], %[rd0] offset:34816\n"
    "ds_read_b64_tr_b16 a[234:235], %[rd0] offset:35840\n"
    "buffer_load_dwordx4 %[w2a], %[srd2], s40 offen lds\n"
    "v_mul_f32 v22, v22, v152\n"
    "v_mul_f32 v21, v21, v151\n"
    "v_cvt_pk_bf16_f32 v51, v20, v21\n"
    "v_mfma_f32_32x32x16_bf16 a[192:207], a[240:243], v[40:43], a[192:207]\n"
    "ds_read_b64_tr_b16 a[236:237], %[rd1] offset:34816\n"
    "ds_read_b64_tr_b16 a[238:239], %[rd1] offset:35840\n"
    "v_exp_f32 v22, v22\n"
    "v_fmaak_f32 v20, v153, v153, 0x409d30fc\n"
    "v_mfma_f32_32x32x16_bf16 a[208:223], a[244:247], v[40:43], a[208:223]\n"
    "ds_read_b64_tr_b16 a[240:241], %[rd0] offset:36864\n"
    "ds_read_b64_tr_b16 a[242:243], %[rd0] offset:37888\n"
    "v_add_f32 v22, 1.0, v22\n"
    "v_rcp_f32 v22, v22\n"
    "v_mfma_f32_32x32x16_bf16 a[192:207], a[248:251], v[44:47], a[192:207]\n"
    "ds_read_b64_tr_b16 a[244:245], %[rd1] offset:36864\n"
    "ds_read_b64_tr_b16 a[246:247], %[rd1] offset:37888\n"
    "v_mul_f32 v20, v20, v153\n"
    "v_mul_f32 v22, v22, v152\n"
    "v_mfma_f32_32x32x16_bf16 a[208:223], a[252:255], v[44:47], a[208:223]\n"
    "s_waitcnt lgkmcnt(4)\n"
    "ds_read_b64_tr_b16 a[248:249], %[rd0] offset:38912\n"
    "ds_read_b64_tr_b16 a[250:251], %[rd0] offset:39936\n"
    "buffer_load_dwordx4 %[w2b], %[srd2], s40 offen offset:1024 lds\n"
    "v_exp_f32 v20, v20\n"
    "v_fmaak_f32 v21, v154, v154, 0x409d30fc\n"
    "v_mfma_f32_32x32x16_bf16 v[176:191], a[224:227], a[0:3], v[176:191]\n"
    "ds_read_b64_tr_b16 a[252:253], %[rd1] offset:38912\n"
    "ds_read_b64_tr_b16 a[254:255], %[rd1] offset:39936\n"
    "v_add_f32 v20, 1.0, v20\n"
    "v_rcp_f32 v20, v20\n"
    "v_mul_f32 v21, v21, v154\n"
    "v_mfma_f32_32x32x16_bf16 v[192:207], a[228:231], a[0:3], v[192:207]\n"
    "ds_read_b64_tr_b16 a[224:225], %[rd0] offset:40960\n"
    "ds_read_b64_tr_b16 a[226:227], %[rd0] offset:41984\n"
    "v_mul_f32 v20, v20, v153\n"
    "v_cvt_pk_bf16_f32 v52, v22, v20\n"
    "v_mfma_f32_32x32x16_bf16 v[176:191], a[232:235], a[4:7], v[176:191]\n"
    "ds_read_b64_tr_b16 a[228:229], %[rd1] offset:40960\n"
    "ds_read_b64_tr_b16 a[230:231], %[rd1] offset:41984\n"
    "s_add_u32 m0, %[ldsw], 139264\n"
    "s_mov_b32 s40, 384\n"
    "v_exp_f32 v21, v21\n"
    "v_fmaak_f32 v22, v155, v155, 0x409d30fc\n"
    "v_mfma_f32_32x32x16_bf16 v[192:207], a[236:239], a[4:7], v[192:207]\n"
    "s_waitcnt lgkmcnt(4)\n"
    "ds_read_b64_tr_b16 a[232:233], %[rd0] offset:43008\n"
    "ds_read_b64_tr_b16 a[234:235], %[rd0] offset:44032\n"
    "buffer_load_dwordx4 %[w1a], %[srd1], s40 offen lds\n"
    "v_add_f32 v21, 1.0, v21\n"
    "v_rcp_f32 v21, v21\n"
    "v_mfma_f32_32x32x16_bf16 v[176:191], a[240:243], a[8:11], v[176:191]\n"
    "ds_read_b64_tr_b16 a[236:237], %[rd1] offset:43008\n"
    "ds_read_b64_tr_b16 a[238:239], %[rd1] offset:44032\n"
    "v_mul_f32 v22, v22, v155\n"
    "v_mul_f32 v21, v21, v154\n"
    "v_mfma_f32_32x32x16_bf16 v[192:207], a[244:247], a[8:11], v[192:207]\n"
    "ds_read_b64_tr_b16 a[240:241], %[rd0] offset:45056\n"
    "ds_read_b64_tr_b16 a[242:243], %[rd0] offset:46080\n"
    "v_exp_f32 v22, v22\n"
    "v_fmaak_f32 v20, v156, v156, 0x409d30fc\n"
    "v_add_f32 v22, 1.0, v22\n"
    "v_mfma_f32_32x32x16_bf16 v[176:191], a[248:251], a[12:15], v[176:191]\n"
    "ds_read_b64_tr_b16 a[244:245], %[rd1] offset:45056\n"
    "ds_read_b64_tr_b16 a[246:247], %[rd1] offset:46080\n"
    "v_rcp_f32 v22, v22\n"
    "v_mul_f32 v20, v20, v156\n"
    "v_mfma_f32_32x32x16_bf16 v[192:207], a[252:255], a[12:15], v[192:207]\n"
    "s_waitcnt lgkmcnt(4)\n"
    "ds_read_b64_tr_b16 a[248:249], %[rd0] offset:47104\n"
    "ds_read_b64_tr_b16 a[250:251], %[rd0] offset:48128\n"
    "buffer_load_dwordx4 %[w1b], %[srd1], s40 offen offset:1024 lds\n"
    "v_mul_f32 v22, v22, v155\n"
    "v_cvt_pk_bf16_f32 v53, v21, v22\n"
    "v_mfma_f32_32x32x16_bf16 v[176:191], a[224:227], a[16:19], v[176:191]\n"
    "ds_read_b64_tr_b16 a[252:253], %[rd1] offset:47104\n"
    "ds_read_b64_tr_b16 a[254:255], %[rd1] offset:48128\n"
    "v_exp_f32 v20, v20\n"
    "v_fmaak_f32 v21, v157, v157, 0x409d30fc\n"
    "v_mfma_f32_32x32x16_bf16 v[192:207], a[228:231], a[16:19], v[192:207]\n"
    "s_waitcnt vmcnt(18)\n"
    "s_barrier\n"
    "ds_read_b64_tr_b16 a[224:225], v16 offset:0\n"
    "ds_read_b64_tr_b16 a[226:227], v16 offset:1024\n"
    "v_mfma_f32_32x32x16_bf16 v[176:191], a[232:235], a[20:23], v[176:191]\n"
    "ds_read_b64_tr_b16 a[228:229], v17 offset:0\n"
    "ds_read_b64_tr_b16 a[230:231], v17 offset:1024\n"
    "s_add_u32 m0, %[ldsw], 0\n"
    "s_mov_b32 s40, 590208\n"
    "v_add_f32 v20, 1.0, v20\n"
    "v_rcp_f32 v20, v20\n"
    "v_mfma_f32_32x32x16_bf16 v[192:207], a[236:239], a[20:23], v[192:207]\n"
    "s_waitcnt lgkmcnt(4)\n"
    "ds_read_b64_tr_b16 a[232:233], v16 offset:2048\n"
    "ds_read_b64_tr_b16 a[234:235], v16 offset:3072\n"
    "buffer_load_dwordx4 %[w1a], %[srd1], s40 offen lds\n"
    "v_mul_f32 v21, v21, v157\n"
    "v_mul_f32 v20, v20, v156\n"
    "v_exp_f32 v21, v21\n"
    "v_mfma_f32_32x32x16_bf16 v[176:191], a[240:243], a[24:27], v[176:191]\n"
    "ds_read_b64_tr_b16 a[236:237], v17 offset:2048\n"
    "ds_read_b64_tr_b16 a[238:239], v17 offset:3072\n"
    "v_fmaak_f32 v22, v158, v158, 0x409d30fc\n"
    "v_add_f32 v21, 1.0, v21\n"
    "v_mfma_f32_32x32x16_bf16 v[192:207], a[244:247], a[24:27], v[192:207]\n"
    "ds_read_b64_tr_b16 a[240:241], v16 offset:4096\n"
    "ds_read_b64_tr_b16 a[242:243], v16 offset:5120\n"
    "v_rcp_f32 v21, v21\n"
    "v_mul_f32 v22, v22, v158\n"
    "v_mfma_f32_32x32x16_bf16 v[176:191], a[248:251], a[28:31], v[176:191]\n"
    "ds_read_b64_tr_b16 a[244:245], v17 offset:4096\n"
    "ds_read_b64_tr_b16 a[246:247], v17 offset:5120\n"
    "v_mul_f32 v21, v21, v157\n"
    "v_cvt_pk_bf16_f32 v54, v20, v21\n"
    "v_mfma_f32_32x32x16_bf16 v[192:207], a[252:255], a[28:31], v[192:207]\n"
    "s_waitcnt lgkmcnt(4)\n"
    "ds_read_b64_tr_b16 a[248:249], v16 offset:6144\n"
    "ds_read_b64_tr_b16 a[250:251], v16 offset:7168\n"
    "buffer_load_dwordx4 %[w1b], %[srd1], s40 offen offset:1024 lds\n"
    "v_exp_f32 v22, v22\n"
    "v_fmaak_f32 v20, v159, v159, 0x409d30fc\n"
    "v_add_f32 v22, 1.0, v22\n"
    "v_mfma_f32_32x32x16_bf16 v[176:191], a[224:227], a[32:35], v[176:191]\n"
    "ds_read_b64_tr_b16 a[252:253], v17 offset:6144\n"
    "ds_read_b64_tr_b16 a[254:255], v17 offset:7168\n"
    "v_rcp_f32 v22, v22\n"
    "v_mul_f32 v20, v20, v159\n"
    "v_mfma_f32_32x32x16_bf16 v[192:207], a[228:231], a[32:35], v[192:207]\n"
    "ds_read_b64_tr_b16 a[224:225], v16 offset:8192\n"
    "ds_read_b64_tr_b16 a[226:227], v16 offset:9216\n"
    "v_mul_f32 v22, v22, v158\n"
    "v_exp_f32 v20, v20\n"
    "v_mfma_f32_32x32x16_bf16 v[176:191], a[232:235], a[36:39], v[176:191]\n"
    "ds_read_b64_tr_b16 a[228:229], v17 offset:8192\n"
    "ds_read_b64_tr_b16 a[230:231], v17 offset:9216\n"
    "s_add_u32 m0, %[ldsw], 8192\n"
    "s_mov_b32 s40, 1180032\n"
    "v_fmaak_f32 v21, v160, v160, 0x409d30fc\n"
    "v_add_f32 v20, 1.0, v20\n"
    "v_mfma_f32_32x32x16_bf16 v[192:207], a[236:239], a[36:39], v[192:207]\n"
    "s_waitcnt lgkmcnt(4)\n"
    "ds_read_b64_tr_b16 a[232:233], v16 offset:10240\n"
    "ds_read_b64_tr_b16 a[234:235], v16 offset:11264\n"
    "buffer_load_dwordx4 %[w1a], %[srd1], s40 offen lds\n"
    "v_rcp_f32 v20, v20\n"
    "v_mul_f32 v21, v21, v160\n"
    "v_mfma_f32_32x32x16_bf16 v[176:191], a[240:243], a[40:43], v[176:191]\n"
    "ds_read_b64_tr_b16 a[236:237], v17 offset:10240\n"
    "ds_read_b64_tr_b16 a[238:239], v17 offset:11264\n"
    "v_mul_f32 v20, v20, v159\n"
    "v_cvt_pk_bf16_f32 v55, v22, v20\n"
    "v_exp_f32 v21, v21\n"
    "v_mfma_f32_32x32x16_bf16 v[192:207], a[244:247], a[40:43], v[192:207]\n"
    "ds_read_b64_tr_b16 a[240:241], v16 offset:12288\n"
    "ds_read_b64_tr_b16 a[242:243], v16 offset:13312\n"
    "v_fmaak_f32 v22, v161, v161, 0x409d30fc\n"
    "v_add_f32 v21, 1.0, v21\n"
    "v_mfma_f32_32x32x16_bf16 v[176:191], a[248:251], a[44:47], v[176:191]\n"
    "ds_read_b64_tr_b16 a[244:245], v17 offset:12288\n"
    "ds_read_b64_tr_b16 a[246:247], v17 offset:13312\n"
    "v_rcp_f32 v21, v21\n"
    "v_mul_f32 v22, v22, v161\n"
    "v_mfma_f32_32x32x16_bf16 v[192:207], a[252:255], a[44:47], v[192:207]\n"
    "s_waitcnt lgkmcnt(4)\n"
    "ds_read_b64_tr_b16 a[248:249], v16 offset:14336\n"
    "ds_read_b64_tr_b16 a[250:251], v16 offset:15360\n"
    "buffer_load_dwordx4 %[w1b], %[srd1], s40 offen offset:1024 lds\n"
    "v_mul_f32 v21, v21, v160\n"
    "v_exp_f32 v22, v22\n"
    "v_mfma_f32_32x32x16_bf16 v[176:191], a[224:227], a[48:51], v[176:191]\n"
    "ds_read_b64_tr_b16 a[252:253], v17 offset:14336\n"
    "ds_read_b64_tr_b16 a[254:255], v17 offset:15360\n"
    "v_fmaak_f32 v20, v162, v162, 0x409d30fc\n"
    "v_add_f32 v22, 1.0, v22\n"
    "v_mfma_f32_32x32x16_bf16 v[192:207], a[228:231], a[48:51], v[192:207]\n"
    "ds_read_b64_tr_b16 a[224:225], v16 offset:16384\n"
    "ds_read_b64_tr_b16 a[226:227], v16 offset:17408\n"
    "v_rcp_f32 v22, v22\n"
    "v_mul_f32 v20, v20, v162\n"
    "v_mul_f32 v22, v22, v161\n"
    "v_mfma_f32_32x32x16_bf16 v[176:191], a[232:235], a[52:55], v[176:191]\n"
    "ds_read_b64_tr_b16 a[228:229], v17 offset:16384\n"
    "ds_read_b64_tr_b16 a[230:231], v17 offset:17408\n"
    "s_add_u32 m0, %[ldsw], 16384\n"
    "s_mov_b32 s40, 1769856\n"
    "v_cvt_pk_bf16_f32 v56, v21, v22\n"
    "v_exp_f32 v20, v20\n"
    "v_mfma_f32_32x32x16_bf16 v[192:207], a[236:239], a[52:55], v[192:207]\n"
    "s_waitcnt lgkmcnt(4)\n"
    "ds_read_b64_tr_b16 a[232:233], v16 offset:18432\n"
    "ds_read_b64_tr_b16 a[234:235], v16 offset:19456\n"
    "buffer_load_dwordx4 %[w1a], %[srd1], s40 offen lds\n"
    "v_fmaak_f32 v21, v163, v163, 0x409d30fc\n"
    "v_add_f32 v20, 1.0, v20\n"
    "v_mfma_f32_32x32x16_bf16 v[176:191], a[240:243], a[56:59], v[176:191]\n"
    "ds_read_b64_tr_b16 a[236:237], v17 offset:18432\n"
    "ds_read_b64_tr_b16 a[238:239], v17 offset:19456\n"
    "v_rcp_f32 v20, v20\n"
    "v_mul_f32 v21, v21, v163\n"
    "v_mfma_f32_32x32x16_bf16 v[192:207], a[244:247], a[56:59], v[192:207]\n"
    "ds_read_b64_tr_b16 a[240:241], v16 offset:20480\n"
    "ds_read_b64_tr_b16 a[242:243], v16 offset:21504\n"
    "v_mul_f32 v20, v20, v162\n"
    "v_exp_f32 v21, v21\n"
    "v_mfma_f32_32x32x16_bf16 v[176:191], a[248:251], a[60:63], v[176:191]\n"
    "ds_read_b64_tr_b16 a[244:245], v17 offset:20480\n"
    "ds_read_b64_tr_b16 a[246:247], v17 offset:21504\n"
    "v_fmaak_f32 v22, v164, v164, 0x409d30fc\n"
    "v_add_f32 v21, 1.0, v21\n"
    "v_rcp_f32 v21, v21\n"
    "v_mfma_f32_32x32x16_bf16 v[192:207], a[252:255], a[60:63], v[192:207]\n"
    "s_waitcnt lgkmcnt(4)\n"
    "ds_read_b64_tr_b16 a[248:249], v16 offset:22528\n"
    "ds_read_b64_tr_b16 a[250:251], v16 offset:23552\n"
    "buffer_load_dwordx4 %[w1b], %[srd1], s40 offen offset:1024 lds\n"
    "v_mul_f32 v22, v22, v164\n"
    "v_mul_f32 v21, v21, v163\n"
    "v_mfma_f32_32x32x16_bf16 v[176:191], a[224:227], a[64:67], v[176:191]\n"
    "ds_read_b64_tr_b16 a[252:253], v17 offset:22528\n"
    "ds_read_b64_tr_b16 a[254:255], v17 offset:23552\n"
    "v_cvt_pk_bf16_f32 v57, v20, v21\n"
    "v_exp_f32 v22, v22\n"
    "v_mfma_f32_32x32x16_bf16 v[192:207], a[228:231], a[64:67], v[192:207]\n"
    "s_waitcnt vmcnt(18)\n"
    "s_barrier\n"
    "ds_read_b64_tr_b16 a[224:225], v16 offset:24576\n"
    "ds_read_b64_tr_b16 a[226:227], v16 offset:25600\n"
    "v_mfma_f32_32x32x16_bf16 v[176:191], a[232:235], a[68:71], v[176:191]\n"
    "ds_read_b64_tr_b16 a[228:229], v17 offset:24576\n"
    "ds_read_b64_tr_b16 a[230:231], v17 offset:25600\n"
    "s_add_u32 m0, %[ldsw], 24576\n"
    "s_mov_b32 s40, 2359680\n"
    "v_fmaak_f32 v20, v165, v165, 0x409d30fc\n"
    "v_add_f32 v22, 1.0, v22\n"
    "v_mfma_f32_32x32x16_bf16 v[192:207], a[236:239], a[68:71], v[192:207]\n"
    "s_waitcnt lgkmcnt(4)\n"
    "ds_read_b64_tr_b16 a[232:233], v16 offset:26624\n"
    "ds_read_b64_tr_b16 a[234:235], v16 offset:27648\n"
    "buffer_load_dwordx4 %[w1a], %[srd1], s40 offen lds\n"
    "v_rcp_f32 v22, v22\n"
    "v_mul_f32 v20, v20, v165\n"
    "v_mul_f32 v22, v22, v164\n"
    "v_mfma_f32_32x32x16_bf16 v[176:191], a[240:243], a[72:75], v[176:191]\n"
    "ds_read_b64_tr_b16 a[236:237], v17 offset:26624\n"
    "ds_read_b64_tr_b16 a[238:239], v17 offset:27648\n"
    "v_exp_f32 v20, v20\n"
    "v_fmaak_f32 v21, v166, v166, 0x409d30fc\n"
    "v_mfma_f32_32x32x16_bf16 v[192:207], a[244:247], a[72:75], v[192:207]\n"
    "ds_read_b64_tr_b16 a[240:241], v16 offset:28672\n"
    "ds_read_b64_tr_b16 a[242:243], v16 offset:29696\n"
    "v_add_f32 v20, 1.0, v20\n"
    "v_rcp_f32 v20, v20\n"
    "v_mfma_f32_32x32x16_bf16 v[176:191], a[248:251], a[76:79], v[176:191]\n"
    "ds_read_b64_tr_b16 a[244:245], v17 offset:28672\n"
    "ds_read_b64_tr_b16 a[246:247], v17 offset:29696\n"
    "v_mul_f32 v21, v21, v166\n"
    "v_mul_f32 v20, v20, v165\n"
    "v_mfma_f32_32x32x16_bf16 v[192:207], a[252:255], a[76:79], v[192:207]\n"
    "s_waitcnt lgkmcnt(4)\n"
    "ds_read_b64_tr_b16 a[248:249], v16 offset:30720\n"
    "ds_read_b64_tr_b16 a[250:251], v16 offset:31744\n"
    "buffer_load_dwordx4 %[w1b], %[srd1], s40 offen offset:1024 lds\n"
    "v_cvt_pk_bf16_f32 v58, v22, v20\n"
    "v_exp_f32 v21, v21\n"
    "v_mfma_f32_32x32x16_bf16 v[176:191], a[224:227], a[80:83], v[176:191]\n"
    "ds_read_b64_tr_b16 a[252:253], v17 offset:30720\n"
    "ds_read_b64_tr_b16 a[254:255], v17 offset:31744\n"
    "v_fmaak_f32 v22, v167, v167, 0x409d30fc\n"
    "v_add_f32 v21, 1.0, v21\n"
    "v_rcp_f32 v21, v21\n"
    "v_mfma_f32_32x32x16_bf16 v[192:207], a[228:231], a[80:83], v[192:207]\n"
    "ds_read_b64_tr_b16 a[224:225], v16 offset:32768\n"
    "ds_read_b64_tr_b16 a[226:227], v16 offset:33792\n"
    "v_mul_f32 v22, v22, v167\n"
    "v_mul_f32 v21, v21, v166\n"
    "v_mfma_f32_32x32x16_bf16 v[176:191], a[232:235], a[84:87], v[176:191]\n"
    "ds_read_b64_tr_b16 a[228:229], v17 offset:32768\n"
    "ds_read_b64_tr_b16 a[230:231], v17 offset:33792\n"
    "s_add_u32 m0, %[ldsw], 32768\n"
    "s_mov_b32 s40, 2949504\n"
    "v_exp_f32 v22, v22\n"
    "v_fmaak_f32 v20, v168, v168, 0x409d30fc\n"
    "v_mfma_f32_32x32x16_bf16 v[192:207], a[236:239], a[84:87], v[192:207]\n"
    "s_waitcnt lgkmcnt(4)\n"
    "ds_read_b64_tr_b16 a[232:233], v16 offset:34816\n"
    "ds_read_b64_tr_b16 a[234:235], v16 offset:35840\n"
    "buffer_load_dwordx4 %[w1a], %[srd1], s40 offen lds\n"
    "v_add_f32 v22, 1.0, v22\n"
    "v_rcp_f32 v22, v22\n"
    "v_mfma_f32_32x32x16_bf16 v[176:191], a[240:243], a[88:91], v[176:191]\n"
    "ds_read_b64_tr_b16 a[236:237], v17 offset:34816\n"
    "ds_read_b64_tr_b16 a[238:239], v17 offset:35840\n"
    "v_mul_f32 v20, v20, v168\n"
    "v_mul_f32 v22, v22, v167\n"
    "v_mfma_f32_32x32x16_bf16 v[192:207], a[244:247], a[88:91], v[192:207]\n"
    "ds_read_b64_tr_b16 a[240:241], v16 offset:36864\n"
    "ds_read_b64_tr_b16 a[242:243], v16 offset:37888\n"
    "v_cvt_pk_bf16_f32 v59, v21, v22\n"
    "v_exp_f32 v20, v20\n"
    "v_fmaak_f32 v21, v169, v169, 0x409d30fc\n"
    "v_mfma_f32_32x32x16_bf16 v[176:191], a[248:251], a[92:95], v[176:191]\n"
    "ds_read_b64_tr_b16 a[244:245], v17 offset:36864\n"
    "ds_read_b64_tr_b16 a[246:247], v17 offset:37888\n"
    "v_add_f32 v20, 1.0, v20\n"
    "v_rcp_f32 v20, v20\n"
    "v_mfma_f32_32x32x16_bf16 v[192:207], a[252:255], a[92:95], v[192:207]\n"
    "s_waitcnt lgkmcnt(4)\n"
    "ds_read_b64_tr_b16 a[248:249], v16 offset:38912\n"
    "ds_read_b64_tr_b16 a[250:251], v16 offset:39936\n"
    "buffer_load_dwordx4 %[w1b], %[srd1], s40 offen offset:1024 lds\n"
    "v_mul_f32 v21, v21, v169\n"
    "v_mul_f32 v20, v20, v168\n"
    "v_mfma_f32_32x32x16_bf16 v[176:191], a[224:227], a[96:99], v[176:191]\n"
    "ds_read_b64_tr_b16 a[252:253], v17 offset:38912\n"
    "ds_read_b64_tr_b16 a[254:255], v17 offset:39936\n"
    "v_exp_f32 v21, v21\n"
    "v_fmaak_f32 v22, v170, v170, 0x409d30fc\n"
    "v_mfma_f32_32x32x16_bf16 v[192:207], a[228:231], a[96:99], v[192:207]\n"
    "ds_read_b64_tr_b16 a[224:225], v16 offset:40960\n"
    "ds_read_b64_tr_b16 a[226:227], v16 offset:41984\n"
    "v_add_f32 v21, 1.0, v21\n"
    "v_rcp_f32 v21, v21\n"
    "v_mfma_f32_32x32x16_bf16 v[176:191], a[232:235], a[100:103], v[176:191]\n"
    "ds_read_b64_tr_b16 a[228:229], v17 offset:40960\n"
    "ds_read_b64_tr_b16 a[230:231], v17 offset:41984\n"
    "s_add_u32 m0, %[ldsw], 40960\n"
    "s_mov_b32 s40, 3539328\n"
    "v_mul_f32 v22, v22, v170\n"
    "v_mul_f32 v21, v21, v169\n"
    "v_cvt_pk_bf16_f32 v60, v20, v21\n"
    "v_mfma_f32_32x32x16_bf16 v[192:207], a[236:239], a[100:103], v[192:207]\n"
    "s_waitcnt lgkmcnt(4)\n"
    "ds_read_b64_tr_b16 a[232:233], v16 offset:43008\n"
    "ds_read_b64_tr_b16 a[234:235], v16 offset:44032\n"
    "buffer_load_dwordx4 %[w1a], %[srd1], s40 offen lds\n"
    "v_exp_f32 v22, v22\n"
    "v_fmaak_f32 v20, v171, v171, 0x409d30fc\n"
    "v_mfma_f32_32x32x16_bf16 v[176:191], a[240:243], a[104:107], v[176:191]\n"
    "ds_read_b64_tr_b16 a[236:237], v17 offset:43008\n"
    "ds_read_b64_tr_b16 a[238:239], v17 offset:44032\n"
    "v_add_f32 v22, 1.0, v22\n"
    "v_rcp_f32 v22, v22\n"
    "v_mfma_f32_32x32x16_bf16 v[192:207], a[244:247], a[104:107], v[192:207]\n"
    "ds_read_b64_tr_b16 a[240:241], v16 offset:45056\n"
    "ds_read_b64_tr_b16 a[242:243], v16 offset:46080\n"
    "v_mul_f32 v20, v20, v171\n"
    "v_mul_f32 v22, v22, v170\n"
    "v_mfma_f32_32x32x16_bf16 v[176:191], a[248:251], a[108:111], v[176:191]\n"
    "ds_read_b64_tr_b16 a[244:245], v17 offset:45056\n"
    "ds_read_b64_tr_b16 a[246:247], v17 offset:46080\n"
    "v_exp_f32 v20, v20\n"
    "v_fmaak_f32 v21, v172, v172, 0x409d30fc\n"
    "v_add_f32 v20, 1.0, v20\n"
    "v_mfma_f32_32x32x16_bf16 v[192:207], a[252:255], a[108:111], v[192:207]\n"
    "s_waitcnt lgkmcnt(4)\n"
    "ds_read_b64_tr_b16 a[248:249], v16 offset:47104\n"
    "ds_read_b64_tr_b16 a[250:251], v16 offset:48128\n"
    "buffer_load_dwordx4 %[w1b], %[srd1], s40 offen offset:1024 lds\n"
    "v_rcp_f32 v20, v20\n"
    "v_mul_f32 v21, v21, v172\n"
    "v_mfma_f32_32x32x16_bf16 v[176:191], a[224:227], a[112:115], v[176:191]\n"
    "ds_read_b64_tr_b16 a[252:253], v17 offset:47104\n"
    "ds_read_b64_tr_b16 a[254:255], v17 offset:48128\n"
    "v_mul_f32 v20, v20, v171\n"
    "v_cvt_pk_bf16_f32 v61, v22, v20\n"
    "v_mfma_f32_32x32x16_bf16 v[192:207], a[228:231], a[112:115], v[192:207]\n"
    "s_waitcnt vmcnt(18)\n"
    "s_barrier\n"
    "ds_read_b64_tr_b16 a[224:225], v18 offset:0\n"
    "ds_read_b64_tr_b16 a[226:227], v18 offset:1024\n"
    "v_mfma_f32_32x32x16_bf16 v[176:191], a[232:235], a[116:119], v[176:191]\n"
    "ds_read_b64_tr_b16 a[228:229], v19 offset:0\n"
    "ds_read_b64_tr_b16 a[230:231], v19 offset:1024\n"
    "s_add_u32 m0, %[ldsw], 49152\n"
    "s_mov_b32 s40, 4129152\n"
    "v_exp_f32 v21, v21\n"
    "v_fmaak_f32 v22, v173, v173, 0x409d30fc\n"
    "v_mfma_f32_32x32x16_bf16 v[192:207], a[236:239], a[116:119], v[192:207]\n"
    "s_waitcnt lgkmcnt(4)\n"
    "ds_read_b64_tr_b16 a[232:233], v18 offset:2048\n"
    "ds_read_b64_tr_b16 a[234:235], v18 offset:3072\n"
    "buffer_load_dwordx4 %[w1a], %[srd1], s40 offen lds\n"
    "v_add_f32 v21, 1.0, v21\n"
    "v_rcp_f32 v21, v21\n"
    "v_mfma_f32_32x32x16_bf16 v[176:191], a[240:243], a[120:123], v[176:191]\n"
    "ds_read_b64_tr_b16 a[236:237], v19 offset:2048\n"
    "ds_read_b64_tr_b16 a[238:239], v19 offset:3072\n"
    "v_mul_f32 v22, v22, v173\n"
    "v_mul_f32 v21, v21, v172\n"
    "v_exp_f32 v22, v22\n"
    "v_mfma_f32_32x32x16_bf16 v[192:207], a[244:247], a[120:123], v[192:207]\n"
    "ds_read_b64_tr_b16 a[240:241], v18 offset:4096\n"
    "ds_read_b64_tr_b16 a[242:243], v18 offset:5120\n"
    "v_fmaak_f32 v20, v174, v174, 0x409d30fc\n"
    "v_add_f32 v22, 1.0, v22\n"
    "v_mfma_f32_32x32x16_bf16 v[176:191], a[248:251], a[124:127], v[176:191]\n"
    "ds_read_b64_tr_b16 a[244:245], v19 offset:4096\n"
    "ds_read_b64_tr_b16 a[246:247], v19 offset:5120\n"
    "v_rcp_f32 v22, v22\n"
    "v_mul_f32 v20, v20, v174\n"
    "v_mfma_f32_32x32x16_bf16 v[192:207], a[252:255], a[124:127], v[192:207]\n"
    "s_waitcnt lgkmcnt(4)\n"
    "ds_read_b64_tr_b16 a[248:249], v18 offset:6144\n"
    "ds_read_b64_tr_b16 a[250:251], v18 offset:7168\n"
    "buffer_load_dwordx4 %[w1b], %[srd1], s40 offen offset:1024 lds\n"
    "v_mul_f32 v22, v22, v173\n"
    "v_cvt_pk_bf16_f32 v62, v21, v22\n"
    "v_mfma_f32_32x32x16_bf16 v[176:191], a[224:227], a[128:131], v[176:191]\n"
    "ds_read_b64_tr_b16 a[252:253], v19 offset:6144\n"
    "ds_read_b64_tr_b16 a[254:255], v19 offset:7168\n"
    "v_exp_f32 v20, v20\n"
    "v_fmaak_f32 v21, v175, v175, 0x409d30fc\n"
    "v_mfma_f32_32x32x16_bf16 v[192:207], a[228:231], a[128:131], v[192:207]\n"
    "ds_read_b64_tr_b16 a[224:225], v18 offset:8192\n"
    "ds_read_b64_tr_b16 a[226:227], v18 offset:9216\n"
    "v_add_f32 v20, 1.0, v20\n"
    "v_rcp_f32 v20, v20\n"
    "v_mul_f32 v21, v21, v175\n"
    "v_mfma_f32_32x32x16_bf16 v[176:191], a[232:235], a[132:135], v[176:191]\n"
    "ds_read_b64_tr_b16 a[228:229], v19 offset:8192\n"
    "ds_read_b64_tr_b16 a[230:231], v19 offset:9216\n"
    "s_add_u32 m0, %[ldsw], 57344\n"
    "s_mov_b32 s40, 4718976\n"
    "v_mul_f32 v20, v20, v174\n"
    "v_exp_f32 v21, v21\n"
    "v_mfma_f32_32x32x16_bf16 v[192:207], a[236:239], a[132:135], v[192:207]\n"
    "s_waitcnt lgkmcnt(4)\n"
    "ds_read_b64_tr_b16 a[232:233], v18 offset:10240\n"
    "ds_read_b64_tr_b16 a[234:235], v18 offset:11264\n"
    "buffer_load_dwordx4 %[w1a], %[srd1], s40 offen lds\n"
    "s_nop 0\n"
    "v_add_f32 v21, 1.0, v21\n"
    "v_mfma_f32_32x32x16_bf16 v[176:191], a[240:243], a[136:139], v[176:191]\n"
    "ds_read_b64_tr_b16 a[236:237], v19 offset:10240\n"
    "ds_read_b64_tr_b16 a[238:239], v19 offset:11264\n"
    "v_rcp_f32 v21, v21\n"
    "s_nop 0\n"
    "v_mfma_f32_32x32x16_bf16 v[192:207], a[244:247], a[136:139], v[192:207]\n"
    "ds_read_b64_tr_b16 a[240:241], v18 offset:12288\n"
    "ds_read_b64_tr_b16 a[242:243], v18 offset:13312\n"
    "v_mul_f32 v21, v21, v175\n"
    "v_cvt_pk_bf16_f32 v63, v20, v21\n"
    "v_mfma_f32_32x32x16_bf16 v[176:191], a[248:251], a[140:143], v[176:191]\n"
    "ds_read_b64_tr_b16 a[244:245], v19 offset:12288\n"
    "ds_read_b64_tr_b16 a[246:247], v19 offset:13312\n"
    "v_mfma_f32_32x32x16_bf16 v[192:207], a[252:255], a[140:143], v[192:207]\n"
    "s_waitcnt lgkmcnt(4)\n"
    "ds_read_b64_tr_b16 a[248:249], v18 offset:14336\n"
    "ds_read_b64_tr_b16 a[250:251], v18 offset:15360\n"
    "buffer_load_dwordx4 %[w1b], %[srd1], s40 offen offset:1024 lds\n"
    "v_mfma_f32_32x32x16_bf16 v[208:223], a[224:227], v[48:51], v[208:223]\n"
    "ds_read_b64_tr_b16 a[252:253], v19 offset:14336\n"
    "ds_read_b64_tr_b16 a[254:255], v19 offset:15360\n"
    "v_mfma_f32_32x32x16_bf16 v[224:239], a[228:231], v[48:51], v[224:239]\n"
    "ds_read_b64_tr_b16 a[224:225], v18 offset:16384\n"
    "ds_read_b64_tr_b16 a[226:227], v18 offset:17408\n"
    "v_mfma_f32_32x32x16_bf16 v[208:223], a[232:235], v[52:55], v[208:223]\n"
    "ds_read_b64_tr_b16 a[228:229], v19 offset:16384\n"
    "ds_read_b64_tr_b16 a[230:231], v19 offset:17408\n"
    "s_add_u32 m0, %[ldsw], 65536\n"
    "s_mov_b32 s40, 147456\n"
    "ds_read_b128 v[144:147], %[vb] offset:768\n"
    "v_mfma_f32_32x32x16_bf16 v[224:239], a[236:239], v[52:55], v[224:239]\n"
    "s_waitcnt lgkmcnt(5)\n"
    "ds_read_b64_tr_b16 a[232:233], v18 offset:18432\n"
    "ds_read_b64_tr_b16 a[234:235], v18 offset:19456\n"
    "buffer_load_dwordx4 %[w2a], %[srd2], s40 offen lds\n"
    "v_fmaak_f32 v20, v176, v176, 0x409d30fc\n"
    "v_mul_f32 v20, v20, v176\n"
    "v_exp_f32 v20, v20\n"
    "v_mfma_f32_32x32x16_bf16 v[208:223], a[240:243], v[56:59], v[208:223]\n"
    "ds_read_b64_tr_b16 a[236:237], v19 offset:18432\n"
    "ds_read_b64_tr_b16 a[238:239], v19 offset:19456\n"
    "v_fmaak_f32 v21, v177, v177, 0x409d30fc\n"
    "v_add_f32 v20, 1.0, v20\n"
    "v_mfma_f32_32x32x16_bf16 v[224:239], a[244:247], v[56:59], v[224:239]\n"
    "ds_read_b64_tr_b16 a[240:241], v18 offset:20480\n"
    "ds_read_b64_tr_b16 a[242:243], v18 offset:21504\n"
    "ds_read_b128 v[148:151], %[vb] offset:800\n"
    "v_rcp_f32 v20, v20\n"
    "v_mul_f32 v21, v21, v177\n"
    "v_mfma_f32_32x32x16_bf16 v[208:223], a[248:251], v[60:63], v[208:223]\n"
    "ds_read_b64_tr_b16 a[244:245], v19 offset:20480\n"
    "ds_read_b64_tr_b16 a[246:247], v19 offset:21504\n"
    "v_mul_f32 v20, v20, v176\n"
    "v_exp_f32 v21, v21\n"
    "v_mfma_f32_32x32x16_bf16 v[224:239], a[252:255], v[60:63], v[224:239]\n"
    "s_waitcnt lgkmcnt(5)\n"
    "ds_read_b64_tr_b16 a[248:249], v18 offset:22528\n"
    "ds_read_b64_tr_b16 a[250:251], v18 offset:23552\n"
    "buffer_load_dwordx4 %[w2b], %[srd2], s40 offen offset:1024 lds\n"
    "v_fmaak_f32 v22, v178, v178, 0x409d30fc\n"
    "v_add_f32 v21, 1.0, v21\n"
    "v_rcp_f32 v21, v21\n"
    "v_mfma_f32_32x32x16_bf16 v[240:255], a[224:227], v[48:51], v[240:255]\n"
    "ds_read_b64_tr_b16 a[252:253], v19 offset:22528\n"
    "ds_read_b64_tr_b16 a[254:255], v19 offset:23552\n"
    "ds_read_b128 v[152:155], %[vb] offset:832\n"
    "v_mul_f32 v22, v22, v178\n"
    "v_mul_f32 v21, v21, v177\n"
    "v_mfma_f32_32x32x16_bf16 a[144:159], a[228:231], v[48:51], a[144:159]\n"
    "s_waitcnt vmcnt(18)\n"
    "s_barrier\n"
    "ds_read_b64_tr_b16 a[224:225], v18 offset:24576\n"
    "ds_read_b64_tr_b16 a[226:227], v18 offset:25600\n"
    "v_mfma_f32_32x32x16_bf16 v[240:255], a[232:235], v[52:55], v[240:255]\n"
    "ds_read_b64_tr_b16 a[228:229], v19 offset:24576\n"
    "ds_read_b64_tr_b16 a[230:231], v19 offset:25600\n"
    "s_add_u32 m0, %[ldsw], 73728\n"
    "s_mov_b32 s40, 147584\n"
    "v_cvt_pk_bf16_f32 v64, v20, v21\n"
    "v_exp_f32 v22, v22\n"
    "v_mfma_f32_32x32x16_bf16 a[144:159], a[236:239], v[52:55], a[144:159]\n"
    "s_waitcnt lgkmcnt(5)\n"
    "ds_read_b64_tr_b16 a[232:233], v18 offset:26624\n"
    "ds_read_b64_tr_b16 a[234:235], v18 offset:27648\n"
    "buffer_load_dwordx4 %[w2a], %[srd2], s40 offen lds\n"
    "v_fmaak_f32 v20, v179, v179, 0x409d30fc\n"
    "v_add_f32 v22, 1.0, v22\n"
    "v_mfma_f32_32x32x16_bf16 v[240:255], a[240:243], v[56:59], v[240:255]\n"
    "ds_read_b64_tr_b16 a[236:237], v19 offset:26624\n"
    "ds_read_b64_tr_b16 a[238:239], v19 offset:27648\n"
    "ds_read_b128 v[156:159], %[vb] offset:864\n"
    "v_rcp_f32 v22, v22\n"
    "v_mul_f32 v20, v20, v179\n"
    "v_mfma_f32_32x32x16_bf16 a[144:159], a[244:247], v[56:59], a[144:159]\n"
    "ds_read_b64_tr_b16 a[240:241], v18 offset:28672\n"
    "ds_read_b64_tr_b16 a[242:243], v18 offset:29696\n"
    "v_mul_f32 v22, v22, v178\n"
    "v_exp_f32 v20, v20\n"
    "v_fmaak_f32 v21, v180, v180, 0x409d30fc\n"
    "v_mfma_f32_32x32x16_bf16 v[240:255], a[248:251], v[60:63], v[240:255]\n"
    "ds_read_b64_tr_b16 a[244:245], v19 offset:28672\n"
    "ds_read_b64_tr_b16 a[246:247], v19 offset:29696\n"
    "v_add_f32 v20, 1.0, v20\n"
    "v_rcp_f32 v20, v20\n"
    "v_mfma_f32_32x32x16_bf16 a[144:159], a[252:255], v[60:63], a[144:159]\n"
    "s_waitcnt lgkmcnt(5)\n"
    "ds_read_b64_tr_b16 a[248:249], v18 offset:30720\n"
    "ds_read_b64_tr_b16 a[250:251], v18 offset:31744\n"
    "buffer_load_dwordx4 %[w2b], %[srd2], s40 offen offset:1024 lds\n"
    "ds_read_b128 v[160:163], %[vb] offset:896\n"
    "v_mul_f32 v21, v21, v180\n"
    "v_mul_f32 v20, v20, v179\n"
    "v_mfma_f32_32x32x16_bf16 a[160:175], a[224:227], v[48:51], a[160:175]\n"
    "ds_read_b64_tr_b16 a[252:253], v19 offset:30720\n"
    "ds_read_b64_tr_b16 a[254:255], v19 offset:31744\n"
    "v_cvt_pk_bf16_f32 v65, v22, v20\n"
    "v_exp_f32 v21, v21\n"
    "v_mfma_f32_32x32x16_bf16 a[176:191], a[228:231], v[48:51], a[176:191]\n"
    "ds_read_b64_tr_b16 a[224:225], v18 offset:32768\n"
    "ds_read_b64_tr_b16 a[226:227], v18 offset:33792\n"
    "v_fmaak_f32 v22, v181, v181, 0x409d30fc\n"
    "v_add_f32 v21, 1.0, v21\n"
    "v_mfma_f32_32x32x16_bf16 a[160:175], a[232:235], v[52:55], a[160:175]\n"
    "ds_read_b64_tr_b16 a[228:229], v19 offset:32768\n"
    "ds_read_b64_tr_b16 a[230:231], v19 offset:33792\n"
    "s_add_u32 m0, %[ldsw], 81920\n"
    "s_mov_b32 s40, 147712\n"
    "ds_read_b128 v[164:167], %[vb] offset:928\n"
    "v_rcp_f32 v21, v21\n"
    "v_mul_f32 v22, v22, v181\n"
    "v_mul_f32 v21, v21, v180\n"
    "v_mfma_f32_32x32x16_bf16 a[176:191], a[236:239], v[52:55], a[176:191]\n"
    "s_waitcnt lgkmcnt(5)\n"
    "ds_read_b64_tr_b16 a[232:233], v18 offset:34816\n"
    "ds_read_b64_tr_b16 a[234:235], v18 offset:35840\n"
    "buffer_load_dwordx4 %[w2a], %[srd2], s40 offen lds\n"
    "v_exp_f32 v22, v22\n"
    "v_fmaak_f32 v20, v182, v182, 0x409d30fc\n"
    "v_mfma_f32_32x32x16_bf16 a[160:175], a[240:243], v[56:59], a[160:175]\n"
    "ds_read_b64_tr_b16 a[236:237], v19 offset:34816\n"
    "ds_read_b64_tr_b16 a[238:239], v19 offset:35840\n"
    "v_add_f32 v22, 1.0, v22\n"
    "v_rcp_f32 v22, v22\n"
    "v_mfma_f32_32x32x16_bf16 a[176:191], a[244:247], v[56:59], a[176:191]\n"
    "ds_read_b64_tr_b16 a[240:241], v18 offset:36864\n"
    "ds_read_b64_tr_b16 a[242:243], v18 offset:37888\n"
    "ds_read_b128 v[168:171], %[vb] offset:960\n"
    "v_mul_f32 v20, v20, v182\n"
    "v_mul_f32 v22, v22, v181\n"
    "v_mfma_f32_32x32x16_bf16 a[160:175], a[248:251], v[60:63], a[160:175]\n"
    "ds_read_b64_tr_b16 a[244:245], v19 offset:36864\n"
    "ds_read_b64_tr_b16 a[246:247], v19 offset:37888\n"
    "v_cvt_pk_bf16_f32 v66, v21, v22\n"
    "v_exp_f32 v20, v20\n"
    "v_mfma_f32_32x32x16_bf16 a[176:191], a[252:255], v[60:63], a[176:191]\n"
    "s_waitcnt lgkmcnt(5)\n"
    "ds_read_b64_tr_b16 a[248:249], v18 offset:38912\n"
    "ds_read_b64_tr_b16 a[250:251], v18 offset:39936\n"
    "buffer_load_dwordx4 %[w2b], %[srd2], s40 offen offset:1024 lds\n"
    "v_fmaak_f32 v21, v183, v183, 0x409d30fc\n"
    "v_add_f32 v20, 1.0, v20\n"
    "v_rcp_f32 v20, v20\n"
    "v_mfma_f32_32x32x16_bf16 a[192:207], a[224:227], v[48:51], a[192:207]\n"
    "ds_read_b64_tr_b16 a[252:253], v19 offset:38912\n"
    "ds_read_b64_tr_b16 a[254:255], v19 offset:39936\n"
    "ds_read_b128 v[172:175], %[vb] offset:992\n"
    "v_mul_f32 v21, v21, v183\n"
    "v_mul_f32 v20, v20, v182\n"
    "v_mfma_f32_32x32x16_bf16 a[208:223], a[228:231], v[48:51], a[208:223]\n"
    "ds_read_b64_tr_b16 a[224:225], v18 offset:40960\n"
    "ds_read_b64_tr_b16 a[226:227], v18 offset:41984\n"
    "v_exp_f32 v21, v21\n"
    "v_fmaak_f32 v22, v184, v184, 0x409d30fc\n"
    "v_mfma_f32_32x32x16_bf16 a[192:207], a[232:235], v[52:55], a[192:207]\n"
    "ds_read_b64_tr_b16 a[228:229], v19 offset:40960\n"
    "ds_read_b64_tr_b16 a[230:231], v19 offset:41984\n"
    "s_add_u32 m0, %[ldsw], 90112\n"
    "s_mov_b32 s40, 147840\n"
    "v_add_f32 v21, 1.0, v21\n"
    "v_rcp_f32 v21, v21\n"
    "v_mfma_f32_32x32x16_bf16 a[208:223], a[236:239], v[52:55], a[208:223]\n"
    "s_waitcnt lgkmcnt(5)\n"
    "ds_read_b64_tr_b16 a[232:233], v18 offset:43008\n"
    "ds_read_b64_tr_b16 a[234:235], v18 offset:44032\n"
    "buffer_load_dwordx4 %[w2a], %[srd2], s40 offen lds\n"
    "v_mul_f32 v22, v22, v184\n"
    "v_mul_f32 v21, v21, v183\n"
    "v_cvt_pk_bf16_f32 v67, v20, v21\n"
    "v_mfma_f32_32x32x16_bf16 a[192:207], a[240:243], v[56:59], a[192:207]\n"
    "ds_read_b64_tr_b16 a[236:237], v19 offset:43008\n"
    "ds_read_b64_tr_b16 a[238:239], v19 offset:44032\n"
    "v_exp_f32 v22, v22\n"
    "v_fmaak_f32 v20, v185, v185, 0x409d30fc\n"
    "v_mfma_f32_32x32x16_bf16 a[208:223], a[244:247], v[56:59], a[208:223]\n"
    "ds_read_b64_tr_b16 a[240:241], v18 offset:45056\n"
    "ds_read_b64_tr_b16 a[242:243], v18 offset:46080\n"
    "v_add_f32 v22, 1.0, v22\n"
    "v_rcp_f32 v22, v22\n"
    "v_mfma_f32_32x32x16_bf16 a[192:207], a[248:251], v[60:63], a[192:207]\n"
    "ds_read_b64_tr_b16 a[244:245], v19 offset:45056\n"
    "ds_read_b64_tr_b16 a[246:247], v19 offset:46080\n"
    "v_mul_f32 v20, v20, v185\n"
    "v_mul_f32 v22, v22, v184\n"
    "v_mfma_f32_32x32x16_bf16 a[208:223], a[252:255], v[60:63], a[208:223]\n"
    "s_waitcnt lgkmcnt(4)\n"
    "ds_read_b64_tr_b16 a[248:249], v18 offset:47104\n"
    "ds_read_b64_tr_b16 a[250:251], v18 offset:48128\n"
    "buffer_load_dwordx4 %[w2b], %[srd2], s40 offen offset:1024 lds\n"
    "v_exp_f32 v20, v20\n"
    "v_fmaak_f32 v21, v186, v186, 0x409d30fc\n"
    "v_mfma_f32_32x32x16_bf16 v[144:159], a[224:227], a[0:3], v[144:159]\n"
    "ds_read_b64_tr_b16 a[252:253], v19 offset:47104\n"
    "ds_read_b64_tr_b16 a[254:255], v19 offset:48128\n"
    "v_add_f32 v20, 1.0, v20\n"
    "v_rcp_f32 v20, v20\n"
    "v_mul_f32 v21, v21, v186\n"
    "v_mfma_f32_32x32x16_bf16 v[160:175], a[228:231], a[0:3], v[160:175]\n"
    "s_waitcnt vmcnt(18)\n"
    "s_barrier\n"
    "ds_read_b64_tr_b16 a[224:225], %[rd0] offset:0\n"
    "ds_read_b64_tr_b16 a[226:227], %[rd0] offset:1024\n"
    "v_mfma_f32_32x32x16_bf16 v[144:159], a[232:235], a[4:7], v[144:159]\n"
    "ds_read_b64_tr_b16 a[228:229], %[rd1] offset:0\n"
    "ds_read_b64_tr_b16 a[230:231], %[rd1] offset:1024\n"
    "s_add_u32 m0, %[ldsw], 98304\n"
    "s_mov_b32 s40, 512\n"
    "v_mul_f32 v20, v20, v185\n"
    "v_cvt_pk_bf16_f32 v68, v22, v20\n"
    "v_mfma_f32_32x32x16_bf16 v[160:175], a[236:239], a[4:7], v[160:175]\n"
    "s_waitcnt lgkmcnt(4)\n"
    "ds_read_b64_tr_b16 a[232:233], %[rd0] offset:2048\n"
    "ds_read_b64_tr_b16 a[234:235], %[rd0] offset:3072\n"
    "buffer_load_dwordx4 %[w1a], %[srd1], s40 offen lds\n"
    "v_exp_f32 v21, v21\n"
    "v_fmaak_f32 v22, v187, v187, 0x409d30fc\n"
    "v_mfma_f32_32x32x16_bf16 v[144:159], a[240:243], a[8:11], v[144:159]\n"
    "ds_read_b64_tr_b16 a[236:237], %[rd1] offset:2048\n"
    "ds_read_b64_tr_b16 a[238:239], %[rd1] offset:3072\n"
    "v_add_f32 v21, 1.0, v21\n"
    "v_rcp_f32 v21, v21\n"
    "v_mfma_f32_32x32x16_bf16 v[160:175], a[244:247], a[8:11], v[160:175]\n"
    "ds_read_b64_tr_b16 a[240:241], %[rd0] offset:4096\n"
    "ds_read_b64_tr_b16 a[242:243], %[rd0] offset:5120\n"
    "v_mul_f32 v22, v22, v187\n"
    "v_mul_f32 v21, v21, v186\n"
    "v_mfma_f32_32x32x16_bf16 v[144:159], a[248:251], a[12:15], v[144:159]\n"
    "ds_read_b64_tr_b16 a[244:245], %[rd1] offset:4096\n"
    "ds_read_b64_tr_b16 a[246:247], %[rd1] offset:5120\n"
    "v_exp_f32 v22, v22\n"
    "v_fmaak_f32 v20, v188, v188, 0x409d30fc\n"
    "v_add_f32 v22, 1.0, v22\n"
    "v_mfma_f32_32x32x16_bf16 v[160:175], a[252:255], a[12:15], v[160:175]\n"
    "s_waitcnt lgkmcnt(4)\n"
    "ds_read_b64_tr_b16 a[248:249], %[rd0] offset:6144\n"
    "ds_read_b64_tr_b16 a[250:251], %[rd0] offset:7168\n"
    "buffer_load_dwordx4 %[w1b], %[srd1], s40 offen offset:1024 lds\n"
    "v_rcp_f32 v22, v22\n"
    "v_mul_f32 v20, v20, v188\n"
    "v_mfma_f32_32x32x16_bf16 v[144:159], a[224:227], a[16:19], v[144:159]\n"
    "ds_read_b64_tr_b16 a[252:253], %[rd1] offset:6144\n"
    "ds_read_b64_tr_b16 a[254:255], %[rd1] offset:7168\n"
    "v_mul_f32 v22, v22, v187\n"
    "v_cvt_pk_bf16_f32 v69, v21, v22\n"
    "v_mfma_f32_32x32x16_bf16 v[160:175], a[228:231], a[16:19], v[160:175]\n"
    "ds_read_b64_tr_b16 a[224:225], %[rd0] offset:8192\n"
    "ds_read_b64_tr_b16 a[226:227], %[rd0] offset:9216\n"
    "v_exp_f32 v20, v20\n"
    "v_fmaak_f32 v21, v189, v189, 0x409d30fc\n"
    "v_mfma_f32_32x32x16_bf16 v[144:159], a[232:235], a[20:23], v[144:159]\n"
    "ds_read_b64_tr_b16 a[228:229], %[rd1] offset:8192\n"
    "ds_read_b64_tr_b16 a[230:231], %[rd1] offset:9216\n"
    "s_add_u32 m0, %[ldsw], 106496\n"
    "s_mov_b32 s40, 590336\n"
    "v_add_f32 v20, 1.0, v20\n"
    "v_rcp_f32 v20, v20\n"
    "v_mfma_f32_32x32x16_bf16 v[160:175], a[236:239], a[20:23], v[160:175]\n"
    "s_waitcnt lgkmcnt(4)\n"
    "ds_read_b64_tr_b16 a[232:233], %[rd0] offset:10240\n"
    "ds_read_b64_tr_b16 a[234:235], %[rd0] offset:11264\n"
    "buffer_load_dwordx4 %[w1a], %[srd1], s40 offen lds\n"
    "v_mul_f32 v21, v21, v189\n"
    "v_mul_f32 v20, v20, v188\n"
    "v_exp_f32 v21, v21\n"
    "v_mfma_f32_32x32x16_bf16 v[144:159], a[240:243], a[24:27], v[144:159]\n"
    "ds_read_b64_tr_b16 a[236:237], %[rd1] offset:10240\n"
    "ds_read_b64_tr_b16 a[238:239], %[rd1] offset:11264\n"
    "v_fmaak_f32 v22, v190, v190, 0x409d30fc\n"
    "v_add_f32 v21, 1.0, v21\n"
    "v_mfma_f32_32x32x16_bf16 v[160:175], a[244:247], a[24:27], v[160:175]\n"
    "ds_read_b64_tr_b16 a[240:241], %[rd0] offset:12288\n"
    "ds_read_b64_tr_b16 a[242:243], %[rd0] offset:13312\n"
    "v_rcp_f32 v21, v21\n"
    "v_mul_f32 v22, v22, v190\n"
    "v_mfma_f32_32x32x16_bf16 v[144:159], a[248:251], a[28:31], v[144:159]\n"
    "ds_read_b64_tr_b16 a[244:245], %[rd1] offset:12288\n"
    "ds_read_b64_tr_b16 a[246:247], %[rd1] offset:13312\n"
    "v_mul_f32 v21, v21, v189\n"
    "v_cvt_pk_bf16_f32 v70, v20, v21\n"
    "v_mfma_f32_32x32x16_bf16 v[160:175], a[252:255], a[28:31], v[160:175]\n"
    "s_waitcnt lgkmcnt(4)\n"
    "ds_read_b64_tr_b16 a[248:249], %[rd0] offset:14336\n"
    "ds_read_b64_tr_b16 a[250:251], %[rd0] offset:15360\n"
    "buffer_load_dwordx4 %[w1b], %[srd1], s40 offen offset:1024 lds\n"
    "v_exp_f32 v22, v22\n"
    "v_fmaak_f32 v20, v191, v191, 0x409d30fc\n"
    "v_add_f32 v22, 1.0, v22\n"
    "v_mfma_f32_32x32x16_bf16 v[144:159], a[224:227], a[32:35], v[144:159]\n"
    "ds_read_b64_tr_b16 a[252:253], %[rd1] offset:14336\n"
    "ds_read_b64_tr_b16 a[254:255], %[rd1] offset:15360\n"
    "v_rcp_f32 v22, v22\n"
    "v_mul_f32 v20, v20, v191\n"
    "v_mfma_f32_32x32x16_bf16 v[160:175], a[228:231], a[32:35], v[160:175]\n"
    "ds_read_b64_tr_b16 a[224:225], %[rd0] offset:16384\n"
    "ds_read_b64_tr_b16 a[226:227], %[rd0] offset:17408\n"
    "v_mul_f32 v22, v22, v190\n"
    "v_exp_f32 v20, v20\n"
    "v_mfma_f32_32x32x16_bf16 v[144:159], a[232:235], a[36:39], v[144:159]\n"
    "ds_read_b64_tr_b16 a[228:229], %[rd1] offset:16384\n"
    "ds_read_b64_tr_b16 a[230:231], %[rd1] offset:17408\n"
    "s_add_u32 m0, %[ldsw], 114688\n"
    "s_mov_b32 s40, 1180160\n"
    "v_fmaak_f32 v21, v192, v192, 0x409d30fc\n"
    "v_add_f32 v20, 1.0, v20\n"
    "v_mfma_f32_32x32x16_bf16 v[160:175], a[236:239], a[36:39], v[160:175]\n"
    "s_waitcnt lgkmcnt(4)\n"
    "ds_read_b64_tr_b16 a[232:233], %[rd0] offset:18432\n"
    "ds_read_b64_tr_b16 a[234:235], %[rd0] offset:19456\n"
    "buffer_load_dwordx4 %[w1a], %[srd1], s40 offen lds\n"
    "v_rcp_f32 v20, v20\n"
    "v_mul_f32 v21, v21, v192\n"
    "v_mfma_f32_32x32x16_bf16 v[144:159], a[240:243], a[40:43], v[144:159]\n"
    "ds_read_b64_tr_b16 a[236:237], %[rd1] offset:18432\n"
    "ds_read_b64_tr_b16 a[238:239], %[rd1] offset:19456\n"
    "v_mul_f32 v20, v20, v191\n"
    "v_cvt_pk_bf16_f32 v71, v22, v20\n"
    "v_exp_f32 v21, v21\n"
    "v_mfma_f32_32x32x16_bf16 v[160:175], a[244:247], a[40:43], v[160:175]\n"
    "ds_read_b64_tr_b16 a[240:241], %[rd0] offset:20480\n"
    "ds_read_b64_tr_b16 a[242:243], %[rd0] offset:21504\n"
    "v_fmaak_f32 v22, v193, v193, 0x409d30fc\n"
    "v_add_f32 v21, 1.0, v21\n"
    "v_mfma_f32_32x32x16_bf16 v[144:159], a[248:251], a[44:47], v[144:159]\n"
    "ds_read_b64_tr_b16 a[244:245], %[rd1] offset:20480\n"
    "ds_read_b64_tr_b16 a[246:247], %[rd1] offset:21504\n"
    "v_rcp_f32 v21, v21\n"
    "v_mul_f32 v22, v22, v193\n"
    "v_mfma_f32_32x32x16_bf16 v[160:175], a[252:255], a[44:47], v[160:175]\n"
    "s_waitcnt lgkmcnt(4)\n"
    "ds_read_b64_tr_b16 a[248:249], %[rd0] offset:22528\n"
    "ds_read_b64_tr_b16 a[250:251], %[rd0] offset:23552\n"
    "buffer_load_dwordx4 %[w1b], %[srd1], s40 offen offset:1024 lds\n"
    "v_mul_f32 v21, v21, v192\n"
    "v_exp_f32 v22, v22\n"
    "v_mfma_f32_32x32x16_bf16 v[144:159], a[224:227], a[48:51], v[144:159]\n"
    "ds_read_b64_tr_b16 a[252:253], %[rd1] offset:22528\n"
    "ds_read_b64_tr_b16 a[254:255], %[rd1] offset:23552\n"
    "v_fmaak_f32 v20, v194, v194, 0x409d30fc\n"
    "v_add_f32 v22, 1.0, v22\n"
    "v_mfma_f32_32x32x16_bf16 v[160:175], a[228:231], a[48:51], v[160:175]\n"
    "s_waitcnt vmcnt(18)\n"
    "s_barrier\n"
    "ds_read_b64_tr_b16 a[224:225], %[rd0] offset:24576\n"
    "ds_read_b64_tr_b16 a[226:227], %[rd0] offset:25600\n"
    "v_mfma_f32_32x32x16_bf16 v[144:159], a[232:235], a[52:55], v[144:159]\n"
    "ds_read_b64_tr_b16 a[228:229], %[rd1] offset:24576\n"
    "ds_read_b64_tr_b16 a[230:231], %[rd1] offset:25600\n"
    "s_add_u32 m0, %[ldsw], 122880\n"
    "s_mov_b32 s40, 1769984\n"
    "v_rcp_f32 v22, v22\n"
    "v_mul_f32 v20, v20, v194\n"
    "v_mul_f32 v22, v22, v193\n"
    "v_mfma_f32_32x32x16_bf16 v[160:175], a[236:239], a[52:55], v[160:175]\n"
    "s_waitcnt lgkmcnt(4)\n"
    "ds_read_b64_tr_b16 a[232:233], %[rd0] offset:26624\n"
    "ds_read_b64_tr_b16 a[234:235], %[rd0] offset:27648\n"
    "buffer_load_dwordx4 %[w1a], %[srd1], s40 offen lds\n"
    "v_cvt_pk_bf16_f32 v72, v21, v22\n"
    "v_exp_f32 v20, v20\n"
    "v_mfma_f32_32x32x16_bf16 v[144:159], a[240:243], a[56:59], v[144:159]\n"
    "ds_read_b64_tr_b16 a[236:237], %[rd1] offset:26624\n"
    "ds_read_b64_tr_b16 a[238:239], %[rd1] offset:27648\n"
    "v_fmaak_f32 v21, v195, v195, 0x409d30fc\n"
    "v_add_f32 v20, 1.0, v20\n"
    "v_mfma_f32_32x32x16_bf16 v[160:175], a[244:247], a[56:59], v[160:175]\n"
    "ds_read_b64_tr_b16 a[240:241], %[rd0] offset:28672\n"
    "ds_read_b64_tr_b16 a[242:243], %[rd0] offset:29696\n"
    "v_rcp_f32 v20, v20\n"
    "v_mul_f32 v21, v21, v195\n"
    "v_mfma_f32_32x32x16_bf16 v[144:159], a[248:251], a[60:63], v[144:159]\n"
    "ds_read_b64_tr_b16 a[244:245], %[rd1] offset:28672\n"
    "ds_read_b64_tr_b16 a[246:247], %[rd1] offset:29696\n"
    "v_mul_f32 v20, v20, v194\n"
    "v_exp_f32 v21, v21\n"
    "v_mfma_f32_32x32x16_bf16 v[160:175], a[252:255], a[60:63], v[160:175]\n"
    "s_waitcnt lgkmcnt(4)\n"
    "ds_read_b64_tr_b16 a[248:249], %[rd0] offset:30720\n"
    "ds_read_b64_tr_b16 a[250:251], %[rd0] offset:31744\n"
    "buffer_load_dwordx4 %[w1b], %[srd1], s40 offen offset:1024 lds\n"
    "v_fmaak_f32 v22, v196, v196, 0x409d30fc\n"
    "v_add_f32 v21, 1.0, v21\n"
    "v_rcp_f32 v21, v21\n"
    "v_mfma_f32_32x32x16_bf16 v[144:159], a[224:227], a[64:67], v[144:159]\n"
    "ds_read_b64_tr_b16 a[252:253], %[rd1] offset:30720\n"
    "ds_read_b64_tr_b16 a[254:255], %[rd1] offset:31744\n"
    "v_mul_f32 v22, v22, v196\n"
    "v_mul_f32 v21, v21, v195\n"
    "v_mfma_f32_32x32x16_bf16 v[160:175], a[228:231], a[64:67], v[160:175]\n"
    "ds_read_b64_tr_b16 a[224:225], %[rd0] offset:32768\n"
    "ds_read_b64_tr_b16 a[226:227], %[rd0] offset:33792\n"
    "v_cvt_pk_bf16_f32 v73, v20, v21\n"
    "v_exp_f32 v22, v22\n"
    "v_mfma_f32_32x32x16_bf16 v[144:159], a[232:235], a[68:71], v[144:159]\n"
    "ds_read_b64_tr_b16 a[228:229], %[rd1] offset:32768\n"
    "ds_read_b64_tr_b16 a[230:231], %[rd1] offset:33792\n"
    "s_add_u32 m0, %[ldsw], 131072\n"
    "s_mov_b32 s40, 2359808\n"
    "v_fmaak_f32 v20, v197, v197, 0x409d30fc\n"
    "v_add_f32 v22, 1.0, v22\n"
    "v_mfma_f32_32x32x16_bf16 v[160:175], a[236:239], a[68:71], v[160:175]\n"
    "s_waitcnt lgkmcnt(4)\n"
    "ds_read_b64_tr_b16 a[232:233], %[rd0] offset:34816\n"
    "ds_read_b64_tr_b16 a[234:235], %[rd0] offset:35840\n"
    "buffer_load_dwordx4 %[w1a], %[srd1], s40 offen lds\n"
    "v_rcp_f32 v22, v22\n"
    "v_mul_f32 v20, v20, v197\n"
    "v_mul_f32 v22, v22, v196\n"
    "v_mfma_f32_32x32x16_bf16 v[144:159], a[240:243], a[72:75], v[144:159]\n"
    "ds_read_b64_tr_b16 a[236:237], %[rd1] offset:34816\n"
    "ds_read_b64_tr_b16 a[238:239], %[rd1] offset:35840\n"
    "v_exp_f32 v20, v20\n"
    "v_fmaak_f32 v21, v198, v198, 0x409d30fc\n"
    "v_mfma_f32_32x32x16_bf16 v[160:175], a[244:247], a[72:75], v[160:175]\n"
    "ds_read_b64_tr_b16 a[240:241], %[rd0] offset:36864\n"
    "ds_read_b64_tr_b16 a[242:243], %[rd0] offset:37888\n"
    "v_add_f32 v20, 1.0, v20\n"
    "v_rcp_f32 v20, v20\n"
    "v_mfma_f32_32x32x16_bf16 v[144:159], a[248:251], a[76:79], v[144:159]\n"
    "ds_read_b64_tr_b16 a[244:245], %[rd1] offset:36864\n"
    "ds_read_b64_tr_b16 a[246:247], %[rd1] offset:37888\n"
    "v_mul_f32 v21, v21, v198\n"
    "v_mul_f32 v20, v20, v197\n"
    "v_mfma_f32_32x32x16_bf16 v[160:175], a[252:255], a[76:79], v[160:175]\n"
    "s_waitcnt lgkmcnt(4)\n"
    "ds_read_b64_tr_b16 a[248:249], %[rd0] offset:38912\n"
    "ds_read_b64_tr_b16 a[250:251], %[rd0] offset:39936\n"
    "buffer_load_dwordx4 %[w1b], %[srd1], s40 offen offset:1024 lds\n"
    "v_cvt_pk_bf16_f32 v74, v22, v20\n"
    "v_exp_f32 v21, v21\n"
    "v_mfma_f32_32x32x16_bf16 v[144:159], a[224:227], a[80:83], v[144:159]\n"
    "ds_read_b64_tr_b16 a[252:253], %[rd1] offset:38912\n"
    "ds_read_b64_tr_b16 a[254:255], %[rd1] offset:39936\n"
    "v_fmaak_f32 v22, v199, v199, 0x409d30fc\n"
    "v_add_f32 v21, 1.0, v21\n"
    "v_rcp_f32 v21, v21\n"
    "v_mfma_f32_32x32x16_bf16 v[160:175], a[228:231], a[80:83], v[160:175]\n"
    "ds_read_b64_tr_b16 a[224:225], %[rd0] offset:40960\n"
    "ds_read_b64_tr_b16 a[226:227], %[rd0] offset:41984\n"
    "v_mul_f32 v22, v22, v199\n"
    "v_mul_f32 v21, v21, v198\n"
    "v_mfma_f32_32x32x16_bf16 v[144:159], a[232:235], a[84:87], v[144:159]\n"
    "ds_read_b64_tr_b16 a[228:229], %[rd1] offset:40960\n"
    "ds_read_b64_tr_b16 a[230:231], %[rd1] offset:41984\n"
    "s_add_u32 m0, %[ldsw], 139264\n"
    "s_mov_b32 s40, 2949632\n"
    "v_exp_f32 v22, v22\n"
    "v_fmaak_f32 v20, v200, v200, 0x409d30fc\n"
    "v_mfma_f32_32x32x16_bf16 v[160:175], a[236:239], a[84:87], v[160:175]\n"
    "s_waitcnt lgkmcnt(4)\n"
    "ds_read_b64_tr_b16 a[232:233], %[rd0] offset:43008\n"
    "ds_read_b64_tr_b16 a[234:235], %[rd0] offset:44032\n"
    "buffer_load_dwordx4 %[w1a], %[srd1], s40 offen lds\n"
    "v_add_f32 v22, 1.0, v22\n"
    "v_rcp_f32 v22, v22\n"
    "v_mfma_f32_32x32x16_bf16 v[144:159], a[240:243], a[88:91], v[144:159]\n"
    "ds_read_b64_tr_b16 a[236:237], %[rd1] offset:43008\n"
    "ds_read_b64_tr_b16 a[238:239], %[rd1] offset:44032\n"
    "v_mul_f32 v20, v20, v200\n"
    "v_mul_f32 v22, v22, v199\n"
    "v_mfma_f32_32x32x16_bf16 v[160:175], a[244:247], a[88:91], v[160:175]\n"
    "ds_read_b64_tr_b16 a[240:241], %[rd0] offset:45056\n"
    "ds_read_b64_tr_b16 a[242:243], %[rd0] offset:46080\n"
    "v_cvt_pk_bf16_f32 v75, v21, v22\n"
    "v_exp_f32 v20, v20\n"
    "v_fmaak_f32 v21, v201, v201, 0x409d30fc\n"
    "v_mfma_f32_32x32x16_bf16 v[144:159], a[248:251], a[92:95], v[144:159]\n"
    "ds_read_b64_tr_b16 a[244:245], %[rd1] offset:45056\n"
    "ds_read_b64_tr_b16 a[246:247], %[rd1] offset:46080\n"
    "v_add_f32 v20, 1.0, v20\n"
    "v_rcp_f32 v20, v20\n"
    "v_mfma_f32_32x32x16_bf16 v[160:175], a[252:255], a[92:95], v[160:175]\n"
    "s_waitcnt lgkmcnt(4)\n"
    "ds_read_b64_tr_b16 a[248:249], %[rd0] offset:47104\n"
    "ds_read_b64_tr_b16 a[250:251], %[rd0] offset:48128\n"
    "buffer_load_dwordx4 %[w1b], %[srd1], s40 offen offset:1024 lds\n"
    "v_mul_f32 v21, v21, v201\n"
    "v_mul_f32 v20, v20, v200\n"
    "v_mfma_f32_32x32x16_bf16 v[144:159], a[224:227], a[96:99], v[144:159]\n"
    "ds_read_b64_tr_b16 a[252:253], %[rd1] offset:47104\n"
    "ds_read_b64_tr_b16 a[254:255], %[rd1] offset:48128\n"
    "v_exp_f32 v21, v21\n"
    "v_fmaak_f32 v22, v202, v202, 0x409d30fc\n"
    "v_mfma_f32_32x32x16_bf16 v[160:175], a[228:231], a[96:99], v[160:175]\n"
    "s_waitcnt vmcnt(18)\n"
    "s_barrier\n"
    "ds_read_b64_tr_b16 a[224:225], v16 offset:0\n"
    "ds_read_b64_tr_b16 a[226:227], v16 offset:1024\n"
    "v_mfma_f32_32x32x16_bf16 v[144:159], a[232:235], a[100:103], v[144:159]\n"
    "ds_read_b64_tr_b16 a[228:229], v17 offset:0\n"
    "ds_read_b64_tr_b16 a[230:231], v17 offset:1024\n"
    "s_add_u32 m0, %[ldsw], 0\n"
    "s_mov_b32 s40, 3539456\n"
    "v_add_f32 v21, 1.0, v21\n"
    "v_rcp_f32 v21, v21\n"
    "v_mfma_f32_32x32x16_bf16 v[160:175], a[236:239], a[100:103], v[160:175]\n"
    "s_waitcnt lgkmcnt(4)\n"
    "ds_read_b64_tr_b16 a[232:233], v16 offset:2048\n"
    "ds_read_b64_tr_b16 a[234:235], v16 offset:3072\n"
    "buffer_load_dwordx4 %[w1a], %[srd1], s40 offen lds\n"
    "v_mul_f32 v22, v22, v202\n"
    "v_mul_f32 v21, v21, v201\n"
    "v_cvt_pk_bf16_f32 v76, v20, v21\n"
    "v_mfma_f32_32x32x16_bf16 v[144:159], a[240:243], a[104:107], v[144:159]\n"
    "ds_read_b64_tr_b16 a[236:237], v17 offset:2048\n"
    "ds_read_b64_tr_b16 a[238:239], v17 offset:3072\n"
    "v_exp_f32 v22, v22\n"
    "v_fmaak_f32 v20, v203, v203, 0x409d30fc\n"
    "v_mfma_f32_32x32x16_bf16 v[160:175], a[244:247], a[104:107], v[160:175]\n"
    "ds_read_b64_tr_b16 a[240:241], v16 offset:4096\n"
    "ds_read_b64_tr_b16 a[242:243], v16 offset:5120\n"
    "v_add_f32 v22, 1.0, v22\n"
    "v_rcp_f32 v22, v22\n"
    "v_mfma_f32_32x32x16_bf16 v[144:159], a[248:251], a[108:111], v[144:159]\n"
    "ds_read_b64_tr_b16 a[244:245], v17 offset:4096\n"
    "ds_read_b64_tr_b16 a[246:247], v17 offset:5120\n"
    "v_mul_f32 v20, v20, v203\n"
    "v_mul_f32 v22, v22, v202\n"
    "v_mfma_f32_32x32x16_bf16 v[160:175], a[252:255], a[108:111], v[160:175]\n"
    "s_waitcnt lgkmcnt(4)\n"
    "ds_read_b64_tr_b16 a[248:249], v16 offset:6144\n"
    "ds_read_b64_tr_b16 a[250:251], v16 offset:7168\n"
    "buffer_load_dwordx4 %[w1b], %[srd1], s40 offen offset:1024 lds\n"
    "v_exp_f32 v20, v20\n"
    "v_fmaak_f32 v21, v204, v204, 0x409d30fc\n"
    "v_add_f32 v20, 1.0, v20\n"
    "v_mfma_f32_32x32x16_bf16 v[144:159], a[224:227], a[112:115], v[144:159]\n"
    "ds_read_b64_tr_b16 a[252:253], v17 offset:6144\n"
    "ds_read_b64_tr_b16 a[254:255], v17 offset:7168\n"
    "v_rcp_f32 v20, v20\n"
    "v_mul_f32 v21, v21, v204\n"
    "v_mfma_f32_32x32x16_bf16 v[160:175], a[228:231], a[112:115], v[160:175]\n"
    "ds_read_b64_tr_b16 a[224:225], v16 offset:8192\n"
    "ds_read_b64_tr_b16 a[226:227], v16 offset:9216\n"
    "v_mul_f32 v20, v20, v203\n"
    "v_cvt_pk_bf16_f32 v77, v22, v20\n"
    "v_mfma_f32_32x32x16_bf16 v[144:159], a[232:235], a[116:119], v[144:159]\n"
    "ds_read_b64_tr_b16 a[228:229], v17 offset:8192\n"
    "ds_read_b64_tr_b16 a[230:231], v17 offset:9216\n"
    "s_add_u32 m0, %[ldsw], 8192\n"
    "s_mov_b32 s40, 4129280\n"
    "v_exp_f32 v21, v21\n"
    "v_fmaak_f32 v22, v205, v205, 0x409d30fc\n"
    "v_mfma_f32_32x32x16_bf16 v[160:175], a[236:239], a[116:119], v[160:175]\n"
    "s_waitcnt lgkmcnt(4)\n"
    "ds_read_b64_tr_b16 a[232:233], v16 offset:10240\n"
    "ds_read_b64_tr_b16 a[234:235], v16 offset:11264\n"
    "buffer_load_dwordx4 %[w1a], %[srd1], s40 offen lds\n"
    "v_add_f32 v21, 1.0, v21\n"
    "v_rcp_f32 v21, v21\n"
    "v_mfma_f32_32x32x16_bf16 v[144:159], a[240:243], a[120:123], v[144:159]\n"
    "ds_read_b64_tr_b16 a[236:237], v17 offset:10240\n"
    "ds_read_b64_tr_b16 a[238:239], v17 offset:11264\n"
    "v_mul_f32 v22, v22, v205\n"
    "v_mul_f32 v21, v21, v204\n"
    "v_exp_f32 v22, v22\n"
    "v_mfma_f32_32x32x16_bf16 v[160:175], a[244:247], a[120:123], v[160:175]\n"
    "ds_read_b64_tr_b16 a[240:241], v16 offset:12288\n"
    "ds_read_b64_tr_b16 a[242:243], v16 offset:13312\n"
    "v_fmaak_f32 v20, v206, v206, 0x409d30fc\n"
    "v_add_f32 v22, 1.0, v22\n"
    "v_mfma_f32_32x32x16_bf16 v[144:159], a[248:251], a[124:127], v[144:159]\n"
    "ds_read_b64_tr_b16 a[244:245], v17 offset:12288\n"
    "ds_read_b64_tr_b16 a[246:247], v17 offset:13312\n"
    "v_rcp_f32 v22, v22\n"
    "v_mul_f32 v20, v20, v206\n"
    "v_mfma_f32_32x32x16_bf16 v[160:175], a[252:255], a[124:127], v[160:175]\n"
    "s_waitcnt lgkmcnt(4)\n"
    "ds_read_b64_tr_b16 a[248:249], v16 offset:14336\n"
    "ds_read_b64_tr_b16 a[250:251], v16 offset:15360\n"
    "buffer_load_dwordx4 %[w1b], %[srd1], s40 offen offset:1024 lds\n"
    "v_mul_f32 v22, v22, v205\n"
    "v_cvt_pk_bf16_f32 v78, v21, v22\n"
    "v_mfma_f32_32x32x16_bf16 v[144:159], a[224:227], a[128:131], v[144:159]\n"
    "ds_read_b64_tr_b16 a[252:253], v17 offset:14336\n"
    "ds_read_b64_tr_b16 a[254:255], v17 offset:15360\n"
    "v_exp_f32 v20, v20\n"
    "v_fmaak_f32 v21, v207, v207, 0x409d30fc\n"
    "v_mfma_f32_32x32x16_bf16 v[160:175], a[228:231], a[128:131], v[160:175]\n"
    "ds_read_b64_tr_b16 a[224:225], v16 offset:16384\n"
    "ds_read_b64_tr_b16 a[226:227], v16 offset:17408\n"
    "v_add_f32 v20, 1.0, v20\n"
    "v_rcp_f32 v20, v20\n"
    "v_mul_f32 v21, v21, v207\n"
    "v_mfma_f32_32x32x16_bf16 v[144:159], a[232:235], a[132:135], v[144:159]\n"
    "ds_read_b64_tr_b16 a[228:229], v17 offset:16384\n"
    "ds_read_b64_tr_b16 a[230:231], v17 offset:17408\n"
    "s_add_u32 m0, %[ldsw], 16384\n"
    "s_mov_b32 s40, 4719104\n"
    "v_mul_f32 v20, v20, v206\n"
    "v_exp_f32 v21, v21\n"
    "v_mfma_f32_32x32x16_bf16 v[160:175], a[236:239], a[132:135], v[160:175]\n"
    "s_waitcnt lgkmcnt(4)\n"
    "ds_read_b64_tr_b16 a[232:233], v16 offset:18432\n"
    "ds_read_b64_tr_b16 a[234:235], v16 offset:19456\n"
    "buffer_load_dwordx4 %[w1a], %[srd1], s40 offen lds\n"
    "s_nop 0\n"
    "v_add_f32 v21, 1.0, v21\n"
    "v_mfma_f32_32x32x16_bf16 v[144:159], a[240:243], a[136:139], v[144:159]\n"
    "ds_read_b64_tr_b16 a[236:237], v17 offset:18432\n"
    "ds_read_b64_tr_b16 a[238:239], v17 offset:19456\n"
    "v_rcp_f32 v21, v21\n"
    "s_nop 0\n"
    "v_mfma_f32_32x32x16_bf16 v[160:175], a[244:247], a[136:139], v[160:175]\n"
    "ds_read_b64_tr_b16 a[240:241], v16 offset:20480\n"
    "ds_read_b64_tr_b16 a[242:243], v16 offset:21504\n"
    "v_mul_f32 v21, v21, v207\n"
    "v_cvt_pk_bf16_f32 v79, v20, v21\n"
    "v_mfma_f32_32x32x16_bf16 v[144:159], a[248:251], a[140:143], v[144:159]\n"
    "ds_read_b64_tr_b16 a[244:245], v17 offset:20480\n"
    "ds_read_b64_tr_b16 a[246:247], v17 offset:21504\n"
    "v_mfma_f32_32x32x16_bf16 v[160:175], a[252:255], a[140:143], v[160:175]\n"
    "s_waitcnt lgkmcnt(4)\n"
    "ds_read_b64_tr_b16 a[248:249], v16 offset:22528\n"
    "ds_read_b64_tr_b16 a[250:251], v16 offset:23552\n"
    "buffer_load_dwordx4 %[w1b], %[srd1], s40 offen offset:1024 lds\n"
    "v_mfma_f32_32x32x16_bf16 v[208:223], a[224:227], v[64:67], v[208:223]\n"
    "ds_read_b64_tr_b16 a[252:253], v17 offset:22528\n"
    "ds_read_b64_tr_b16 a[254:255], v17 offset:23552\n"
    "v_mfma_f32_32x32x16_bf16 v[224:239], a[228:231], v[64:67], v[224:239]\n"
    "s_waitcnt vmcnt(18)\n"
    "s_barrier\n"
    "ds_read_b64_tr_b16 a[224:225], v16 offset:24576\n"
    "ds_read_b64_tr_b16 a[226:227], v16 offset:25600\n"
    "v_mfma_f32_32x32x16_bf16 v[208:223], a[232:235], v[68:71], v[208:223]\n"
    "ds_read_b64_tr_b16 a[228:229], v17 offset:24576\n"
    "ds_read_b64_tr_b16 a[230:231], v17 offset:25600\n"
    "s_add_u32 m0, %[ldsw], 24576\n"
    "s_mov_b32 s40, 221184\n"
    "ds_read_b128 v[176:179], %[vb] offset:1024\n"
    "v_mfma_f32_32x32x16_bf16 v[224:239], a[236:239], v[68:71], v[224:239]\n"
    "s_waitcnt lgkmcnt(5)\n"
    "ds_read_b64_tr_b16 a[232:233], v16 offset:26624\n"
    "ds_read_b64_tr_b16 a[234:235], v16 offset:27648\n"
    "buffer_load_dwordx4 %[w2a], %[srd2], s40 offen lds\n"
    "v_fmaak_f32 v20, v144, v144, 0x409d30fc\n"
    "v_mul_f32 v20, v20, v144\n"
    "v_exp_f32 v20, v20\n"
    "v_mfma_f32_32x32x16_bf16 v[208:223], a[240:243], v[72:75], v[208:223]\n"
    "ds_read_b64_tr_b16 a[236:237], v17 offset:26624\n"
    "ds_read_b64_tr_b16 a[238:239], v17 offset:27648\n"
    "v_fmaak_f32 v21, v145, v145, 0x409d30fc\n"
    "v_add_f32 v20, 1.0, v20\n"
    "v_mfma_f32_32x32x16_bf16 v[224:239], a[244:247], v[72:75], v[224:239]\n"
    "ds_read_b64_tr_b16 a[240:241], v16 offset:28672\n"
    "ds_read_b64_tr_b16 a[242:243], v16 offset:29696\n"
    "ds_read_b128 v[180:183], %[vb] offset:1056\n"
    "v_rcp_f32 v20, v20\n"
    "v_mul_f32 v21, v21, v145\n"
    "v_mfma_f32_32x32x16_bf16 v[208:223], a[248:251], v[76:79], v[208:223]\n"
    "ds_read_b64_tr_b16 a[244:245], v17 offset:28672\n"
    "ds_read_b64_tr_b16 a[246:247], v17 offset:29696\n"
    "v_mul_f32 v20, v20, v144\n"
    "v_exp_f32 v21, v21\n"
    "v_mfma_f32_32x32x16_bf16 v[224:239], a[252:255], v[76:79], v[224:239]\n"
    "s_waitcnt lgkmcnt(5)\n"
    "ds_read_b64_tr_b16 a[248:249], v16 offset:30720\n"
    "ds_read_b64_tr_b16 a[250:251], v16 offset:31744\n"
    "buffer_load_dwordx4 %[w2b], %[srd2], s40 offen offset:1024 lds\n"
    "v_fmaak_f32 v22, v146, v146, 0x409d30fc\n"
    "v_add_f32 v21, 1.0, v21\n"
    "v_rcp_f32 v21, v21\n"
    "v_mfma_f32_32x32x16_bf16 v[240:255], a[224:227], v[64:67], v[240:255]\n"
    "ds_read_b64_tr_b16 a[252:253], v17 offset:30720\n"
    "ds_read_b64_tr_b16 a[254:255], v17 offset:31744\n"
    "ds_read_b128 v[184:187], %[vb] offset:1088\n"
    "v_mul_f32 v22, v22, v146\n"
    "v_mul_f32 v21, v21, v145\n"
    "v_mfma_f32_32x32x16_bf16 a[144:159], a[228:231], v[64:67], a[144:159]\n"
    "ds_read_b64_tr_b16 a[224:225], v16 offset:32768\n"
    "ds_read_b64_tr_b16 a[226:227], v16 offset:33792\n"
    "v_cvt_pk_bf16_f32 v80, v20, v21\n"
    "v_exp_f32 v22, v22\n"
    "v_mfma_f32_32x32x16_bf16 v[240:255], a[232:235], v[68:71], v[240:255]\n"
    "ds_read_b64_tr_b16 a[228:229], v17 offset:32768\n"
    "ds_read_b64_tr_b16 a[230:231], v17 offset:33792\n"
    "s_add_u32 m0, %[ldsw], 32768\n"
    "s_mov_b32 s40, 221312\n"
    "v_fmaak_f32 v20, v147, v147, 0x409d30fc\n"
    "v_add_f32 v22, 1.0, v22\n"
    "v_mfma_f32_32x32x16_bf16 a[144:159], a[236:239], v[68:71], a[144:159]\n"
    "s_waitcnt lgkmcnt(5)\n"
    "ds_read_b64_tr_b16 a[232:233], v16 offset:34816\n"
    "ds_read_b64_tr_b16 a[234:235], v16 offset:35840\n"
    "buffer_load_dwordx4 %[w2a], %[srd2], s40 offen lds\n"
    "ds_read_b128 v[188:191], %[vb] offset:1120\n"
    "v_rcp_f32 v22, v22\n"
    "v_mul_f32 v20, v20, v147\n"
    "v_mfma_f32_32x32x16_bf16 v[240:255], a[240:243], v[72:75], v[240:255]\n"
    "ds_read_b64_tr_b16 a[236:237], v17 offset:34816\n"
    "ds_read_b64_tr_b16 a[238:239], v17 offset:35840\n"
    "v_mul_f32 v22, v22, v146\n"
    "v_exp_f32 v20, v20\n"
    "v_fmaak_f32 v21, v148, v148, 0x409d30fc\n"
    "v_mfma_f32_32x32x16_bf16 a[144:159], a[244:247], v[72:75], a[144:159]\n"
    "ds_read_b64_tr_b16 a[240:241], v16 offset:36864\n"
    "ds_read_b64_tr_b16 a[242:243], v16 offset:37888\n"
    "v_add_f32 v20, 1.0, v20\n"
    "v_rcp_f32 v20, v20\n"
    "v_mfma_f32_32x32x16_bf16 v[240:255], a[248:251], v[76:79], v[240:255]\n"
    "ds_read_b64_tr_b16 a[244:245], v17 offset:36864\n"
    "ds_read_b64_tr_b16 a[246:247], v17 offset:37888\n"
    "ds_read_b128 v[192:195], %[vb] offset:1152\n"
    "v_mul_f32 v21, v21, v148\n"
    "v_mul_f32 v20, v20, v147\n"
    "v_mfma_f32_32x32x16_bf16 a[144:159], a[252:255], v[76:79], a[144:159]\n"
    "s_waitcnt lgkmcnt(5)\n"
    "ds_read_b64_tr_b16 a[248:249], v16 offset:38912\n"
    "ds_read_b64_tr_b16 a[250:251], v16 offset:39936\n"
    "buffer_load_dwordx4 %[w2b], %[srd2], s40 offen offset:1024 lds\n"
    "v_cvt_pk_bf16_f32 v81, v22, v20\n"
    "v_exp_f32 v21, v21\n"
    "v_mfma_f32_32x32x16_bf16 a[160:175], a[224:227], v[64:67], a[160:175]\n"
    "ds_read_b64_tr_b16 a[252:253], v17 offset:38912\n"
    "ds_read_b64_tr_b16 a[254:255], v17 offset:39936\n"
    "v_fmaak_f32 v22, v149, v149, 0x409d30fc\n"
    "v_add_f32 v21, 1.0, v21\n"
    "v_mfma_f32_32x32x16_bf16 a[176:191], a[228:231], v[64:67], a[176:191]\n"
    "ds_read_b64_tr_b16 a[224:225], v16 offset:40960\n"
    "ds_read_b64_tr_b16 a[226:227], v16 offset:41984\n"
    "ds_read_b128 v[196:199], %[vb] offset:1184\n"
    "v_rcp_f32 v21, v21\n"
    "v_mul_f32 v22, v22, v149\n"
    "v_mul_f32 v21, v21, v148\n"
    "v_mfma_f32_32x32x16_bf16 a[160:175], a[232:235], v[68:71], a[160:175]\n"
    "ds_read_b64_tr_b16 a[228:229], v17 offset:40960\n"
    "ds_read_b64_tr_b16 a[230:231], v17 offset:41984\n"
    "s_add_u32 m0, %[ldsw], 40960\n"
    "s_mov_b32 s40, 221440\n"
    "v_exp_f32 v22, v22\n"
    "v_fmaak_f32 v20, v150, v150, 0x409d30fc\n"
    "v_mfma_f32_32x32x16_bf16 a[176:191], a[236:239], v[68:71], a[176:191]\n"
    "s_waitcnt lgkmcnt(5)\n"
    "ds_read_b64_tr_b16 a[232:233], v16 offset:43008\n"
    "ds_read_b64_tr_b16 a[234:235], v16 offset:44032\n"
    "buffer_load_dwordx4 %[w2a], %[srd2], s40 offen lds\n"
    "v_add_f32 v22, 1.0, v22\n"
    "v_rcp_f32 v22, v22\n"
    "v_mfma_f32_32x32x16_bf16 a[160:175], a[240:243], v[72:75], a[160:175]\n"
    "ds_read_b64_tr_b16 a[236:237], v17 offset:43008\n"
    "ds_read_b64_tr_b16 a[238:239], v17 offset:44032\n"
    "ds_read_b128 v[200:203], %[vb] offset:1216\n"
    "v_mul_f32 v20, v20, v150\n"
    "v_mul_f32 v22, v22, v149\n"
    "v_mfma_f32_32x32x16_bf16 a[176:191], a[244:247], v[72:75], a[176:191]\n"
    "ds_read_b64_tr_b16 a[240:241], v16 offset:45056\n"
    "ds_read_b64_tr_b16 a[242:243], v16 offset:46080\n"
    "v_cvt_pk_bf16_f32 v82, v21, v22\n"
    "v_exp_f32 v20, v20\n"
    "v_mfma_f32_32x32x16_bf16 a[160:175], a[248:251], v[76:79], a[160:175]\n"
    "ds_read_b64_tr_b16 a[244:245], v17 offset:45056\n"
    "ds_read_b64_tr_b16 a[246:247], v17 offset:46080\n"
    "v_fmaak_f32 v21, v151, v151, 0x409d30fc\n"
    "v_add_f32 v20, 1.0, v20\n"
    "v_rcp_f32 v20, v20\n"
    "v_mfma_f32_32x32x16_bf16 a[176:191], a[252:255], v[76:79], a[176:191]\n"
    "s_waitcnt lgkmcnt(5)\n"
    "ds_read_b64_tr_b16 a[248:249], v16 offset:47104\n"
    "ds_read_b64_tr_b16 a[250:251], v16 offset:48128\n"
    "buffer_load_dwordx4 %[w2b], %[srd2], s40 offen offset:1024 lds\n"
    "ds_read_b128 v[204:207], %[vb] offset:1248\n"
    "v_mul_f32 v21, v21, v151\n"
    "v_mul_f32 v20, v20, v150\n"
    "v_mfma_f32_32x32x16_bf16 a[192:207], a[224:227], v[64:67], a[192:207]\n"
    "ds_read_b64_tr_b16 a[252:253], v17 offset:47104\n"
    "ds_read_b64_tr_b16 a[254:255], v17 offset:48128\n"
    "v_exp_f32 v21, v21\n"
    "v_fmaak_f32 v22, v152, v152, 0x409d30fc\n"
    "v_mfma_f32_32x32x16_bf16 a[208:223], a[228:231], v[64:67], a[208:223]\n"
    "s_waitcnt vmcnt(18)\n"
    "s_barrier\n"
    "ds_read_b64_tr_b16 a[224:225], v18 offset:0\n"
    "ds_read_b64_tr_b16 a[226:227], v18 offset:1024\n"
    "v_mfma_f32_32x32x16_bf16 a[192:207], a[232:235], v[68:71], a[192:207]\n"
    "ds_read_b64_tr_b16 a[228:229], v19 offset:0\n"
    "ds_read_b64_tr_b16 a[230:231], v19 offset:1024\n"
    "s_add_u32 m0, %[ldsw], 49152\n"
    "s_mov_b32 s40, 221568\n"
    "v_add_f32 v21, 1.0, v21\n"
    "v_rcp_f32 v21, v21\n"
    "v_mfma_f32_32x32x16_bf16 a[208:223], a[236:239], v[68:71], a[208:223]\n"
    "s_waitcnt lgkmcnt(4)\n"
    "ds_read_b64_tr_b16 a[232:233], v18 offset:2048\n"
    "ds_read_b64_tr_b16 a[234:235], v18 offset:3072\n"
    "buffer_load_dwordx4 %[w2a], %[srd2], s40 offen lds\n"
    "v_mul_f32 v22, v22, v152\n"
    "v_mul_f32 v21, v21, v151\n"
    "v_cvt_pk_bf16_f32 v83, v20, v21\n"
    "v_mfma_f32_32x32x16_bf16 a[192:207], a[240:243], v[72:75], a[192:207]\n"
    "ds_read_b64_tr_b16 a[236:237], v19 offset:2048\n"
    "ds_read_b64_tr_b16 a[238:239], v19 offset:3072\n"
    "v_exp_f32 v22, v22\n"
    "v_fmaak_f32 v20, v153, v153, 0x409d30fc\n"
    "v_mfma_f32_32x32x16_bf16 a[208:223], a[244:247], v[72:75], a[208:223]\n"
    "ds_read_b64_tr_b16 a[240:241], v18 offset:4096\n"
    "ds_read_b64_tr_b16 a[242:243], v18 offset:5120\n"
    "v_add_f32 v22, 1.0, v22\n"
    "v_rcp_f32 v22, v22\n"
    "v_mfma_f32_32x32x16_bf16 a[192:207], a[248:251], v[76:79], a[192:207]\n"
    "ds_read_b64_tr_b16 a[244:245], v19 offset:4096\n"
    "ds_read_b64_tr_b16 a[246:247], v19 offset:5120\n"
    "v_mul_f32 v20, v20, v153\n"
    "v_mul_f32 v22, v22, v152\n"
    "v_mfma_f32_32x32x16_bf16 a[208:223], a[252:255], v[76:79], a[208:223]\n"
    "s_waitcnt lgkmcnt(4)\n"
    "ds_read_b64_tr_b16 a[248:249], v18 offset:6144\n"
    "ds_read_b64_tr_b16 a[250:251], v18 offset:7168\n"
    "buffer_load_dwordx4 %[w2b], %[srd2], s40 offen offset:1024 lds\n"
    "v_exp_f32 v20, v20\n"
    "v_fmaak_f32 v21, v154, v154, 0x409d30fc\n"
    "v_mfma_f32_32x32x16_bf16 v[176:191], a[224:227], a[0:3], v[176:191]\n"
    "ds_read_b64_tr_b16 a[252:253], v19 offset:6144\n"
    "ds_read_b64_tr_b16 a[254:255], v19 offset:7168\n"
    "v_add_f32 v20, 1.0, v20\n"
    "v_rcp_f32 v20, v20\n"
    "v_mul_f32 v21, v21, v154\n"
    "v_mfma_f32_32x32x16_bf16 v[192:207], a[228:231], a[0:3], v[192:207]\n"
    "ds_read_b64_tr_b16 a[224:225], v18 offset:8192\n"
    "ds_read_b64_tr_b16 a[226:227], v18 offset:9216\n"
    "v_mul_f32 v20, v20, v153\n"
    "v_cvt_pk_bf16_f32 v84, v22, v20\n"
    "v_mfma_f32_32x32x16_bf16 v[176:191], a[232:235], a[4:7], v[176:191]\n"
    "ds_read_b64_tr_b16 a[228:229], v19 offset:8192\n"
    "ds_read_b64_tr_b16 a[230:231], v19 offset:9216\n"
    "s_add_u32 m0, %[ldsw], 57344\n"
    "s_mov_b32 s40, 640\n"
    "v_exp_f32 v21, v21\n"
    "v_fmaak_f32 v22, v155, v155, 0x409d30fc\n"
    "v_mfma_f32_32x32x16_bf16 v[192:207], a[236:239], a[4:7], v[192:207]\n"
    "s_waitcnt lgkmcnt(4)\n"
    "ds_read_b64_tr_b16 a[232:233], v18 offset:10240\n"
    "ds_read_b64_tr_b16 a[234:235], v18 offset:11264\n"
    "buffer_load_dwordx4 %[w1a], %[srd1], s40 offen lds\n"
    "v_add_f32 v21, 1.0, v21\n"
    "v_rcp_f32 v21, v21\n"
    "v_mfma_f32_32x32x16_bf16 v[176:191], a[240:243], a[8:11], v[176:191]\n"
    "ds_read_b64_tr_b16 a[236:237], v19 offset:10240\n"
    "ds_read_b64_tr_b16 a[238:239], v19 offset:11264\n"
    "v_mul_f32 v22, v22, v155\n"
    "v_mul_f32 v21, v21, v154\n"
    "v_mfma_f32_32x32x16_bf16 v[192:207], a[244:247], a[8:11], v[192:207]\n"
    "ds_read_b64_tr_b16 a[240:241], v18 offset:12288\n"
    "ds_read_b64_tr_b16 a[242:243], v18 offset:13312\n"
    "v_exp_f32 v22, v22\n"
    "v_fmaak_f32 v20, v156, v156, 0x409d30fc\n"
    "v_add_f32 v22, 1.0, v22\n"
    "v_mfma_f32_32x32x16_bf16 v[176:191], a[248:251], a[12:15], v[176:191]\n"
    "ds_read_b64_tr_b16 a[244:245], v19 offset:12288\n"
    "ds_read_b64_tr_b16 a[246:247], v19 offset:13312\n"
    "v_rcp_f32 v22, v22\n"
    "v_mul_f32 v20, v20, v156\n"
    "v_mfma_f32_32x32x16_bf16 v[192:207], a[252:255], a[12:15], v[192:207]\n"
    "s_waitcnt lgkmcnt(4)\n"
    "ds_read_b64_tr_b16 a[248:249], v18 offset:14336\n"
    "ds_read_b64_tr_b16 a[250:251], v18 offset:15360\n"
    "buffer_load_dwordx4 %[w1b], %[srd1], s40 offen offset:1024 lds\n"
    "v_mul_f32 v22, v22, v155\n"
    "v_cvt_pk_bf16_f32 v85, v21, v22\n"
    "v_mfma_f32_32x32x16_bf16 v[176:191], a[224:227], a[16:19], v[176:191]\n"
    "ds_read_b64_tr_b16 a[252:253], v19 offset:14336\n"
    "ds_read_b64_tr_b16 a[254:255], v19 offset:15360\n"
    "v_exp_f32 v20, v20\n"
    "v_fmaak_f32 v21, v157, v157, 0x409d30fc\n"
    "v_mfma_f32_32x32x16_bf16 v[192:207], a[228:231], a[16:19], v[192:207]\n"
    "ds_read_b64_tr_b16 a[224:225], v18 offset:16384\n"
    "ds_read_b64_tr_b16 a[226:227], v18 offset:17408\n"
    "v_add_f32 v20, 1.0, v20\n"
    "v_rcp_f32 v20, v20\n"
    "v_mfma_f32_32x32x16_bf16 v[176:191], a[232:235], a[20:23], v[176:191]\n"
    "ds_read_b64_tr_b16 a[228:229], v19 offset:16384\n"
    "ds_read_b64_tr_b16 a[230:231], v19 offset:17408\n"
    "s_add_u32 m0, %[ldsw], 65536\n"
    "s_mov_b32 s40, 590464\n"
    "v_mul_f32 v21, v21, v157\n"
    "v_mul_f32 v20, v20, v156\n"
    "v_exp_f32 v21, v21\n"
    "v_mfma_f32_32x32x16_bf16 v[192:207], a[236:239], a[20:23], v[192:207]\n"
    "s_waitcnt lgkmcnt(4)\n"
    "ds_read_b64_tr_b16 a[232:233], v18 offset:18432\n"
    "ds_read_b64_tr_b16 a[234:235], v18 offset:19456\n"
    "buffer_load_dwordx4 %[w1a], %[srd1], s40 offen lds\n"
    "v_fmaak_f32 v22, v158, v158, 0x409d30fc\n"
    "v_add_f32 v21, 1.0, v21\n"
    "v_mfma_f32_32x32x16_bf16 v[176:191], a[240:243], a[24:27], v[176:191]\n"
    "ds_read_b64_tr_b16 a[236:237], v19 offset:18432\n"
    "ds_read_b64_tr_b16 a[238:239], v19 offset:19456\n"
    "v_rcp_f32 v21, v21\n"
    "v_mul_f32 v22, v22, v158\n"
    "v_mfma_f32_32x32x16_bf16 v[192:207], a[244:247], a[24:27], v[192:207]\n"
    "ds_read_b64_tr_b16 a[240:241], v18 offset:20480\n"
    "ds_read_b64_tr_b16 a[242:243], v18 offset:21504\n"
    "v_mul_f32 v21, v21, v157\n"
    "v_cvt_pk_bf16_f32 v86, v20, v21\n"
    "v_mfma_f32_32x32x16_bf16 v[176:191], a[248:251], a[28:31], v[176:191]\n"
    "ds_read_b64_tr_b16 a[244:245], v19 offset:20480\n"
    "ds_read_b64_tr_b16 a[246:247], v19 offset:21504\n"
    "v_exp_f32 v22, v22\n"
    "v_fmaak_f32 v20, v159, v159, 0x409d30fc\n"
    "v_add_f32 v22, 1.0, v22\n"
    "v_mfma_f32_32x32x16_bf16 v[192:207], a[252:255], a[28:31], v[192:207]\n"
    "s_waitcnt lgkmcnt(4)\n"
    "ds_read_b64_tr_b16 a[248:249], v18 offset:22528\n"
    "ds_read_b64_tr_b16 a[250:251], v18 offset:23552\n"
    "buffer_load_dwordx4 %[w1b], %[srd1], s40 offen offset:1024 lds\n"
    "v_rcp_f32 v22, v22\n"
    "v_mul_f32 v20, v20, v159\n"
    "v_mfma_f32_32x32x16_bf16 v[176:191], a[224:227], a[32:35], v[176:191]\n"
    "ds_read_b64_tr_b16 a[252:253], v19 offset:22528\n"
    "ds_read_b64_tr_b16 a[254:255], v19 offset:23552\n"
    "v_mul_f32 v22, v22, v158\n"
    "v_exp_f32 v20, v20\n"
    "v_mfma_f32_32x32x16_bf16 v[192:207], a[228:231], a[32:35], v[192:207]\n"
    "s_waitcnt vmcnt(18)\n"
    "s_barrier\n"
    "ds_read_b64_tr_b16 a[224:225], v18 offset:24576\n"
    "ds_read_b64_tr_b16 a[226:227], v18 offset:25600\n"
    "v_mfma_f32_32x32x16_bf16 v[176:191], a[232:235], a[36:39], v[176:191]\n"
    "ds_read_b64_tr_b16 a[228:229], v19 offset:24576\n"
    "ds_read_b64_tr_b16 a[230:231], v19 offset:25600\n"
    "s_add_u32 m0, %[ldsw], 73728\n"
    "s_mov_b32 s40, 1180288\n"
    "v_fmaak_f32 v21, v160, v160, 0x409d30fc\n"
    "v_add_f32 v20, 1.0, v20\n"
    "v_mfma_f32_32x32x16_bf16 v[192:207], a[236:239], a[36:39], v[192:207]\n"
    "s_waitcnt lgkmcnt(4)\n"
    "ds_read_b64_tr_b16 a[232:233], v18 offset:26624\n"
    "ds_read_b64_tr_b16 a[234:235], v18 offset:27648\n"
    "buffer_load_dwordx4 %[w1a], %[srd1], s40 offen lds\n"
    "v_rcp_f32 v20, v20\n"
    "v_mul_f32 v21, v21, v160\n"
    "v_mfma_f32_32x32x16_bf16 v[176:191], a[240:243], a[40:43], v[176:191]\n"
    "ds_read_b64_tr_b16 a[236:237], v19 offset:26624\n"
    "ds_read_b64_tr_b16 a[238:239], v19 offset:27648\n"
    "v_mul_f32 v20, v20, v159\n"
    "v_cvt_pk_bf16_f32 v87, v22, v20\n"
    "v_exp_f32 v21, v21\n"
    "v_mfma_f32_32x32x16_bf16 v[192:207], a[244:247], a[40:43], v[192:207]\n"
    "ds_read_b64_tr_b16 a[240:241], v18 offset:28672\n"
    "ds_read_b64_tr_b16 a[242:243], v18 offset:29696\n"
    "v_fmaak_f32 v22, v161, v161, 0x409d30fc\n"
    "v_add_f32 v21, 1.0, v21\n"
    "v_mfma_f32_32x32x16_bf16 v[176:191], a[248:251], a[44:47], v[176:191]\n"
    "ds_read_b64_tr_b16 a[244:245], v19 offset:28672\n"
    "ds_read_b64_tr_b16 a[246:247], v19 offset:29696\n"
    "v_rcp_f32 v21, v21\n"
    "v_mul_f32 v22, v22, v161\n"
    "v_mfma_f32_32x32x16_bf16 v[192:207], a[252:255], a[44:47], v[192:207]\n"
    "s_waitcnt lgkmcnt(4)\n"
    "ds_read_b64_tr_b16 a[248:249], v18 offset:30720\n"
    "ds_read_b64_tr_b16 a[250:251], v18 offset:31744\n"
    "buffer_load_dwordx4 %[w1b], %[srd1], s40 offen offset:1024 lds\n"
    "v_mul_f32 v21, v21, v160\n"
    "v_exp_f32 v22, v22\n"
    "v_mfma_f32_32x32x16_bf16 v[176:191], a[224:227], a[48:51], v[176:191]\n"
    "ds_read_b64_tr_b16 a[252:253], v19 offset:30720\n"
    "ds_read_b64_tr_b16 a[254:255], v19 offset:31744\n"
    "v_fmaak_f32 v20, v162, v162, 0x409d30fc\n"
    "v_add_f32 v22, 1.0, v22\n"
    "v_mfma_f32_32x32x16_bf16 v[192:207], a[228:231], a[48:51], v[192:207]\n"
    "ds_read_b64_tr_b16 a[224:225], v18 offset:32768\n"
    "ds_read_b64_tr_b16 a[226:227], v18 offset:33792\n"
    "v_rcp_f32 v22, v22\n"
    "v_mul_f32 v20, v20, v162\n"
    "v_mul_f32 v22, v22, v161\n"
    "v_mfma_f32_32x32x16_bf16 v[176:191], a[232:235], a[52:55], v[176:191]\n"
    "ds_read_b64_tr_b16 a[228:229], v19 offset:32768\n"
    "ds_read_b64_tr_b16 a[230:231], v19 offset:33792\n"
    "s_add_u32 m0, %[ldsw], 81920\n"
    "s_mov_b32 s40, 1770112\n"
    "v_cvt_pk_bf16_f32 v88, v21, v22\n"
    "v_exp_f32 v20, v20\n"
    "v_mfma_f32_32x32x16_bf16 v[192:207], a[236:239], a[52:55], v[192:207]\n"
    "s_waitcnt lgkmcnt(4)\n"
    "ds_read_b64_tr_b16 a[232:233], v18 offset:34816\n"
    "ds_read_b64_tr_b16 a[234:235], v18 offset:35840\n"
    "buffer_load_dwordx4 %[w1a], %[srd1], s40 offen lds\n"
    "v_fmaak_f32 v21, v163, v163, 0x409d30fc\n"
    "v_add_f32 v20, 1.0, v20\n"
    "v_mfma_f32_32x32x16_bf16 v[176:191], a[240:243], a[56:59], v[176:191]\n"
    "ds_read_b64_tr_b16 a[236:237], v19 offset:34816\n"
    "ds_read_b64_tr_b16 a[238:239], v19 offset:35840\n"
    "v_rcp_f32 v20, v20\n"
    "v_mul_f32 v21, v21, v163\n"
    "v_mfma_f32_32x32x16_bf16 v[192:207], a[244:247], a[56:59], v[192:207]\n"
    "ds_read_b64_tr_b16 a[240:241], v18 offset:36864\n"
    "ds_read_b64_tr_b16 a[242:243], v18 offset:37888\n"
    "v_mul_f32 v20, v20, v162\n"
    "v_exp_f32 v21, v21\n"
    "v_mfma_f32_32x32x16_bf16 v[176:191], a[248:251], a[60:63], v[176:191]\n"
    "ds_read_b64_tr_b16 a[244:245], v19 offset:36864\n"
    "ds_read_b64_tr_b16 a[246:247], v19 offset:37888\n"
    "v_fmaak_f32 v22, v164, v164, 0x409d30fc\n"
    "v_add_f32 v21, 1.0, v21\n"
    "v_rcp_f32 v21, v21\n"
    "v_mfma_f32_32x32x16_bf16 v[192:207], a[252:255], a[60:63], v[192:207]\n"
    "s_waitcnt lgkmcnt(4)\n"
    "ds_read_b64_tr_b16 a[248:249], v18 offset:38912\n"
    "ds_read_b64_tr_b16 a[250:251], v18 offset:39936\n"
    "buffer_load_dwordx4 %[w1b], %[srd1], s40 offen offset:1024 lds\n"
    "v_mul_f32 v22, v22, v164\n"
    "v_mul_f32 v21, v21, v163\n"
    "v_mfma_f32_32x32x16_bf16 v[176:191], a[224:227], a[64:67], v[176:191]\n"
    "ds_read_b64_tr_b16 a[252:253], v19 offset:38912\n"
    "ds_read_b64_tr_b16 a[254:255], v19 offset:39936\n"
    "v_cvt_pk_bf16_f32 v89, v20, v21\n"
    "v_exp_f32 v22, v22\n"
    "v_mfma_f32_32x32x16_bf16 v[192:207], a[228:231], a[64:67], v[192:207]\n"
    "ds_read_b64_tr_b16 a[224:225], v18 offset:40960\n"
    "ds_read_b64_tr_b16 a[226:227], v18 offset:41984\n"
    "v_fmaak_f32 v20, v165, v165, 0x409d30fc\n"
    "v_add_f32 v22, 1.0, v22\n"
    "v_mfma_f32_32x32x16_bf16 v[176:191], a[232:235], a[68:71], v[176:191]\n"
    "ds_read_b64_tr_b16 a[228:229], v19 offset:40960\n"
    "ds_read_b64_tr_b16 a[230:231], v19 offset:41984\n"
    "s_add_u32 m0, %[ldsw], 90112\n"
    "s_mov_b32 s40, 2359936\n"
    "v_rcp_f32 v22, v22\n"
    "v_mul_f32 v20, v20, v165\n"
    "v_mul_f32 v22, v22, v164\n"
    "v_mfma_f32_32x32x16_bf16 v[192:207], a[236:239], a[68:71], v[192:207]\n"
    "s_waitcnt lgkmcnt(4)\n"
    "ds_read_b64_tr_b16 a[232:233], v18 offset:43008\n"
    "ds_read_b64_tr_b16 a[234:235], v18 offset:44032\n"
    "buffer_load_dwordx4 %[w1a], %[srd1], s40 offen lds\n"
    "v_exp_f32 v20, v20\n"
    "v_fmaak_f32 v21, v166, v166, 0x409d30fc\n"
    "v_mfma_f32_32x32x16_bf16 v[176:191], a[240:243], a[72:75], v[176:191]\n"
    "ds_read_b64_tr_b16 a[236:237], v19 offset:43008\n"
    "ds_read_b64_tr_b16 a[238:239], v19 offset:44032\n"
    "v_add_f32 v20, 1.0, v20\n"
    "v_rcp_f32 v20, v20\n"
    "v_mfma_f32_32x32x16_bf16 v[192:207], a[244:247], a[72:75], v[192:207]\n"
    "ds_read_b64_tr_b16 a[240:241], v18 offset:45056\n"
    "ds_read_b64_tr_b16 a[242:243], v18 offset:46080\n"
    "v_mul_f32 v21, v21, v166\n"
    "v_mul_f32 v20, v20, v165\n"
    "v_mfma_f32_32x32x16_bf16 v[176:191], a[248:251], a[76:79], v[176:191]\n"
    "ds_read_b64_tr_b16 a[244:245], v19 offset:45056\n"
    "ds_read_b64_tr_b16 a[246:247], v19 offset:46080\n"
    "v_cvt_pk_bf16_f32 v90, v22, v20\n"
    "v_exp_f32 v21, v21\n"
    "v_mfma_f32_32x32x16_bf16 v[192:207], a[252:255], a[76:79], v[192:207]\n"
    "s_waitcnt lgkmcnt(4)\n"
    "ds_read_b64_tr_b16 a[248:249], v18 offset:47104\n"
    "ds_read_b64_tr_b16 a[250:251], v18 offset:48128\n"
    "buffer_load_dwordx4 %[w1b], %[srd1], s40 offen offset:1024 lds\n"
    "v_fmaak_f32 v22, v167, v167, 0x409d30fc\n"
    "v_add_f32 v21, 1.0, v21\n"
    "v_rcp_f32 v21, v21\n"
    "v_mfma_f32_32x32x16_bf16 v[176:191], a[224:227], a[80:83], v[176:191]\n"
    "ds_read_b64_tr_b16 a[252:253], v19 offset:47104\n"
    "ds_read_b64_tr_b16 a[254:255], v19 offset:48128\n"
    "v_mul_f32 v22, v22, v167\n"
    "v_mul_f32 v21, v21, v166\n"
    "v_mfma_f32_32x32x16_bf16 v[192:207], a[228:231], a[80:83], v[192:207]\n"
    "s_waitcnt vmcnt(18)\n"
    "s_barrier\n"
    "ds_read_b64_tr_b16 a[224:225], %[rd0] offset:0\n"
    "ds_read_b64_tr_b16 a[226:227], %[rd0] offset:1024\n"
    "v_mfma_f32_32x32x16_bf16 v[176:191], a[232:235], a[84:87], v[176:191]\n"
    "ds_read_b64_tr_b16 a[228:229], %[rd1] offset:0\n"
    "ds_read_b64_tr_b16 a[230:231], %[rd1] offset:1024\n"
    "s_add_u32 m0, %[ldsw], 98304\n"
    "s_mov_b32 s40, 2949760\n"
    "v_exp_f32 v22, v22\n"
    "v_fmaak_f32 v20, v168, v168, 0x409d30fc\n"
    "v_mfma_f32_32x32x16_bf16 v[192:207], a[236:239], a[84:87], v[192:207]\n"
    "s_waitcnt lgkmcnt(4)\n"
    "ds_read_b64_tr_b16 a[232:233], %[rd0] offset:2048\n"
    "ds_read_b64_tr_b16 a[234:235], %[rd0] offset:3072\n"
    "buffer_load_dwordx4 %[w1a], %[srd1], s40 offen lds\n"
    "v_add_f32 v22, 1.0, v22\n"
    "v_rcp_f32 v22, v22\n"
    "v_mfma_f32_32x32x16_bf16 v[176:191], a[240:243], a[88:91], v[176:191]\n"
    "ds_read_b64_tr_b16 a[236:237], %[rd1] offset:2048\n"
    "ds_read_b64_tr_b16 a[238:239], %[rd1] offset:3072\n"
    "v_mul_f32 v20, v20, v168\n"
    "v_mul_f32 v22, v22, v167\n"
    "v_mfma_f32_32x32x16_bf16 v[192:207], a[244:247], a[88:91], v[192:207]\n"
    "ds_read_b64_tr_b16 a[240:241], %[rd0] offset:4096\n"
    "ds_read_b64_tr_b16 a[242:243], %[rd0] offset:5120\n"
    "v_cvt_pk_bf16_f32 v91, v21, v22\n"
    "v_exp_f32 v20, v20\n"
    "v_fmaak_f32 v21, v169, v169, 0x409d30fc\n"
    "v_mfma_f32_32x32x16_bf16 v[176:191], a[248:251], a[92:95], v[176:191]\n"
    "ds_read_b64_tr_b16 a[244:245], %[rd1] offset:4096\n"
    "ds_read_b64_tr_b16 a[246:247], %[rd1] offset:5120\n"
    "v_add_f32 v20, 1.0, v20\n"
    "v_rcp_f32 v20, v20\n"
    "v_mfma_f32_32x32x16_bf16 v[192:207], a[252:255], a[92:95], v[192:207]\n"
    "s_waitcnt lgkmcnt(4)\n"
    "ds_read_b64_tr_b16 a[248:249], %[rd0] offset:6144\n"
    "ds_read_b64_tr_b16 a[250:251], %[rd0] offset:7168\n"
    "buffer_load_dwordx4 %[w1b], %[srd1], s40 offen offset:1024 lds\n"
    "v_mul_f32 v21, v21, v169\n"
    "v_mul_f32 v20, v20, v168\n"
    "v_mfma_f32_32x32x16_bf16 v[176:191], a[224:227], a[96:99], v[176:191]\n"
    "ds_read_b64_tr_b16 a[252:253], %[rd1] offset:6144\n"
    "ds_read_b64_tr_b16 a[254:255], %[rd1] offset:7168\n"
    "v_exp_f32 v21, v21\n"
    "v_fmaak_f32 v22, v170, v170, 0x409d30fc\n"
    "v_mfma_f32_32x32x16_bf16 v[192:207], a[228:231], a[96:99], v[192:207]\n"
    "ds_read_b64_tr_b16 a[224:225], %[rd0] offset:8192\n"
    "ds_read_b64_tr_b16 a[226:227], %[rd0] offset:9216\n"
    "v_add_f32 v21, 1.0, v21\n"
    "v_rcp_f32 v21, v21\n"
    "v_mfma_f32_32x32x16_bf16 v[176:191], a[232:235], a[100:103], v[176:191]\n"
    "ds_read_b64_tr_b16 a[228:229], %[rd1] offset:8192\n"
    "ds_read_b64_tr_b16 a[230:231], %[rd1] offset:9216\n"
    "s_add_u32 m0, %[ldsw], 106496\n"
    "s_mov_b32 s40, 3539584\n"
    "v_mul_f32 v22, v22, v170\n"
    "v_mul_f32 v21, v21, v169\n"
    "v_cvt_pk_bf16_f32 v92, v20, v21\n"
    "v_mfma_f32_32x32x16_bf16 v[192:207], a[236:239], a[100:103], v[192:207]\n"
    "s_waitcnt lgkmcnt(4)\n"
    "ds_read_b64_tr_b16 a[232:233], %[rd0] offset:10240\n"
    "ds_read_b64_tr_b16 a[234:235], %[rd0] offset:11264\n"
    "buffer_load_dwordx4 %[w1a], %[srd1], s40 offen lds\n"
    "v_exp_f32 v22, v22\n"
    "v_fmaak_f32 v20, v171, v171, 0x409d30fc\n"
    "v_mfma_f32_32x32x16_bf16 v[176:191], a[240:243], a[104:107], v[176:191]\n"
    "ds_read_b64_tr_b16 a[236:237], %[rd1] offset:10240\n"
    "ds_read_b64_tr_b16 a[238:239], %[rd1] offset:11264\n"
    "v_add_f32 v22, 1.0, v22\n"
    "v_rcp_f32 v22, v22\n"
    "v_mfma_f32_32x32x16_bf16 v[192:207], a[244:247], a[104:107], v[192:207]\n"
    "ds_read_b64_tr_b16 a[240:241], %[rd0] offset:12288\n"
    "ds_read_b64_tr_b16 a[242:243], %[rd0] offset:13312\n"
    "v_mul_f32 v20, v20, v171\n"
    "v_mul_f32 v22, v22, v170\n"
    "v_mfma_f32_32x32x16_bf16 v[176:191], a[248:251], a[108:111], v[176:191]\n"
    "ds_read_b64_tr_b16 a[244:245], %[rd1] offset:12288\n"
    "ds_read_b64_tr_b16 a[246:247], %[rd1] offset:13312\n"
    "v_exp_f32 v20, v20\n"
    "v_fmaak_f32 v21, v172, v172, 0x409d30fc\n"
    "v_add_f32 v20, 1.0, v20\n"
    "v_mfma_f32_32x32x16_bf16 v[192:207], a[252:255], a[108:111], v[192:207]\n"
    "s_waitcnt lgkmcnt(4)\n"
    "ds_read_b64_tr_b16 a[248:249], %[rd0] offset:14336\n"
    "ds_read_b64_tr_b16 a[250:251], %[rd0] offset:15360\n"
    "buffer_load_dwordx4 %[w1b], %[srd1], s40 offen offset:1024 lds\n"
    "v_rcp_f32 v20, v20\n"
    "v_mul_f32 v21, v21, v172\n"
    "v_mfma_f32_32x32x16_bf16 v[176:191], a[224:227], a[112:115], v[176:191]\n"
    "ds_read_b64_tr_b16 a[252:253], %[rd1] offset:14336\n"
    "ds_read_b64_tr_b16 a[254:255], %[rd1] offset:15360\n"
    "v_mul_f32 v20, v20, v171\n"
    "v_cvt_pk_bf16_f32 v93, v22, v20\n"
    "v_mfma_f32_32x32x16_bf16 v[192:207], a[228:231], a[112:115], v[192:207]\n"
    "ds_read_b64_tr_b16 a[224:225], %[rd0] offset:16384\n"
    "ds_read_b64_tr_b16 a[226:227], %[rd0] offset:17408\n"
    "v_exp_f32 v21, v21\n"
    "v_fmaak_f32 v22, v173, v173, 0x409d30fc\n"
    "v_mfma_f32_32x32x16_bf16 v[176:191], a[232:235], a[116:119], v[176:191]\n"
    "ds_read_b64_tr_b16 a[228:229], %[rd1] offset:16384\n"
    "ds_read_b64_tr_b16 a[230:231], %[rd1] offset:17408\n"
    "s_add_u32 m0, %[ldsw], 114688\n"
    "s_mov_b32 s40, 4129408\n"
    "v_add_f32 v21, 1.0, v21\n"
    "v_rcp_f32 v21, v21\n"
    "v_mfma_f32_32x32x16_bf16 v[192:207], a[236:239], a[116:119], v[192:207]\n"
    "s_waitcnt lgkmcnt(4)\n"
    "ds_read_b64_tr_b16 a[232:233], %[rd0] offset:18432\n"
    "ds_read_b64_tr_b16 a[234:235], %[rd0] offset:19456\n"
    "buffer_load_dwordx4 %[w1a], %[srd1], s40 offen lds\n"
    "v_mul_f32 v22, v22, v173\n"
    "v_mul_f32 v21, v21, v172\n"
    "v_exp_f32 v22, v22\n"
    "v_mfma_f32_32x32x16_bf16 v[176:191], a[240:243], a[120:123], v[176:191]\n"
    "ds_read_b64_tr_b16 a[236:237], %[rd1] offset:18432\n"
    "ds_read_b64_tr_b16 a[238:239], %[rd1] offset:19456\n"
    "v_fmaak_f32 v20, v174, v174, 0x409d30fc\n"
    "v_add_f32 v22, 1.0, v22\n"
    "v_mfma_f32_32x32x16_bf16 v[192:207], a[244:247], a[120:123], v[192:207]\n"
    "ds_read_b64_tr_b16 a[240:241], %[rd0] offset:20480\n"
    "ds_read_b64_tr_b16 a[242:243], %[rd0] offset:21504\n"
    "v_rcp_f32 v22, v22\n"
    "v_mul_f32 v20, v20, v174\n"
    "v_mfma_f32_32x32x16_bf16 v[176:191], a[248:251], a[124:127], v[176:191]\n"
    "ds_read_b64_tr_b16 a[244:245], %[rd1] offset:20480\n"
    "ds_read_b64_tr_b16 a[246:247], %[rd1] offset:21504\n"
    "v_mul_f32 v22, v22, v173\n"
    "v_cvt_pk_bf16_f32 v94, v21, v22\n"
    "v_mfma_f32_32x32x16_bf16 v[192:207], a[252:255], a[124:127], v[192:207]\n"
    "s_waitcnt lgkmcnt(4)\n"
    "ds_read_b64_tr_b16 a[248:249], %[rd0] offset:22528\n"
    "ds_read_b64_tr_b16 a[250:251], %[rd0] offset:23552\n"
    "buffer_load_dwordx4 %[w1b], %[srd1], s40 offen offset:1024 lds\n"
    "v_exp_f32 v20, v20\n"
    "v_fmaak_f32 v21, v175, v175, 0x409d30fc\n"
    "v_mfma_f32_32x32x16_bf16 v[176:191], a[224:227], a[128:131], v[176:191]\n"
    "ds_read_b64_tr_b16 a[252:253], %[rd1] offset:22528\n"
    "ds_read_b64_tr_b16 a[254:255], %[rd1] offset:23552\n"
    "v_add_f32 v20, 1.0, v20\n"
    "v_rcp_f32 v20, v20\n"
    "v_mul_f32 v21, v21, v175\n"
    "v_mfma_f32_32x32x16_bf16 v[192:207], a[228:231], a[128:131], v[192:207]\n"
    "s_waitcnt vmcnt(18)\n"
    "s_barrier\n"
    "ds_read_b64_tr_b16 a[224:225], %[rd0] offset:24576\n"
    "ds_read_b64_tr_b16 a[226:227], %[rd0] offset:25600\n"
    "v_mfma_f32_32x32x16_bf16 v[176:191], a[232:235], a[132:135], v[176:191]\n"
    "ds_read_b64_tr_b16 a[228:229], %[rd1] offset:24576\n"
    "ds_read_b64_tr_b16 a[230:231], %[rd1] offset:25600\n"
    "s_add_u32 m0, %[ldsw], 122880\n"
    "s_mov_b32 s40, 4719232\n"
    "v_mul_f32 v20, v20, v174\n"
    "v_exp_f32 v21, v21\n"
    "v_mfma_f32_32x32x16_bf16 v[192:207], a[236:239], a[132:135], v[192:207]\n"
    "s_waitcnt lgkmcnt(4)\n"
    "ds_read_b64_tr_b16 a[232:233], %[rd0] offset:26624\n"
    "ds_read_b64_tr_b16 a[234:235], %[rd0] offset:27648\n"
    "buffer_load_dwordx4 %[w1a], %[srd1], s40 offen lds\n"
    "s_nop 0\n"
    "v_add_f32 v21, 1.0, v21\n"
    "v_mfma_f32_32x32x16_bf16 v[176:191], a[240:243], a[136:139], v[176:191]\n"
    "ds_read_b64_tr_b16 a[236:237], %[rd1] offset:26624\n"
    "ds_read_b64_tr_b16 a[238:239], %[rd1] offset:27648\n"
    "v_rcp_f32 v21, v21\n"
    "s_nop 0\n"
    "v_mfma_f32_32x32x16_bf16 v[192:207], a[244:247], a[136:139], v[192:207]\n"
    "ds_read_b64_tr_b16 a[240:241], %[rd0] offset:28672\n"
    "ds_read_b64_tr_b16 a[242:243], %[rd0] offset:29696\n"
    "v_mul_f32 v21, v21, v175\n"
    "v_cvt_pk_bf16_f32 v95, v20, v21\n"
    "v_mfma_f32_32x32x16_bf16 v[176:191], a[248:251], a[140:143], v[176:191]\n"
    "ds_read_b64_tr_b16 a[244:245], %[rd1] offset:28672\n"
    "ds_read_b64_tr_b16 a[246:247], %[rd1] offset:29696\n"
    "v_mfma_f32_32x32x16_bf16 v[192:207], a[252:255], a[140:143], v[192:207]\n"
    "s_waitcnt lgkmcnt(4)\n"
    "ds_read_b64_tr_b16 a[248:249], %[rd0] offset:30720\n"
    "ds_read_b64_tr_b16 a[250:251], %[rd0] offset:31744\n"
    "buffer_load_dwordx4 %[w1b], %[srd1], s40 offen offset:1024 lds\n"
    "v_mfma_f32_32x32x16_bf16 v[208:223], a[224:227], v[80:83], v[208:223]\n"
    "ds_read_b64_tr_b16 a[252:253], %[rd1] offset:30720\n"
    "ds_read_b64_tr_b16 a[254:255], %[rd1] offset:31744\n"
    "v_mfma_f32_32x32x16_bf16 v[224:239], a[228:231], v[80:83], v[224:239]\n"
    "ds_read_b64_tr_b16 a[224:225], %[rd0] offset:32768\n"
    "ds_read_b64_tr_b16 a[226:227], %[rd0] offset:33792\n"
    "v_mfma_f32_32x32x16_bf16 v[208:223], a[232:235], v[84:87], v[208:223]\n"
    "ds_read_b64_tr_b16 a[228:229], %[rd1] offset:32768\n"
    "ds_read_b64_tr_b16 a[230:231], %[rd1] offset:33792\n"
    "s_add_u32 m0, %[ldsw], 131072\n"
    "s_mov_b32 s40, 294912\n"
    "ds_read_b128 v[144:147], %[vb] offset:1280\n"
    "v_mfma_f32_32x32x16_bf16 v[224:239], a[236:239], v[84:87], v[224:239]\n"
    "s_waitcnt lgkmcnt(5)\n"
    "ds_read_b64_tr_b16 a[232:233], %[rd0] offset:34816\n"
    "ds_read_b64_tr_b16 a[234:235], %[rd0] offset:35840\n"
    "buffer_load_dwordx4 %[w2a], %[srd2], s40 offen lds\n"
    "v_fmaak_f32 v20, v176, v176, 0x409d30fc\n"
    "v_mul_f32 v20, v20, v176\n"
    "v_exp_f32 v20, v20\n"
    "v_mfma_f32_32x32x16_bf16 v[208:223], a[240:243], v[88:91], v[208:223]\n"
    "ds_read_b64_tr_b16 a[236:237], %[rd1] offset:34816\n"
    "ds_read_b64_tr_b16 a[238:239], %[rd1] offset:35840\n"
    "v_fmaak_f32 v21, v177, v177, 0x409d30fc\n"
    "v_add_f32 v20, 1.0, v20\n"
    "v_mfma_f32_32x32x16_bf16 v[224:239], a[244:247], v[88:91], v[224:239]\n"
    "ds_read_b64_tr_b16 a[240:241], %[rd0] offset:36864\n"
    "ds_read_b64_tr_b16 a[242:243], %[rd0] offset:37888\n"
    "ds_read_b128 v[148:151], %[vb] offset:1312\n"
    "v_rcp_f32 v20, v20\n"
    "v_mul_f32 v21, v21, v177\n"
    "v_mfma_f32_32x32x16_bf16 v[208:223], a[248:251], v[92:95], v[208:223]\n"
    "ds_read_b64_tr_b16 a[244:245], %[rd1] offset:36864\n"
    "ds_read_b64_tr_b16 a[246:247], %[rd1] offset:37888\n"
    "v_mul_f32 v20, v20, v176\n"
    "v_exp_f32 v21, v21\n"
    "v_mfma_f32_32x32x16_bf16 v[224:239], a[252:255], v[92:95], v[224:239]\n"
    "s_waitcnt lgkmcnt(5)\n"
    "ds_read_b64_tr_b16 a[248:249], %[rd0] offset:38912\n"
    "ds_read_b64_tr_b16 a[250:251], %[rd0] offset:39936\n"
    "buffer_load_dwordx4 %[w2b], %[srd2], s40 offen offset:1024 lds\n"
    "v_fmaak_f32 v22, v178, v178, 0x409d30fc\n"
    "v_add_f32 v21, 1.0, v21\n"
    "v_rcp_f32 v21, v21\n"
    "v_mfma_f32_32x32x16_bf16 v[240:255], a[224:227], v[80:83], v[240:255]\n"
    "ds_read_b64_tr_b16 a[252:253], %[rd1] offset:38912\n"
    "ds_read_b64_tr_b16 a[254:255], %[rd1] offset:39936\n"
    "ds_read_b128 v[152:155], %[vb] offset:1344\n"
    "v_mul_f32 v22, v22, v178\n"
    "v_mul_f32 v21, v21, v177\n"
    "v_mfma_f32_32x32x16_bf16 a[144:159], a[228:231], v[80:83], a[144:159]\n"
    "ds_read_b64_tr_b16 a[224:225], %[rd0] offset:40960\n"
    "ds_read_b64_tr_b16 a[226:227], %[rd0] offset:41984\n"
    "v_cvt_pk_bf16_f32 v96, v20, v21\n"
    "v_exp_f32 v22, v22\n"
    "v_mfma_f32_32x32x16_bf16 v[240:255], a[232:235], v[84:87], v[240:255]\n"
    "ds_read_b64_tr_b16 a[228:229], %[rd1] offset:40960\n"
    "ds_read_b64_tr_b16 a[230:231], %[rd1] offset:41984\n"
    "s_add_u32 m0, %[ldsw], 139264\n"
    "s_mov_b32 s40, 295040\n"
    "v_fmaak_f32 v20, v179, v179, 0x409d30fc\n"
    "v_add_f32 v22, 1.0, v22\n"
    "v_mfma_f32_32x32x16_bf16 a[144:159], a[236:239], v[84:87], a[144:159]\n"
    "s_waitcnt lgkmcnt(5)\n"
    "ds_read_b64_tr_b16 a[232:233], %[rd0] offset:43008\n"
    "ds_read_b64_tr_b16 a[234:235], %[rd0] offset:44032\n"
    "buffer_load_dwordx4 %[w2a], %[srd2], s40 offen lds\n"
    "ds_read_b128 v[156:159], %[vb] offset:1376\n"
    "v_rcp_f32 v22, v22\n"
    "v_mul_f32 v20, v20, v179\n"
    "v_mfma_f32_32x32x16_bf16 v[240:255], a[240:243], v[88:91], v[240:255]\n"
    "ds_read_b64_tr_b16 a[236:237], %[rd1] offset:43008\n"
    "ds_read_b64_tr_b16 a[238:239], %[rd1] offset:44032\n"
    "v_mul_f32 v22, v22, v178\n"
    "v_exp_f32 v20, v20\n"
    "v_fmaak_f32 v21, v180, v180, 0x409d30fc\n"
    "v_mfma_f32_32x32x16_bf16 a[144:159], a[244:247], v[88:91], a[144:159]\n"
    "ds_read_b64_tr_b16 a[240:241], %[rd0] offset:45056\n"
    "ds_read_b64_tr_b16 a[242:243], %[rd0] offset:46080\n"
    "v_add_f32 v20, 1.0, v20\n"
    "v_rcp_f32 v20, v20\n"
    "v_mfma_f32_32x32x16_bf16 v[240:255], a[248:251], v[92:95], v[240:255]\n"
    "ds_read_b64_tr_b16 a[244:245], %[rd1] offset:45056\n"
    "ds_read_b64_tr_b16 a[246:247], %[rd1] offset:46080\n"
    "ds_read_b128 v[160:163], %[vb] offset:1408\n"
    "v_mul_f32 v21, v21, v180\n"
    "v_mul_f32 v20, v20, v179\n"
    "v_mfma_f32_32x32x16_bf16 a[144:159], a[252:255], v[92:95], a[144:159]\n"
    "s_waitcnt lgkmcnt(5)\n"
    "ds_read_b64_tr_b16 a[248:249], %[rd0] offset:47104\n"
    "ds_read_b64_tr_b16 a[250:251], %[rd0] offset:48128\n"
    "buffer_load_dwordx4 %[w2b], %[srd2], s40 offen offset:1024 lds\n"
    "v_cvt_pk_bf16_f32 v97, v22, v20\n"
    "v_exp_f32 v21, v21\n"
    "v_mfma_f32_32x32x16_bf16 a[160:175], a[224:227], v[80:83], a[160:175]\n"
    "ds_read_b64_tr_b16 a[252:253], %[rd1] offset:47104\n"
    "ds_read_b64_tr_b16 a[254:255], %[rd1] offset:48128\n"
    "v_fmaak_f32 v22, v181, v181, 0x409d30fc\n"
    "v_add_f32 v21, 1.0, v21\n"
    "v_mfma_f32_32x32x16_bf16 a[176:191], a[228:231], v[80:83], a[176:191]\n"
    "s_waitcnt vmcnt(18)\n"
    "s_barrier\n"
    "ds_read_b64_tr_b16 a[224:225], v16 offset:0\n"
    "ds_read_b64_tr_b16 a[226:227], v16 offset:1024\n"
    "v_mfma_f32_32x32x16_bf16 a[160:175], a[232:235], v[84:87], a[160:175]\n"
    "ds_read_b64_tr_b16 a[228:229], v17 offset:0\n"
    "ds_read_b64_tr_b16 a[230:231], v17 offset:1024\n"
    "s_add_u32 m0, %[ldsw], 0\n"
    "s_mov_b32 s40, 295168\n"
    "ds_read_b128 v[164:167], %[vb] offset:1440\n"
    "v_rcp_f32 v21, v21\n"
    "v_mul_f32 v22, v22, v181\n"
    "v_mul_f32 v21, v21, v180\n"
    "v_mfma_f32_32x32x16_bf16 a[176:191], a[236:239], v[84:87], a[176:191]\n"
    "s_waitcnt lgkmcnt(5)\n"
    "ds_read_b64_tr_b16 a[232:233], v16 offset:2048\n"
    "ds_read_b64_tr_b16 a[234:235], v16 offset:3072\n"
    "buffer_load_dwordx4 %[w2a], %[srd2], s40 offen lds\n"
    "v_exp_f32 v22, v22\n"
    "v_fmaak_f32 v20, v182, v182, 0x409d30fc\n"
    "v_mfma_f32_32x32x16_bf16 a[160:175], a[240:243], v[88:91], a[160:175]\n"
    "ds_read_b64_tr_b16 a[236:237], v17 offset:2048\n"
    "ds_read_b64_tr_b16 a[238:239], v17 offset:3072\n"
    "v_add_f32 v22, 1.0, v22\n"
    "v_rcp_f32 v22, v22\n"
    "v_mfma_f32_32x32x16_bf16 a[176:191], a[244:247], v[88:91], a[176:191]\n"
    "ds_read_b64_tr_b16 a[240:241], v16 offset:4096\n"
    "ds_read_b64_tr_b16 a[242:243], v16 offset:5120\n"
    "ds_read_b128 v[168:171], %[vb] offset:1472\n"
    "v_mul_f32 v20, v20, v182\n"
    "v_mul_f32 v22, v22, v181\n"
    "v_mfma_f32_32x32x16_bf16 a[160:175], a[248:251], v[92:95], a[160:175]\n"
    "ds_read_b64_tr_b16 a[244:245], v17 offset:4096\n"
    "ds_read_b64_tr_b16 a[246:247], v17 offset:5120\n"
    "v_cvt_pk_bf16_f32 v98, v21, v22\n"
    "v_exp_f32 v20, v20\n"
    "v_mfma_f32_32x32x16_bf16 a[176:191], a[252:255], v[92:95], a[176:191]\n"
    "s_waitcnt lgkmcnt(5)\n"
    "ds_read_b64_tr_b16 a[248:249], v16 offset:6144\n"
    "ds_read_b64_tr_b16 a[250:251], v16 offset:7168\n"
    "buffer_load_dwordx4 %[w2b], %[srd2], s40 offen offset:1024 lds\n"
    "v_fmaak_f32 v21, v183, v183, 0x409d30fc\n"
    "v_add_f32 v20, 1.0, v20\n"
    "v_rcp_f32 v20, v20\n"
    "v_mfma_f32_32x32x16_bf16 a[192:207], a[224:227], v[80:83], a[192:207]\n"
    "ds_read_b64_tr_b16 a[252:253], v17 offset:6144\n"
    "ds_read_b64_tr_b16 a[254:255], v17 offset:7168\n"
    "ds_read_b128 v[172:175], %[vb] offset:1504\n"
    "v_mul_f32 v21, v21, v183\n"
    "v_mul_f32 v20, v20, v182\n"
    "v_mfma_f32_32x32x16_bf16 a[208:223], a[228:231], v[80:83], a[208:223]\n"
    "ds_read_b64_tr_b16 a[224:225], v16 offset:8192\n"
    "ds_read_b64_tr_b16 a[226:227], v16 offset:9216\n"
    "v_exp_f32 v21, v21\n"
    "v_fmaak_f32 v22, v184, v184, 0x409d30fc\n"
    "v_mfma_f32_32x32x16_bf16 a[192:207], a[232:235], v[84:87], a[192:207]\n"
    "ds_read_b64_tr_b16 a[228:229], v17 offset:8192\n"
    "ds_read_b64_tr_b16 a[230:231], v17 offset:9216\n"
    "s_add_u32 m0, %[ldsw], 8192\n"
    "s_mov_b32 s40, 295296\n"
    "v_add_f32 v21, 1.0, v21\n"
    "v_rcp_f32 v21, v21\n"
    "v_mfma_f32_32x32x16_bf16 a[208:223], a[236:239], v[84:87], a[208:223]\n"
    "s_waitcnt lgkmcnt(5)\n"
    "ds_read_b64_tr_b16 a[232:233], v16 offset:10240\n"
    "ds_read_b64_tr_b16 a[234:235], v16 offset:11264\n"
    "buffer_load_dwordx4 %[w2a], %[srd2], s40 offen lds\n"
    "v_mul_f32 v22, v22, v184\n"
    "v_mul_f32 v21, v21, v183\n"
    "v_cvt_pk_bf16_f32 v99, v20, v21\n"
    "v_mfma_f32_32x32x16_bf16 a[192:207], a[240:243], v[88:91], a[192:207]\n"
    "ds_read_b64_tr_b16 a[236:237], v17 offset:10240\n"
    "ds_read_b64_tr_b16 a[238:239], v17 offset:11264\n"
    "v_exp_f32 v22, v22\n"
    "v_fmaak_f32 v20, v185, v185, 0x409d30fc\n"
    "v_mfma_f32_32x32x16_bf16 a[208:223], a[244:247], v[88:91], a[208:223]\n"
    "ds_read_b64_tr_b16 a[240:241], v16 offset:12288\n"
    "ds_read_b64_tr_b16 a[242:243], v16 offset:13312\n"
    "v_add_f32 v22, 1.0, v22\n"
    "v_rcp_f32 v22, v22\n"
    "v_mfma_f32_32x32x16_bf16 a[192:207], a[248:251], v[92:95], a[192:207]\n"
    "ds_read_b64_tr_b16 a[244:245], v17 offset:12288\n"
    "ds_read_b64_tr_b16 a[246:247], v17 offset:13312\n"
    "v_mul_f32 v20, v20, v185\n"
    "v_mul_f32 v22, v22, v184\n"
    "v_mfma_f32_32x32x16_bf16 a[208:223], a[252:255], v[92:95], a[208:223]\n"
    "s_waitcnt lgkmcnt(4)\n"
    "ds_read_b64_tr_b16 a[248:249], v16 offset:14336\n"
    "ds_read_b64_tr_b16 a[250:251], v16 offset:15360\n"
    "buffer_load_dwordx4 %[w2b], %[srd2], s40 offen offset:1024 lds\n"
    "v_exp_f32 v20, v20\n"
    "v_fmaak_f32 v21, v186, v186, 0x409d30fc\n"
    "v_mfma_f32_32x32x16_bf16 v[144:159], a[224:227], a[0:3], v[144:159]\n"
    "ds_read_b64_tr_b16 a[252:253], v17 offset:14336\n"
    "ds_read_b64_tr_b16 a[254:255], v17 offset:15360\n"
    "v_add_f32 v20, 1.0, v20\n"
    "v_rcp_f32 v20, v20\n"
    "v_mul_f32 v21, v21, v186\n"
    "v_mfma_f32_32x32x16_bf16 v[160:175], a[228:231], a[0:3], v[160:175]\n"
    "ds_read_b64_tr_b16 a[224:225], v16 offset:16384\n"
    "ds_read_b64_tr_b16 a[226:227], v16 offset:17408\n"
    "v_mul_f32 v20, v20, v185\n"
    "v_cvt_pk_bf16_f32 v100, v22, v20\n"
    "v_mfma_f32_32x32x16_bf16 v[144:159], a[232:235], a[4:7], v[144:159]\n"
    "ds_read_b64_tr_b16 a[228:229], v17 offset:16384\n"
    "ds_read_b64_tr_b16 a[230:231], v17 offset:17408\n"
    "s_add_u32 m0, %[ldsw], 16384\n"
    "s_mov_b32 s40, 768\n"
    "v_exp_f32 v21, v21\n"
    "v_fmaak_f32 v22, v187, v187, 0x409d30fc\n"
    "v_mfma_f32_32x32x16_bf16 v[160:175], a[236:239], a[4:7], v[160:175]\n"
    "s_waitcnt lgkmcnt(4)\n"
    "ds_read_b64_tr_b16 a[232:233], v16 offset:18432\n"
    "ds_read_b64_tr_b16 a[234:235], v16 offset:19456\n"
    "buffer_load_dwordx4 %[w1a], %[srd1], s40 offen lds\n"
    "v_add_f32 v21, 1.0, v21\n"
    "v_rcp_f32 v21, v21\n"
    "v_mfma_f32_32x32x16_bf16 v[144:159], a[240:243], a[8:11], v[144:159]\n"
    "ds_read_b64_tr_b16 a[236:237], v17 offset:18432\n"
    "ds_read_b64_tr_b16 a[238:239], v17 offset:19456\n"
    "v_mul_f32 v22, v22, v187\n"
    "v_mul_f32 v21, v21, v186\n"
    "v_mfma_f32_32x32x16_bf16 v[160:175], a[244:247], a[8:11], v[160:175]\n"
    "ds_read_b64_tr_b16 a[240:241], v16 offset:20480\n"
    "ds_read_b64_tr_b16 a[242:243], v16 offset:21504\n"
    "v_exp_f32 v22, v22\n"
    "v_fmaak_f32 v20, v188, v188, 0x409d30fc\n"
    "v_add_f32 v22, 1.0, v22\n"
    "v_mfma_f32_32x32x16_bf16 v[144:159], a[248:251], a[12:15], v[144:159]\n"
    "ds_read_b64_tr_b16 a[244:245], v17 offset:20480\n"
    "ds_read_b64_tr_b16 a[246:247], v17 offset:21504\n"
    "v_rcp_f32 v22, v22\n"
    "v_mul_f32 v20, v20, v188\n"
    "v_mfma_f32_32x32x16_bf16 v[160:175], a[252:255], a[12:15], v[160:175]\n"
    "s_waitcnt lgkmcnt(4)\n"
    "ds_read_b64_tr_b16 a[248:249], v16 offset:22528\n"
    "ds_read_b64_tr_b16 a[250:251], v16 offset:23552\n"
    "buffer_load_dwordx4 %[w1b], %[srd1], s40 offen offset:1024 lds\n"
    "v_mul_f32 v22, v22, v187\n"
    "v_cvt_pk_bf16_f32 v101, v21, v22\n"
    "v_mfma_f32_32x32x16_bf16 v[144:159], a[224:227], a[16:19], v[144:159]\n"
    "ds_read_b64_tr_b16 a[252:253], v17 offset:22528\n"
    "ds_read_b64_tr_b16 a[254:255], v17 offset:23552\n"
    "v_exp_f32 v20, v20\n"
    "v_fmaak_f32 v21, v189, v189, 0x409d30fc\n"
    "v_mfma_f32_32x32x16_bf16 v[160:175], a[228:231], a[16:19], v[160:175]\n"
    "s_waitcnt vmcnt(18)\n"
    "s_barrier\n"
    "ds_read_b64_tr_b16 a[224:225], v16 offset:24576\n"
    "ds_read_b64_tr_b16 a[226:227], v16 offset:25600\n"
    "v_mfma_f32_32x32x16_bf16 v[144:159], a[232:235], a[20:23], v[144:159]\n"
    "ds_read_b64_tr_b16 a[228:229], v17 offset:24576\n"
    "ds_read_b64_tr_b16 a[230:231], v17 offset:25600\n"
    "s_add_u32 m0, %[ldsw], 24576\n"
    "s_mov_b32 s40, 590592\n"
    "v_add_f32 v20, 1.0, v20\n"
    "v_rcp_f32 v20, v20\n"
    "v_mfma_f32_32x32x16_bf16 v[160:175], a[236:239], a[20:23], v[160:175]\n"
    "s_waitcnt lgkmcnt(4)\n"
    "ds_read_b64_tr_b16 a[232:233], v16 offset:26624\n"
    "ds_read_b64_tr_b16 a[234:235], v16 offset:27648\n"
    "buffer_load_dwordx4 %[w1a], %[srd1], s40 offen lds\n"
    "v_mul_f32 v21, v21, v189\n"
    "v_mul_f32 v20, v20, v188\n"
    "v_exp_f32 v21, v21\n"
    "v_mfma_f32_32x32x16_bf16 v[144:159], a[240:243], a[24:27], v[144:159]\n"
    "ds_read_b64_tr_b16 a[236:237], v17 offset:26624\n"
    "ds_read_b64_tr_b16 a[238:239], v17 offset:27648\n"
    "v_fmaak_f32 v22, v190, v190, 0x409d30fc\n"
    "v_add_f32 v21, 1.0, v21\n"
    "v_mfma_f32_32x32x16_bf16 v[160:175], a[244:247], a[24:27], v[160:175]\n"
    "ds_read_b64_tr_b16 a[240:241], v16 offset:28672\n"
    "ds_read_b64_tr_b16 a[242:243], v16 offset:29696\n"
    "v_rcp_f32 v21, v21\n"
    "v_mul_f32 v22, v22, v190\n"
    "v_mfma_f32_32x32x16_bf16 v[144:159], a[248:251], a[28:31], v[144:159]\n"
    "ds_read_b64_tr_b16 a[244:245], v17 offset:28672\n"
    "ds_read_b64_tr_b16 a[246:247], v17 offset:29696\n"
    "v_mul_f32 v21, v21, v189\n"
    "v_cvt_pk_bf16_f32 v102, v20, v21\n"
    "v_mfma_f32_32x32x16_bf16 v[160:175], a[252:255], a[28:31], v[160:175]\n"
    "s_waitcnt lgkmcnt(4)\n"
    "ds_read_b64_tr_b16 a[248:249], v16 offset:30720\n"
    "ds_read_b64_tr_b16 a[250:251], v16 offset:31744\n"
    "buffer_load_dwordx4 %[w1b], %[srd1], s40 offen offset:1024 lds\n"
    "v_exp_f32 v22, v22\n"
    "v_fmaak_f32 v20, v191, v191, 0x409d30fc\n"
    "v_add_f32 v22, 1.0, v22\n"
    "v_mfma_f32_32x32x16_bf16 v[144:159], a[224:227], a[32:35], v[144:159]\n"
    "ds_read_b64_tr_b16 a[252:253], v17 offset:30720\n"
    "ds_read_b64_tr_b16 a[254:255], v17 offset:31744\n"
    "v_rcp_f32 v22, v22\n"
    "v_mul_f32 v20, v20, v191\n"
    "v_mfma_f32_32x32x16_bf16 v[160:175], a[228:231], a[32:35], v[160:175]\n"
    "ds_read_b64_tr_b16 a[224:225], v16 offset:32768\n"
    "ds_read_b64_tr_b16 a[226:227], v16 offset:33792\n"
    "v_mul_f32 v22, v22, v190\n"
    "v_exp_f32 v20, v20\n"
    "v_mfma_f32_32x32x16_bf16 v[144:159], a[232:235], a[36:39], v[144:159]\n"
    "ds_read_b64_tr_b16 a[228:229], v17 offset:32768\n"
    "ds_read_b64_tr_b16 a[230:231], v17 offset:33792\n"
    "s_add_u32 m0, %[ldsw], 32768\n"
    "s_mov_b32 s40, 1180416\n"
    "v_fmaak_f32 v21, v192, v192, 0x409d30fc\n"
    "v_add_f32 v20, 1.0, v20\n"
    "v_mfma_f32_32x32x16_bf16 v[160:175], a[236:239], a[36:39], v[160:175]\n"
    "s_waitcnt lgkmcnt(4)\n"
    "ds_read_b64_tr_b16 a[232:233], v16 offset:34816\n"
    "ds_read_b64_tr_b16 a[234:235], v16 offset:35840\n"
    "buffer_load_dwordx4 %[w1a], %[srd1], s40 offen lds\n"
    "v_rcp_f32 v20, v20\n"
    "v_mul_f32 v21, v21, v192\n"
    "v_mfma_f32_32x32x16_bf16 v[144:159], a[240:243], a[40:43], v[144:159]\n"
    "ds_read_b64_tr_b16 a[236:237], v17 offset:34816\n"
    "ds_read_b64_tr_b16 a[238:239], v17 offset:35840\n"
    "v_mul_f32 v20, v20, v191\n"
    "v_cvt_pk_bf16_f32 v103, v22, v20\n"
    "v_exp_f32 v21, v21\n"
    "v_mfma_f32_32x32x16_bf16 v[160:175], a[244:247], a[40:43], v[160:175]\n"
    "ds_read_b64_tr_b16 a[240:241], v16 offset:36864\n"
    "ds_read_b64_tr_b16 a[242:243], v16 offset:37888\n"
    "v_fmaak_f32 v22, v193, v193, 0x409d30fc\n"
    "v_add_f32 v21, 1.0, v21\n"
    "v_mfma_f32_32x32x16_bf16 v[144:159], a[248:251], a[44:47], v[144:159]\n"
    "ds_read_b64_tr_b16 a[244:245], v17 offset:36864\n"
    "ds_read_b64_tr_b16 a[246:247], v17 offset:37888\n"
    "v_rcp_f32 v21, v21\n"
    "v_mul_f32 v22, v22, v193\n"
    "v_mfma_f32_32x32x16_bf16 v[160:175], a[252:255], a[44:47], v[160:175]\n"
    "s_waitcnt lgkmcnt(4)\n"
    "ds_read_b64_tr_b16 a[248:249], v16 offset:38912\n"
    "ds_read_b64_tr_b16 a[250:251], v16 offset:39936\n"
    "buffer_load_dwordx4 %[w1b], %[srd1], s40 offen offset:1024 lds\n"
    "v_mul_f32 v21, v21, v192\n"
    "v_exp_f32 v22, v22\n"
    "v_mfma_f32_32x32x16_bf16 v[144:159], a[224:227], a[48:51], v[144:159]\n"
    "ds_read_b64_tr_b16 a[252:253], v17 offset:38912\n"
    "ds_read_b64_tr_b16 a[254:255], v17 offset:39936\n"
    "v_fmaak_f32 v20, v194, v194, 0x409d30fc\n"
    "v_add_f32 v22, 1.0, v22\n"
    "v_mfma_f32_32x32x16_bf16 v[160:175], a[228:231], a[48:51], v[160:175]\n"
    "ds_read_b64_tr_b16 a[224:225], v16 offset:40960\n"
    "ds_read_b64_tr_b16 a[226:227], v16 offset:41984\n"
    "v_rcp_f32 v22, v22\n"
    "v_mul_f32 v20, v20, v194\n"
    "v_mul_f32 v22, v22, v193\n"
    "v_mfma_f32_32x32x16_bf16 v[144:159], a[232:235], a[52:55], v[144:159]\n"
    "ds_read_b64_tr_b16 a[228:229], v17 offset:40960\n"
    "ds_read_b64_tr_b16 a[230:231], v17 offset:41984\n"
    "s_add_u32 m0, %[ldsw], 40960\n"
    "s_mov_b32 s40, 1770240\n"
    "v_cvt_pk_bf16_f32 v104, v21, v22\n"
    "v_exp_f32 v20, v20\n"
    "v_mfma_f32_32x32x16_bf16 v[160:175], a[236:239], a[52:55], v[160:175]\n"
    "s_waitcnt lgkmcnt(4)\n"
    "ds_read_b64_tr_b16 a[232:233], v16 offset:43008\n"
    "ds_read_b64_tr_b16 a[234:235], v16 offset:44032\n"
    "buffer_load_dwordx4 %[w1a], %[srd1], s40 offen lds\n"
    "v_fmaak_f32 v21, v195, v195, 0x409d30fc\n"
    "v_add_f32 v20, 1.0, v20\n"
    "v_mfma_f32_32x32x16_bf16 v[144:159], a[240:243], a[56:59], v[144:159]\n"
    "ds_read_b64_tr_b16 a[236:237], v17 offset:43008\n"
    "ds_read_b64_tr_b16 a[238:239], v17 offset:44032\n"
    "v_rcp_f32 v20, v20\n"
    "v_mul_f32 v21, v21, v195\n"
    "v_mfma_f32_32x32x16_bf16 v[160:175], a[244:247], a[56:59], v[160:175]\n"
    "ds_read_b64_tr_b16 a[240:241], v16 offset:45056\n"
    "ds_read_b64_tr_b16 a[242:243], v16 offset:46080\n"
    "v_mul_f32 v20, v20, v194\n"
    "v_exp_f32 v21, v21\n"
    "v_mfma_f32_32x32x16_bf16 v[144:159], a[248:251], a[60:63], v[144:159]\n"
    "ds_read_b64_tr_b16 a[244:245], v17 offset:45056\n"
    "ds_read_b64_tr_b16 a[246:247], v17 offset:46080\n"
    "v_fmaak_f32 v22, v196, v196, 0x409d30fc\n"
    "v_add_f32 v21, 1.0, v21\n"
    "v_rcp_f32 v21, v21\n"
    "v_mfma_f32_32x32x16_bf16 v[160:175], a[252:255], a[60:63], v[160:175]\n"
    "s_waitcnt lgkmcnt(4)\n"
    "ds_read_b64_tr_b16 a[248:249], v16 offset:47104\n"
    "ds_read_b64_tr_b16 a[250:251], v16 offset:48128\n"
    "buffer_load_dwordx4 %[w1b], %[srd1], s40 offen offset:1024 lds\n"
    "v_mul_f32 v22, v22, v196\n"
    "v_mul_f32 v21, v21, v195\n"
    "v_mfma_f32_32x32x16_bf16 v[144:159], a[224:227], a[64:67], v[144:159]\n"
    "ds_read_b64_tr_b16 a[252:253], v17 offset:47104\n"
    "ds_read_b64_tr_b16 a[254:255], v17 offset:48128\n"
    "v_cvt_pk_bf16_f32 v105, v20, v21\n"
    "v_exp_f32 v22, v22\n"
    "v_mfma_f32_32x32x16_bf16 v[160:175], a[228:231], a[64:67], v[160:175]\n"
    "s_waitcnt vmcnt(18)\n"
    "s_barrier\n"
    "ds_read_b64_tr_b16 a[224:225], v18 offset:0\n"
    "ds_read_b64_tr_b16 a[226:227], v18 offset:1024\n"
    "v_mfma_f32_32x32x16_bf16 v[144:159], a[232:235], a[68:71], v[144:159]\n"
    "ds_read_b64_tr_b16 a[228:229], v19 offset:0\n"
    "ds_read_b64_tr_b16 a[230:231], v19 offset:1024\n"
    "s_add_u32 m0, %[ldsw], 49152\n"
    "s_mov_b32 s40, 2360064\n"
    "v_fmaak_f32 v20, v197, v197, 0x409d30fc\n"
    "v_add_f32 v22, 1.0, v22\n"
    "v_mfma_f32_32x32x16_bf16 v[160:175], a[236:239], a[68:71], v[160:175]\n"
    "s_waitcnt lgkmcnt(4)\n"
    "ds_read_b64_tr_b16 a[232:233], v18 offset:2048\n"
    "ds_read_b64_tr_b16 a[234:235], v18 offset:3072\n"
    "buffer_load_dwordx4 %[w1a], %[srd1], s40 offen lds\n"
    "v_rcp_f32 v22, v22\n"
    "v_mul_f32 v20, v20, v197\n"
    "v_mul_f32 v22, v22, v196\n"
    "v_mfma_f32_32x32x16_bf16 v[144:159], a[240:243], a[72:75], v[144:159]\n"
    "ds_read_b64_tr_b16 a[236:237], v19 offset:2048\n"
    "ds_read_b64_tr_b16 a[238:239], v19 offset:3072\n"
    "v_exp_f32 v20, v20\n"
    "v_fmaak_f32 v21, v198, v198, 0x409d30fc\n"
    "v_mfma_f32_32x32x16_bf16 v[160:175], a[244:247], a[72:75], v[160:175]\n"
    "ds_read_b64_tr_b16 a[240:241], v18 offset:4096\n"
    "ds_read_b64_tr_b16 a[242:243], v18 offset:5120\n"
    "v_add_f32 v20, 1.0, v20\n"
    "v_rcp_f32 v20, v20\n"
    "v_mfma_f32_32x32x16_bf16 v[144:159], a[248:251], a[76:79], v[144:159]\n"
    "ds_read_b64_tr_b16 a[244:245], v19 offset:4096\n"
    "ds_read_b64_tr_b16 a[246:247], v19 offset:5120\n"
    "v_mul_f32 v21, v21, v198\n"
    "v_mul_f32 v20, v20, v197\n"
    "v_mfma_f32_32x32x16_bf16 v[160:175], a[252:255], a[76:79], v[160:175]\n"
    "s_waitcnt lgkmcnt(4)\n"
    "ds_read_b64_tr_b16 a[248:249], v18 offset:6144\n"
    "ds_read_b64_tr_b16 a[250:251], v18 offset:7168\n"
    "buffer_load_dwordx4 %[w1b], %[srd1], s40 offen offset:1024 lds\n"
    "v_cvt_pk_bf16_f32 v106, v22, v20\n"
    "v_exp_f32 v21, v21\n"
    "v_mfma_f32_32x32x16_bf16 v[144:159], a[224:227], a[80:83], v[144:159]\n"
    "ds_read_b64_tr_b16 a[252:253], v19 offset:6144\n"
    "ds_read_b64_tr_b16 a[254:255], v19 offset:7168\n"
    "v_fmaak_f32 v22, v199, v199, 0x409d30fc\n"
    "v_add_f32 v21, 1.0, v21\n"
    "v_rcp_f32 v21, v21\n"
    "v_mfma_f32_32x32x16_bf16 v[160:175], a[228:231], a[80:83], v[160:175]\n"
    "ds_read_b64_tr_b16 a[224:225], v18 offset:8192\n"
    "ds_read_b64_tr_b16 a[226:227], v18 offset:9216\n"
    "v_mul_f32 v22, v22, v199\n"
    "v_mul_f32 v21, v21, v198\n"
    "v_mfma_f32_32x32x16_bf16 v[144:159], a[232:235], a[84:87], v[144:159]\n"
    "ds_read_b64_tr_b16 a[228:229], v19 offset:8192\n"
    "ds_read_b64_tr_b16 a[230:231], v19 offset:9216\n"
    "s_add_u32 m0, %[ldsw], 57344\n"
    "s_mov_b32 s40, 2949888\n"
    "v_exp_f32 v22, v22\n"
    "v_fmaak_f32 v20, v200, v200, 0x409d30fc\n"
    "v_mfma_f32_32x32x16_bf16 v[160:175], a[236:239], a[84:87], v[160:175]\n"
    "s_waitcnt lgkmcnt(4)\n"
    "ds_read_b64_tr_b16 a[232:233], v18 offset:10240\n"
    "ds_read_b64_tr_b16 a[234:235], v18 offset:11264\n"
    "buffer_load_dwordx4 %[w1a], %[srd1], s40 offen lds\n"
    "v_add_f32 v22, 1.0, v22\n"
    "v_rcp_f32 v22, v22\n"
    "v_mfma_f32_32x32x16_bf16 v[144:159], a[240:243], a[88:91], v[144:159]\n"
    "ds_read_b64_tr_b16 a[236:237], v19 offset:10240\n"
    "ds_read_b64_tr_b16 a[238:239], v19 offset:11264\n"
    "v_mul_f32 v20, v20, v200\n"
    "v_mul_f32 v22, v22, v199\n"
    "v_mfma_f32_32x32x16_bf16 v[160:175], a[244:247], a[88:91], v[160:175]\n"
    "ds_read_b64_tr_b16 a[240:241], v18 offset:12288\n"
    "ds_read_b64_tr_b16 a[242:243], v18 offset:13312\n"
    "v_cvt_pk_bf16_f32 v107, v21, v22\n"
    "v_exp_f32 v20, v20\n"
    "v_fmaak_f32 v21, v201, v201, 0x409d30fc\n"
    "v_mfma_f32_32x32x16_bf16 v[144:159], a[248:251], a[92:95], v[144:159]\n"
    "ds_read_b64_tr_b16 a[244:245], v19 offset:12288\n"
    "ds_read_b64_tr_b16 a[246:247], v19 offset:13312\n"
    "v_add_f32 v20, 1.0, v20\n"
    "v_rcp_f32 v20, v20\n"
    "v_mfma_f32_32x32x16_bf16 v[160:175], a[252:255], a[92:95], v[160:175]\n"
    "s_waitcnt lgkmcnt(4)\n"
    "ds_read_b64_tr_b16 a[248:249], v18 offset:14336\n"
    "ds_read_b64_tr_b16 a[250:251], v18 offset:15360\n"
    "buffer_load_dwordx4 %[w1b], %[srd1], s40 offen offset:1024 lds\n"
    "v_mul_f32 v21, v21, v201\n"
    "v_mul_f32 v20, v20, v200\n"
    "v_mfma_f32_32x32x16_bf16 v[144:159], a[224:227], a[96:99], v[144:159]\n"
    "ds_read_b64_tr_b16 a[252:253], v19 offset:14336\n"
    "ds_read_b64_tr_b16 a[254:255], v19 offset:15360\n"
    "v_exp_f32 v21, v21\n"
    "v_fmaak_f32 v22, v202, v202, 0x409d30fc\n"
    "v_mfma_f32_32x32x16_bf16 v[160:175], a[228:231], a[96:99], v[160:175]\n"
    "ds_read_b64_tr_b16 a[224:225], v18 offset:16384\n"
    "ds_read_b64_tr_b16 a[226:227], v18 offset:17408\n"
    "v_add_f32 v21, 1.0, v21\n"
    "v_rcp_f32 v21, v21\n"
    "v_mfma_f32_32x32x16_bf16 v[144:159], a[232:235], a[100:103], v[144:159]\n"
    "ds_read_b64_tr_b16 a[228:229], v19 offset:16384\n"
    "ds_read_b64_tr_b16 a[230:231], v19 offset:17408\n"
    "s_add_u32 m0, %[ldsw], 65536\n"
    "s_mov_b32 s40, 3539712\n"
    "v_mul_f32 v22, v22, v202\n"
    "v_mul_f32 v21, v21, v201\n"
    "v_cvt_pk_bf16_f32 v108, v20, v21\n"
    "v_mfma_f32_32x32x16_bf16 v[160:175], a[236:239], a[100:103], v[160:175]\n"
    "s_waitcnt lgkmcnt(4)\n"
    "ds_read_b64_tr_b16 a[232:233], v18 offset:18432\n"
    "ds_read_b64_tr_b16 a[234:235], v18 offset:19456\n"
    "buffer_load_dwordx4 %[w1a], %[srd1], s40 offen lds\n"
    "v_exp_f32 v22, v22\n"
    "v_fmaak_f32 v20, v203, v203, 0x409d30fc\n"
    "v_mfma_f32_32x32x16_bf16 v[144:159], a[240:243], a[104:107], v[144:159]\n"
    "ds_read_b64_tr_b16 a[236:237], v19 offset:18432\n"
    "ds_read_b64_tr_b16 a[238:239], v19 offset:19456\n"
    "v_add_f32 v22, 1.0, v22\n"
    "v_rcp_f32 v22, v22\n"
    "v_mfma_f32_32x32x16_bf16 v[160:175], a[244:247], a[104:107], v[160:175]\n"
    "ds_read_b64_tr_b16 a[240:241], v18 offset:20480\n"
    "ds_read_b64_tr_b16 a[242:243], v18 offset:21504\n"
    "v_mul_f32 v20, v20, v203\n"
    "v_mul_f32 v22, v22, v202\n"
    "v_mfma_f32_32x32x16_bf16 v[144:159], a[248:251], a[108:111], v[144:159]\n"
    "ds_read_b64_tr_b16 a[244:245], v19 offset:20480\n"
    "ds_read_b64_tr_b16 a[246:247], v19 offset:21504\n"
    "v_exp_f32 v20, v20\n"
    "v_fmaak_f32 v21, v204, v204, 0x409d30fc\n"
    "v_add_f32 v20, 1.0, v20\n"
    "v_mfma_f32_32x32x16_bf16 v[160:175], a[252:255], a[108:111], v[160:175]\n"
    "s_waitcnt lgkmcnt(4)\n"
    "ds_read_b64_tr_b16 a[248:249], v18 offset:22528\n"
    "ds_read_b64_tr_b16 a[250:251], v18 offset:23552\n"
    "buffer_load_dwordx4 %[w1b], %[srd1], s40 offen offset:1024 lds\n"
    "v_rcp_f32 v20, v20\n"
    "v_mul_f32 v21, v21, v204\n"
    "v_mfma_f32_32x32x16_bf16 v[144:159], a[224:227], a[112:115], v[144:159]\n"
    "ds_read_b64_tr_b16 a[252:253], v19 offset:22528\n"
    "ds_read_b64_tr_b16 a[254:255], v19 offset:23552\n"
    "v_mul_f32 v20, v20, v203\n"
    "v_cvt_pk_bf16_f32 v109, v22, v20\n"
    "v_mfma_f32_32x32x16_bf16 v[160:175], a[228:231], a[112:115], v[160:175]\n"
    "s_waitcnt vmcnt(18)\n"
    "s_barrier\n"
    "ds_read_b64_tr_b16 a[224:225], v18 offset:24576\n"
    "ds_read_b64_tr_b16 a[226:227], v18 offset:25600\n"
    "v_mfma_f32_32x32x16_bf16 v[144:159], a[232:235], a[116:119], v[144:159]\n"
    "ds_read_b64_tr_b16 a[228:229], v19 offset:24576\n"
    "ds_read_b64_tr_b16 a[230:231], v19 offset:25600\n"
    "s_add_u32 m0, %[ldsw], 73728\n"
    "s_mov_b32 s40, 4129536\n"
    "v_exp_f32 v21, v21\n"
    "v_fmaak_f32 v22, v205, v205, 0x409d30fc\n"
    "v_mfma_f32_32x32x16_bf16 v[160:175], a[236:239], a[116:119], v[160:175]\n"
    "s_waitcnt lgkmcnt(4)\n"
    "ds_read_b64_tr_b16 a[232:233], v18 offset:26624\n"
    "ds_read_b64_tr_b16 a[234:235], v18 offset:27648\n"
    "buffer_load_dwordx4 %[w1a], %[srd1], s40 offen lds\n"
    "v_add_f32 v21, 1.0, v21\n"
    "v_rcp_f32 v21, v21\n"
    "v_mfma_f32_32x32x16_bf16 v[144:159], a[240:243], a[120:123], v[144:159]\n"
    "ds_read_b64_tr_b16 a[236:237], v19 offset:26624\n"
    "ds_read_b64_tr_b16 a[238:239], v19 offset:27648\n"
    "v_mul_f32 v22, v22, v205\n"
    "v_mul_f32 v21, v21, v204\n"
    "v_exp_f32 v22, v22\n"
    "v_mfma_f32_32x32x16_bf16 v[160:175], a[244:247], a[120:123], v[160:175]\n"
    "ds_read_b64_tr_b16 a[240:241], v18 offset:28672\n"
    "ds_read_b64_tr_b16 a[242:243], v18 offset:29696\n"
    "v_fmaak_f32 v20, v206, v206, 0x409d30fc\n"
    "v_add_f32 v22, 1.0, v22\n"
    "v_mfma_f32_32x32x16_bf16 v[144:159], a[248:251], a[124:127], v[144:159]\n"
    "ds_read_b64_tr_b16 a[244:245], v19 offset:28672\n"
    "ds_read_b64_tr_b16 a[246:247], v19 offset:29696\n"
    "v_rcp_f32 v22, v22\n"
    "v_mul_f32 v20, v20, v206\n"
    "v_mfma_f32_32x32x16_bf16 v[160:175], a[252:255], a[124:127], v[160:175]\n"
    "s_waitcnt lgkmcnt(4)\n"
    "ds_read_b64_tr_b16 a[248:249], v18 offset:30720\n"
    "ds_read_b64_tr_b16 a[250:251], v18 offset:31744\n"
    "buffer_load_dwordx4 %[w1b], %[srd1], s40 offen offset:1024 lds\n"
    "v_mul_f32 v22, v22, v205\n"
    "v_cvt_pk_bf16_f32 v110, v21, v22\n"
    "v_mfma_f32_32x32x16_bf16 v[144:159], a[224:227], a[128:131], v[144:159]\n"
    "ds_read_b64_tr_b16 a[252:253], v19 offset:30720\n"
    "ds_read_b64_tr_b16 a[254:255], v19 offset:31744\n"
    "v_exp_f32 v20, v20\n"
    "v_fmaak_f32 v21, v207, v207, 0x409d30fc\n"
    "v_mfma_f32_32x32x16_bf16 v[160:175], a[228:231], a[128:131], v[160:175]\n"
    "ds_read_b64_tr_b16 a[224:225], v18 offset:32768\n"
    "ds_read_b64_tr_b16 a[226:227], v18 offset:33792\n"
    "v_add_f32 v20, 1.0, v20\n"
    "v_rcp_f32 v20, v20\n"
    "v_mul_f32 v21, v21, v207\n"
    "v_mfma_f32_32x32x16_bf16 v[144:159], a[232:235], a[132:135], v[144:159]\n"
    "ds_read_b64_tr_b16 a[228:229], v19 offset:32768\n"
    "ds_read_b64_tr_b16 a[230:231], v19 offset:33792\n"
    "s_add_u32 m0, %[ldsw], 81920\n"
    "s_mov_b32 s40, 4719360\n"
    "v_mul_f32 v20, v20, v206\n"
    "v_exp_f32 v21, v21\n"
    "v_mfma_f32_32x32x16_bf16 v[160:175], a[236:239], a[132:135], v[160:175]\n"
    "s_waitcnt lgkmcnt(4)\n"
    "ds_read_b64_tr_b16 a[232:233], v18 offset:34816\n"
    "ds_read_b64_tr_b16 a[234:235], v18 offset:35840\n"
    "buffer_load_dwordx4 %[w1a], %[srd1], s40 offen lds\n"
    "s_nop 0\n"
    "v_add_f32 v21, 1.0, v21\n"
    "v_mfma_f32_32x32x16_bf16 v[144:159], a[240:243], a[136:139], v[144:159]\n"
    "ds_read_b64_tr_b16 a[236:237], v19 offset:34816\n"
    "ds_read_b64_tr_b16 a[238:239], v19 offset:35840\n"
    "v_rcp_f32 v21, v21\n"
    "s_nop 0\n"
    "v_mfma_f32_32x32x16_bf16 v[160:175], a[244:247], a[136:139], v[160:175]\n"
    "ds_read_b64_tr_b16 a[240:241], v18 offset:36864\n"
    "ds_read_b64_tr_b16 a[242:243], v18 offset:37888\n"
    "v_mul_f32 v21, v21, v207\n"
    "v_cvt_pk_bf16_f32 v111, v20, v21\n"
    "v_mfma_f32_32x32x16_bf16 v[144:159], a[248:251], a[140:143], v[144:159]\n"
    "ds_read_b64_tr_b16 a[244:245], v19 offset:36864\n"
    "ds_read_b64_tr_b16 a[246:247], v19 offset:37888\n"
    "v_mfma_f32_32x32x16_bf16 v[160:175], a[252:255], a[140:143], v[160:175]\n"
    "s_waitcnt lgkmcnt(4)\n"
    "ds_read_b64_tr_b16 a[248:249], v18 offset:38912\n"
    "ds_read_b64_tr_b16 a[250:251], v18 offset:39936\n"
    "buffer_load_dwordx4 %[w1b], %[srd1], s40 offen offset:1024 lds\n"
    "v_mfma_f32_32x32x16_bf16 v[208:223], a[224:227], v[96:99], v[208:223]\n"
    "ds_read_b64_tr_b16 a[252:253], v19 offset:38912\n"
    "ds_read_b64_tr_b16 a[254:255], v19 offset:39936\n"
    "v_mfma_f32_32x32x16_bf16 v[224:239], a[228:231], v[96:99], v[224:239]\n"
    "ds_read_b64_tr_b16 a[224:225], v18 offset:40960\n"
    "ds_read_b64_tr_b16 a[226:227], v18 offset:41984\n"
    "v_mfma_f32_32x32x16_bf16 v[208:223], a[232:235], v[100:103], v[208:223]\n"
    "ds_read_b64_tr_b16 a[228:229], v19 offset:40960\n"
    "ds_read_b64_tr_b16 a[230:231], v19 offset:41984\n"
    "s_add_u32 m0, %[ldsw], 90112\n"
    "s_mov_b32 s40, 368640\n"
    "ds_read_b128 v[176:179], %[vb] offset:1536\n"
    "v_mfma_f32_32x32x16_bf16 v[224:239], a[236:239], v[100:103], v[224:239]\n"
    "s_waitcnt lgkmcnt(5)\n"
    "ds_read_b64_tr_b16 a[232:233], v18 offset:43008\n"
    "ds_read_b64_tr_b16 a[234:235], v18 offset:44032\n"
    "buffer_load_dwordx4 %[w2a], %[srd2], s40 offen lds\n"
    "v_fmaak_f32 v20, v144, v144, 0x409d30fc\n"
    "v_mul_f32 v20, v20, v144\n"
    "v_exp_f32 v20, v20\n"
    "v_mfma_f32_32x32x16_bf16 v[208:223], a[240:243], v[104:107], v[208:223]\n"
    "ds_read_b64_tr_b16 a[236:237], v19 offset:43008\n"
    "ds_read_b64_tr_b16 a[238:239], v19 offset:44032\n"
    "v_fmaak_f32 v21, v145, v145, 0x409d30fc\n"
    "v_add_f32 v20, 1.0, v20\n"
    "v_mfma_f32_32x32x16_bf16 v[224:239], a[244:247], v[104:107], v[224:239]\n"
    "ds_read_b64_tr_b16 a[240:241], v18 offset:45056\n"
    "ds_read_b64_tr_b16 a[242:243], v18 offset:46080\n"
    "ds_read_b128 v[180:183], %[vb] offset:1568\n"
    "v_rcp_f32 v20, v20\n"
    "v_mul_f32 v21, v21, v145\n"
    "v_mfma_f32_32x32x16_bf16 v[208:223], a[248:251], v[108:111], v[208:223]\n"
    "ds_read_b64_tr_b16 a[244:245], v19 offset:45056\n"
    "ds_read_b64_tr_b16 a[246:247], v19 offset:46080\n"
    "v_mul_f32 v20, v20, v144\n"
    "v_exp_f32 v21, v21\n"
    "v_mfma_f32_32x32x16_bf16 v[224:239], a[252:255], v[108:111], v[224:239]\n"
    "s_waitcnt lgkmcnt(5)\n"
    "ds_read_b64_tr_b16 a[248:249], v18 offset:47104\n"
    "ds_read_b64_tr_b16 a[250:251], v18 offset:48128\n"
    "buffer_load_dwordx4 %[w2b], %[srd2], s40 offen offset:1024 lds\n"
    "v_fmaak_f32 v22, v146, v146, 0x409d30fc\n"
    "v_add_f32 v21, 1.0, v21\n"
    "v_rcp_f32 v21, v21\n"
    "v_mfma_f32_32x32x16_bf16 v[240:255], a[224:227], v[96:99], v[240:255]\n"
    "ds_read_b64_tr_b16 a[252:253], v19 offset:47104\n"
    "ds_read_b64_tr_b16 a[254:255], v19 offset:48128\n"
    "ds_read_b128 v[184:187], %[vb] offset:1600\n"
    "v_mul_f32 v22, v22, v146\n"
    "v_mul_f32 v21, v21, v145\n"
    "v_mfma_f32_32x32x16_bf16 a[144:159], a[228:231], v[96:99], a[144:159]\n"
    "s_waitcnt vmcnt(18)\n"
    "s_barrier\n"
    "ds_read_b64_tr_b16 a[224:225], %[rd0] offset:0\n"
    "ds_read_b64_tr_b16 a[226:227], %[rd0] offset:1024\n"
    "v_mfma_f32_32x32x16_bf16 v[240:255], a[232:235], v[100:103], v[240:255]\n"
    "ds_read_b64_tr_b16 a[228:229], %[rd1] offset:0\n"
    "ds_read_b64_tr_b16 a[230:231], %[rd1] offset:1024\n"
    "s_add_u32 m0, %[ldsw], 98304\n"
    "s_mov_b32 s40, 368768\n"
    "v_cvt_pk_bf16_f32 v112, v20, v21\n"
    "v_exp_f32 v22, v22\n"
    "v_mfma_f32_32x32x16_bf16 a[144:159], a[236:239], v[100:103], a[144:159]\n"
    "s_waitcnt lgkmcnt(5)\n"
    "ds_read_b64_tr_b16 a[232:233], %[rd0] offset:2048\n"
    "ds_read_b64_tr_b16 a[234:235], %[rd0] offset:3072\n"
    "buffer_load_dwordx4 %[w2a], %[srd2], s40 offen lds\n"
    "v_fmaak_f32 v20, v147, v147, 0x409d30fc\n"
    "v_add_f32 v22, 1.0, v22\n"
    "v_mfma_f32_32x32x16_bf16 v[240:255], a[240:243], v[104:107], v[240:255]\n"
    "ds_read_b64_tr_b16 a[236:237], %[rd1] offset:2048\n"
    "ds_read_b64_tr_b16 a[238:239], %[rd1] offset:3072\n"
    "ds_read_b128 v[188:191], %[vb] offset:1632\n"
    "v_rcp_f32 v22, v22\n"
    "v_mul_f32 v20, v20, v147\n"
    "v_mfma_f32_32x32x16_bf16 a[144:159], a[244:247], v[104:107], a[144:159]\n"
    "ds_read_b64_tr_b16 a[240:241], %[rd0] offset:4096\n"
    "ds_read_b64_tr_b16 a[242:243], %[rd0] offset:5120\n"
    "v_mul_f32 v22, v22, v146\n"
    "v_exp_f32 v20, v20\n"
    "v_fmaak_f32 v21, v148, v148, 0x409d30fc\n"
    "v_mfma_f32_32x32x16_bf16 v[240:255], a[248:251], v[108:111], v[240:255]\n"
    "ds_read_b64_tr_b16 a[244:245], %[rd1] offset:4096\n"
    "ds_read_b64_tr_b16 a[246:247], %[rd1] offset:5120\n"
    "v_add_f32 v20, 1.0, v20\n"
    "v_rcp_f32 v20, v20\n"
    "v_mfma_f32_32x32x16_bf16 a[144:159], a[252:255], v[108:111], a[144:159]\n"
    "s_waitcnt lgkmcnt(5)\n"
    "ds_read_b64_tr_b16 a[248:249], %[rd0] offset:6144\n"
    "ds_read_b64_tr_b16 a[250:251], %[rd0] offset:7168\n"
    "buffer_load_dwordx4 %[w2b], %[srd2], s40 offen offset:1024 lds\n"
    "ds_read_b128 v[192:195], %[vb] offset:1664\n"
    "v_mul_f32 v21, v21, v148\n"
    "v_mul_f32 v20, v20, v147\n"
    "v_mfma_f32_32x32x16_bf16 a[160:175], a[224:227], v[96:99], a[160:175]\n"
    "ds_read_b64_tr_b16 a[252:253], %[rd1] offset:6144\n"
    "ds_read_b64_tr_b16 a[254:255], %[rd1] offset:7168\n"
    "v_cvt_pk_bf16_f32 v113, v22, v20\n"
    "v_exp_f32 v21, v21\n"
    "v_mfma_f32_32x32x16_bf16 a[176:191], a[228:231], v[96:99], a[176:191]\n"
    "ds_read_b64_tr_b16 a[224:225], %[rd0] offset:8192\n"
    "ds_read_b64_tr_b16 a[226:227], %[rd0] offset:9216\n"
    "v_fmaak_f32 v22, v149, v149, 0x409d30fc\n"
    "v_add_f32 v21, 1.0, v21\n"
    "v_mfma_f32_32x32x16_bf16 a[160:175], a[232:235], v[100:103], a[160:175]\n"
    "ds_read_b64_tr_b16 a[228:229], %[rd1] offset:8192\n"
    "ds_read_b64_tr_b16 a[230:231], %[rd1] offset:9216\n"
    "s_add_u32 m0, %[ldsw], 106496\n"
    "s_mov_b32 s40, 368896\n"
    "ds_read_b128 v[196:199], %[vb] offset:1696\n"
    "v_rcp_f32 v21, v21\n"
    "v_mul_f32 v22, v22, v149\n"
    "v_mul_f32 v21, v21, v148\n"
    "v_mfma_f32_32x32x16_bf16 a[176:191], a[236:239], v[100:103], a[176:191]\n"
    "s_waitcnt lgkmcnt(5)\n"
    "ds_read_b64_tr_b16 a[232:233], %[rd0] offset:10240\n"
    "ds_read_b64_tr_b16 a[234:235], %[rd0] offset:11264\n"
    "buffer_load_dwordx4 %[w2a], %[srd2], s40 offen lds\n"
    "v_exp_f32 v22, v22\n"
    "v_fmaak_f32 v20, v150, v150, 0x409d30fc\n"
    "v_mfma_f32_32x32x16_bf16 a[160:175], a[240:243], v[104:107], a[160:175]\n"
    "ds_read_b64_tr_b16 a[236:237], %[rd1] offset:10240\n"
    "ds_read_b64_tr_b16 a[238:239], %[rd1] offset:11264\n"
    "v_add_f32 v22, 1.0, v22\n"
    "v_rcp_f32 v22, v22\n"
    "v_mfma_f32_32x32x16_bf16 a[176:191], a[244:247], v[104:107], a[176:191]\n"
    "ds_read_b64_tr_b16 a[240:241], %[rd0] offset:12288\n"
    "ds_read_b64_tr_b16 a[242:243], %[rd0] offset:13312\n"
    "ds_read_b128 v[200:203], %[vb] offset:1728\n"
    "v_mul_f32 v20, v20, v150\n"
    "v_mul_f32 v22, v22, v149\n"
    "v_mfma_f32_32x32x16_bf16 a[160:175], a[248:251], v[108:111], a[160:175]\n"
    "ds_read_b64_tr_b16 a[244:245], %[rd1] offset:12288\n"
    "ds_read_b64_tr_b16 a[246:247], %[rd1] offset:13312\n"
    "v_cvt_pk_bf16_f32 v114, v21, v22\n"
    "v_exp_f32 v20, v20\n"
    "v_mfma_f32_32x32x16_bf16 a[176:191], a[252:255], v[108:111], a[176:191]\n"
    "s_waitcnt lgkmcnt(5)\n"
    "ds_read_b64_tr_b16 a[248:249], %[rd0] offset:14336\n"
    "ds_read_b64_tr_b16 a[250:251], %[rd0] offset:15360\n"
    "buffer_load_dwordx4 %[w2b], %[srd2], s40 offen offset:1024 lds\n"
    "v_fmaak_f32 v21, v151, v151, 0x409d30fc\n"
    "v_add_f32 v20, 1.0, v20\n"
    "v_rcp_f32 v20, v20\n"
    "v_mfma_f32_32x32x16_bf16 a[192:207], a[224:227], v[96:99], a[192:207]\n"
    "ds_read_b64_tr_b16 a[252:253], %[rd1] offset:14336\n"
    "ds_read_b64_tr_b16 a[254:255], %[rd1] offset:15360\n"
    "ds_read_b128 v[204:207], %[vb] offset:1760\n"
    "v_mul_f32 v21, v21, v151\n"
    "v_mul_f32 v20, v20, v150\n"
    "v_mfma_f32_32x32x16_bf16 a[208:223], a[228:231], v[96:99], a[208:223]\n"
    "ds_read_b64_tr_b16 a[224:225], %[rd0] offset:16384\n"
    "ds_read_b64_tr_b16 a[226:227], %[rd0] offset:17408\n"
    "v_exp_f32 v21, v21\n"
    "v_fmaak_f32 v22, v152, v152, 0x409d30fc\n"
    "v_mfma_f32_32x32x16_bf16 a[192:207], a[232:235], v[100:103], a[192:207]\n"
    "ds_read_b64_tr_b16 a[228:229], %[rd1] offset:16384\n"
    "ds_read_b64_tr_b16 a[230:231], %[rd1] offset:17408\n"
    "s_add_u32 m0, %[ldsw], 114688\n"
    "s_mov_b32 s40, 369024\n"
    "v_add_f32 v21, 1.0, v21\n"
    "v_rcp_f32 v21, v21\n"
    "v_mfma_f32_32x32x16_bf16 a[208:223], a[236:239], v[100:103], a[208:223]\n"
    "s_waitcnt lgkmcnt(5)\n"
    "ds_read_b64_tr_b16 a[232:233], %[rd0] offset:18432\n"
    "ds_read_b64_tr_b16 a[234:235], %[rd0] offset:19456\n"
    "buffer_load_dwordx4 %[w2a], %[srd2], s40 offen lds\n"
    "v_mul_f32 v22, v22, v152\n"
    "v_mul_f32 v21, v21, v151\n"
    "v_cvt_pk_bf16_f32 v115, v20, v21\n"
    "v_mfma_f32_32x32x16_bf16 a[192:207], a[240:243], v[104:107], a[192:207]\n"
    "ds_read_b64_tr_b16 a[236:237], %[rd1] offset:18432\n"
    "ds_read_b64_tr_b16 a[238:239], %[rd1] offset:19456\n"
    "v_exp_f32 v22, v22\n"
    "v_fmaak_f32 v20, v153, v153, 0x409d30fc\n"
    "v_mfma_f32_32x32x16_bf16 a[208:223], a[244:247], v[104:107], a[208:223]\n"
    "ds_read_b64_tr_b16 a[240:241], %[rd0] offset:20480\n"
    "ds_read_b64_tr_b16 a[242:243], %[rd0] offset:21504\n"
    "v_add_f32 v22, 1.0, v22\n"
    "v_rcp_f32 v22, v22\n"
    "v_mfma_f32_32x32x16_bf16 a[192:207], a[248:251], v[108:111], a[192:207]\n"
    "ds_read_b64_tr_b16 a[244:245], %[rd1] offset:20480\n"
    "ds_read_b64_tr_b16 a[246:247], %[rd1] offset:21504\n"
    "v_mul_f32 v20, v20, v153\n"
    "v_mul_f32 v22, v22, v152\n"
    "v_mfma_f32_32x32x16_bf16 a[208:223], a[252:255], v[108:111], a[208:223]\n"
    "s_waitcnt lgkmcnt(4)\n"
    "ds_read_b64_tr_b16 a[248:249], %[rd0] offset:22528\n"
    "ds_read_b64_tr_b16 a[250:251], %[rd0] offset:23552\n"
    "buffer_load_dwordx4 %[w2b], %[srd2], s40 offen offset:1024 lds\n"
    "v_exp_f32 v20, v20\n"
    "v_fmaak_f32 v21, v154, v154, 0x409d30fc\n"
    "v_mfma_f32_32x32x16_bf16 v[176:191], a[224:227], a[0:3], v[176:191]\n"
    "ds_read_b64_tr_b16 a[252:253], %[rd1] offset:22528\n"
    "ds_read_b64_tr_b16 a[254:255], %[rd1] offset:23552\n"
    "v_add_f32 v20, 1.0, v20\n"
    "v_rcp_f32 v20, v20\n"
    "v_mul_f32 v21, v21, v154\n"
    "v_mfma_f32_32x32x16_bf16 v[192:207], a[228:231], a[0:3], v[192:207]\n"
    "s_waitcnt vmcnt(18)\n"
    "s_barrier\n"
    "ds_read_b64_tr_b16 a[224:225], %[rd0] offset:24576\n"
    "ds_read_b64_tr_b16 a[226:227], %[rd0] offset:25600\n"
    "v_mfma_f32_32x32x16_bf16 v[176:191], a[232:235], a[4:7], v[176:191]\n"
    "ds_read_b64_tr_b16 a[228:229], %[rd1] offset:24576\n"
    "ds_read_b64_tr_b16 a[230:231], %[rd1] offset:25600\n"
    "s_add_u32 m0, %[ldsw], 122880\n"
    "s_mov_b32 s40, 896\n"
    "v_mul_f32 v20, v20, v153\n"
    "v_cvt_pk_bf16_f32 v116, v22, v20\n"
    "v_mfma_f32_32x32x16_bf16 v[192:207], a[236:239], a[4:7], v[192:207]\n"
    "s_waitcnt lgkmcnt(4)\n"
    "ds_read_b64_tr_b16 a[232:233], %[rd0] offset:26624\n"
    "ds_read_b64_tr_b16 a[234:235], %[rd0] offset:27648\n"
    "buffer_load_dwordx4 %[w1a], %[srd1], s40 offen lds\n"
    "v_exp_f32 v21, v21\n"
    "v_fmaak_f32 v22, v155, v155, 0x409d30fc\n"
    "v_mfma_f32_32x32x16_bf16 v[176:191], a[240:243], a[8:11], v[176:191]\n"
    "ds_read_b64_tr_b16 a[236:237], %[rd1] offset:26624\n"
    "ds_read_b64_tr_b16 a[238:239], %[rd1] offset:27648\n"
    "v_add_f32 v21, 1.0, v21\n"
    "v_rcp_f32 v21, v21\n"
    "v_mfma_f32_32x32x16_bf16 v[192:207], a[244:247], a[8:11], v[192:207]\n"
    "ds_read_b64_tr_b16 a[240:241], %[rd0] offset:28672\n"
    "ds_read_b64_tr_b16 a[242:243], %[rd0] offset:29696\n"
    "v_mul_f32 v22, v22, v155\n"
    "v_mul_f32 v21, v21, v154\n"
    "v_mfma_f32_32x32x16_bf16 v[176:191], a[248:251], a[12:15], v[176:191]\n"
    "ds_read_b64_tr_b16 a[244:245], %[rd1] offset:28672\n"
    "ds_read_b64_tr_b16 a[246:247], %[rd1] offset:29696\n"
    "v_exp_f32 v22, v22\n"
    "v_fmaak_f32 v20, v156, v156, 0x409d30fc\n"
    "v_add_f32 v22, 1.0, v22\n"
    "v_mfma_f32_32x32x16_bf16 v[192:207], a[252:255], a[12:15], v[192:207]\n"
    "s_waitcnt lgkmcnt(4)\n"
    "ds_read_b64_tr_b16 a[248:249], %[rd0] offset:30720\n"
    "ds_read_b64_tr_b16 a[250:251], %[rd0] offset:31744\n"
    "buffer_load_dwordx4 %[w1b], %[srd1], s40 offen offset:1024 lds\n"
    "v_rcp_f32 v22, v22\n"
    "v_mul_f32 v20, v20, v156\n"
    "v_mfma_f32_32x32x16_bf16 v[176:191], a[224:227], a[16:19], v[176:191]\n"
    "ds_read_b64_tr_b16 a[252:253], %[rd1] offset:30720\n"
    "ds_read_b64_tr_b16 a[254:255], %[rd1] offset:31744\n"
    "v_mul_f32 v22, v22, v155\n"
    "v_cvt_pk_bf16_f32 v117, v21, v22\n"
    "v_mfma_f32_32x32x16_bf16 v[192:207], a[228:231], a[16:19], v[192:207]\n"
    "ds_read_b64_tr_b16 a[224:225], %[rd0] offset:32768\n"
    "ds_read_b64_tr_b16 a[226:227], %[rd0] offset:33792\n"
    "v_exp_f32 v20, v20\n"
    "v_fmaak_f32 v21, v157, v157, 0x409d30fc\n"
    "v_mfma_f32_32x32x16_bf16 v[176:191], a[232:235], a[20:23], v[176:191]\n"
    "ds_read_b64_tr_b16 a[228:229], %[rd1] offset:32768\n"
    "ds_read_b64_tr_b16 a[230:231], %[rd1] offset:33792\n"
    "s_add_u32 m0, %[ldsw], 131072\n"
    "s_mov_b32 s40, 590720\n"
    "v_add_f32 v20, 1.0, v20\n"
    "v_rcp_f32 v20, v20\n"
    "v_mfma_f32_32x32x16_bf16 v[192:207], a[236:239], a[20:23], v[192:207]\n"
    "s_waitcnt lgkmcnt(4)\n"
    "ds_read_b64_tr_b16 a[232:233], %[rd0] offset:34816\n"
    "ds_read_b64_tr_b16 a[234:235], %[rd0] offset:35840\n"
    "buffer_load_dwordx4 %[w1a], %[srd1], s40 offen lds\n"
    "v_mul_f32 v21, v21, v157\n"
    "v_mul_f32 v20, v20, v156\n"
    "v_exp_f32 v21, v21\n"
    "v_mfma_f32_32x32x16_bf16 v[176:191], a[240:243], a[24:27], v[176:191]\n"
    "ds_read_b64_tr_b16 a[236:237], %[rd1] offset:34816\n"
    "ds_read_b64_tr_b16 a[238:239], %[rd1] offset:35840\n"
    "v_fmaak_f32 v22, v158, v158, 0x409d30fc\n"
    "v_add_f32 v21, 1.0, v21\n"
    "v_mfma_f32_32x32x16_bf16 v[192:207], a[244:247], a[24:27], v[192:207]\n"
    "ds_read_b64_tr_b16 a[240:241], %[rd0] offset:36864\n"
    "ds_read_b64_tr_b16 a[242:243], %[rd0] offset:37888\n"
    "v_rcp_f32 v21, v21\n"
    "v_mul_f32 v22, v22, v158\n"
    "v_mfma_f32_32x32x16_bf16 v[176:191], a[248:251], a[28:31], v[176:191]\n"
    "ds_read_b64_tr_b16 a[244:245], %[rd1] offset:36864\n"
    "ds_read_b64_tr_b16 a[246:247], %[rd1] offset:37888\n"
    "v_mul_f32 v21, v21, v157\n"
    "v_cvt_pk_bf16_f32 v118, v20, v21\n"
    "v_mfma_f32_32x32x16_bf16 v[192:207], a[252:255], a[28:31], v[192:207]\n"
    "s_waitcnt lgkmcnt(4)\n"
    "ds_read_b64_tr_b16 a[248:249], %[rd0] offset:38912\n"
    "ds_read_b64_tr_b16 a[250:251], %[rd0] offset:39936\n"
    "buffer_load_dwordx4 %[w1b], %[srd1], s40 offen offset:1024 lds\n"
    "v_exp_f32 v22, v22\n"
    "v_fmaak_f32 v20, v159, v159, 0x409d30fc\n"
    "v_add_f32 v22, 1.0, v22\n"
    "v_mfma_f32_32x32x16_bf16 v[176:191], a[224:227], a[32:35], v[176:191]\n"
    "ds_read_b64_tr_b16 a[252:253], %[rd1] offset:38912\n"
    "ds_read_b64_tr_b16 a[254:255], %[rd1] offset:39936\n"
    "v_rcp_f32 v22, v22\n"
    "v_mul_f32 v20, v20, v159\n"
    "v_mfma_f32_32x32x16_bf16 v[192:207], a[228:231], a[32:35], v[192:207]\n"
    "ds_read_b64_tr_b16 a[224:225], %[rd0] offset:40960\n"
    "ds_read_b64_tr_b16 a[226:227], %[rd0] offset:41984\n"
    "v_mul_f32 v22, v22, v158\n"
    "v_exp_f32 v20, v20\n"
    "v_mfma_f32_32x32x16_bf16 v[176:191], a[232:235], a[36:39], v[176:191]\n"
    "ds_read_b64_tr_b16 a[228:229], %[rd1] offset:40960\n"
    "ds_read_b64_tr_b16 a[230:231], %[rd1] offset:41984\n"
    "s_add_u32 m0, %[ldsw], 139264\n"
    "s_mov_b32 s40, 1180544\n"
    "v_fmaak_f32 v21, v160, v160, 0x409d30fc\n"
    "v_add_f32 v20, 1.0, v20\n"
    "v_mfma_f32_32x32x16_bf16 v[192:207], a[236:239], a[36:39], v[192:207]\n"
    "s_waitcnt lgkmcnt(4)\n"
    "ds_read_b64_tr_b16 a[232:233], %[rd0] offset:43008\n"
    "ds_read_b64_tr_b16 a[234:235], %[rd0] offset:44032\n"
    "buffer_load_dwordx4 %[w1a], %[srd1], s40 offen lds\n"
    "v_rcp_f32 v20, v20\n"
    "v_mul_f32 v21, v21, v160\n"
    "v_mfma_f32_32x32x16_bf16 v[176:191], a[240:243], a[40:43], v[176:191]\n"
    "ds_read_b64_tr_b16 a[236:237], %[rd1] offset:43008\n"
    "ds_read_b64_tr_b16 a[238:239], %[rd1] offset:44032\n"
    "v_mul_f32 v20, v20, v159\n"
    "v_cvt_pk_bf16_f32 v119, v22, v20\n"
    "v_exp_f32 v21, v21\n"
    "v_mfma_f32_32x32x16_bf16 v[192:207], a[244:247], a[40:43], v[192:207]\n"
    "ds_read_b64_tr_b16 a[240:241], %[rd0] offset:45056\n"
    "ds_read_b64_tr_b16 a[242:243], %[rd0] offset:46080\n"
    "v_fmaak_f32 v22, v161, v161, 0x409d30fc\n"
    "v_add_f32 v21, 1.0, v21\n"
    "v_mfma_f32_32x32x16_bf16 v[176:191], a[248:251], a[44:47], v[176:191]\n"
    "ds_read_b64_tr_b16 a[244:245], %[rd1] offset:45056\n"
    "ds_read_b64_tr_b16 a[246:247], %[rd1] offset:46080\n"
    "v_rcp_f32 v21, v21\n"
    "v_mul_f32 v22, v22, v161\n"
    "v_mfma_f32_32x32x16_bf16 v[192:207], a[252:255], a[44:47], v[192:207]\n"
    "s_waitcnt lgkmcnt(4)\n"
    "ds_read_b64_tr_b16 a[248:249], %[rd0] offset:47104\n"
    "ds_read_b64_tr_b16 a[250:251], %[rd0] offset:48128\n"
    "buffer_load_dwordx4 %[w1b], %[srd1], s40 offen offset:1024 lds\n"
    "v_mul_f32 v21, v21, v160\n"
    "v_exp_f32 v22, v22\n"
    "v_mfma_f32_32x32x16_bf16 v[176:191], a[224:227], a[48:51], v[176:191]\n"
    "ds_read_b64_tr_b16 a[252:253], %[rd1] offset:47104\n"
    "ds_read_b64_tr_b16 a[254:255], %[rd1] offset:48128\n"
    "v_fmaak_f32 v20, v162, v162, 0x409d30fc\n"
    "v_add_f32 v22, 1.0, v22\n"
    "v_mfma_f32_32x32x16_bf16 v[192:207], a[228:231], a[48:51], v[192:207]\n"
    "s_waitcnt vmcnt(18)\n"
    "s_barrier\n"
    "ds_read_b64_tr_b16 a[224:225], v16 offset:0\n"
    "ds_read_b64_tr_b16 a[226:227], v16 offset:1024\n"
    "v_mfma_f32_32x32x16_bf16 v[176:191], a[232:235], a[52:55], v[176:191]\n"
    "ds_read_b64_tr_b16 a[228:229], v17 offset:0\n"
    "ds_read_b64_tr_b16 a[230:231], v17 offset:1024\n"
    "s_add_u32 m0, %[ldsw], 0\n"
    "s_mov_b32 s40, 1770368\n"
    "v_rcp_f32 v22, v22\n"
    "v_mul_f32 v20, v20, v162\n"
    "v_mul_f32 v22, v22, v161\n"
    "v_mfma_f32_32x32x16_bf16 v[192:207], a[236:239], a[52:55], v[192:207]\n"
    "s_waitcnt lgkmcnt(4)\n"
    "ds_read_b64_tr_b16 a[232:233], v16 offset:2048\n"
    "ds_read_b64_tr_b16 a[234:235], v16 offset:3072\n"
    "buffer_load_dwordx4 %[w1a], %[srd1], s40 offen lds\n"
    "v_cvt_pk_bf16_f32 v120, v21, v22\n"
    "v_exp_f32 v20, v20\n"
    "v_mfma_f32_32x32x16_bf16 v[176:191], a[240:243], a[56:59], v[176:191]\n"
    "ds_read_b64_tr_b16 a[236:237], v17 offset:2048\n"
    "ds_read_b64_tr_b16 a[238:239], v17 offset:3072\n"
    "v_fmaak_f32 v21, v163, v163, 0x409d30fc\n"
    "v_add_f32 v20, 1.0, v20\n"
    "v_mfma_f32_32x32x16_bf16 v[192:207], a[244:247], a[56:59], v[192:207]\n"
    "ds_read_b64_tr_b16 a[240:241], v16 offset:4096\n"
    "ds_read_b64_tr_b16 a[242:243], v16 offset:5120\n"
    "v_rcp_f32 v20, v20\n"
    "v_mul_f32 v21, v21, v163\n"
    "v_mfma_f32_32x32x16_bf16 v[176:191], a[248:251], a[60:63], v[176:191]\n"
    "ds_read_b64_tr_b16 a[244:245], v17 offset:4096\n"
    "ds_read_b64_tr_b16 a[246:247], v17 offset:5120\n"
    "v_mul_f32 v20, v20, v162\n"
    "v_exp_f32 v21, v21\n"
    "v_mfma_f32_32x32x16_bf16 v[192:207], a[252:255], a[60:63], v[192:207]\n"
    "s_waitcnt lgkmcnt(4)\n"
    "ds_read_b64_tr_b16 a[248:249], v16 offset:6144\n"
    "ds_read_b64_tr_b16 a[250:251], v16 offset:7168\n"
    "buffer_load_dwordx4 %[w1b], %[srd1], s40 offen offset:1024 lds\n"
    "v_fmaak_f32 v22, v164, v164, 0x409d30fc\n"
    "v_add_f32 v21, 1.0, v21\n"
    "v_rcp_f32 v21, v21\n"
    "v_mfma_f32_32x32x16_bf16 v[176:191], a[224:227], a[64:67], v[176:191]\n"
    "ds_read_b64_tr_b16 a[252:253], v17 offset:6144\n"
    "ds_read_b64_tr_b16 a[254:255], v17 offset:7168\n"
    "v_mul_f32 v22, v22, v164\n"
    "v_mul_f32 v21, v21, v163\n"
    "v_mfma_f32_32x32x16_bf16 v[192:207], a[228:231], a[64:67], v[192:207]\n"
    "ds_read_b64_tr_b16 a[224:225], v16 offset:8192\n"
    "ds_read_b64_tr_b16 a[226:227], v16 offset:9216\n"
    "v_cvt_pk_bf16_f32 v121, v20, v21\n"
    "v_exp_f32 v22, v22\n"
    "v_mfma_f32_32x32x16_bf16 v[176:191], a[232:235], a[68:71], v[176:191]\n"
    "ds_read_b64_tr_b16 a[228:229], v17 offset:8192\n"
    "ds_read_b64_tr_b16 a[230:231], v17 offset:9216\n"
    "s_add_u32 m0, %[ldsw], 8192\n"
    "s_mov_b32 s40, 2360192\n"
    "v_fmaak_f32 v20, v165, v165, 0x409d30fc\n"
    "v_add_f32 v22, 1.0, v22\n"
    "v_mfma_f32_32x32x16_bf16 v[192:207], a[236:239], a[68:71], v[192:207]\n"
    "s_waitcnt lgkmcnt(4)\n"
    "ds_read_b64_tr_b16 a[232:233], v16 offset:10240\n"
    "ds_read_b64_tr_b16 a[234:235], v16 offset:11264\n"
    "buffer_load_dwordx4 %[w1a], %[srd1], s40 offen lds\n"
    "v_rcp_f32 v22, v22\n"
    "v_mul_f32 v20, v20, v165\n"
    "v_mul_f32 v22, v22, v164\n"
    "v_mfma_f32_32x32x16_bf16 v[176:191], a[240:243], a[72:75], v[176:191]\n"
    "ds_read_b64_tr_b16 a[236:237], v17 offset:10240\n"
    "ds_read_b64_tr_b16 a[238:239], v17 offset:11264\n"
    "v_exp_f32 v20, v20\n"
    "v_fmaak_f32 v21, v166, v166, 0x409d30fc\n"
    "v_mfma_f32_32x32x16_bf16 v[192:207], a[244:247], a[72:75], v[192:207]\n"
    "ds_read_b64_tr_b16 a[240:241], v16 offset:12288\n"
    "ds_read_b64_tr_b16 a[242:243], v16 offset:13312\n"
    "v_add_f32 v20, 1.0, v20\n"
    "v_rcp_f32 v20, v20\n"
    "v_mfma_f32_32x32x16_bf16 v[176:191], a[248:251], a[76:79], v[176:191]\n"
    "ds_read_b64_tr_b16 a[244:245], v17 offset:12288\n"
    "ds_read_b64_tr_b16 a[246:247], v17 offset:13312\n"
    "v_mul_f32 v21, v21, v166\n"
    "v_mul_f32 v20, v20, v165\n"
    "v_mfma_f32_32x32x16_bf16 v[192:207], a[252:255], a[76:79], v[192:207]\n"
    "s_waitcnt lgkmcnt(4)\n"
    "ds_read_b64_tr_b16 a[248:249], v16 offset:14336\n"
    "ds_read_b64_tr_b16 a[250:251], v16 offset:15360\n"
    "buffer_load_dwordx4 %[w1b], %[srd1], s40 offen offset:1024 lds\n"
    "v_cvt_pk_bf16_f32 v122, v22, v20\n"
    "v_exp_f32 v21, v21\n"
    "v_mfma_f32_32x32x16_bf16 v[176:191], a[224:227], a[80:83], v[176:191]\n"
    "ds_read_b64_tr_b16 a[252:253], v17 offset:14336\n"
    "ds_read_b64_tr_b16 a[254:255], v17 offset:15360\n"
    "v_fmaak_f32 v22, v167, v167, 0x409d30fc\n"
    "v_add_f32 v21, 1.0, v21\n"
    "v_rcp_f32 v21, v21\n"
    "v_mfma_f32_32x32x16_bf16 v[192:207], a[228:231], a[80:83], v[192:207]\n"
    "ds_read_b64_tr_b16 a[224:225], v16 offset:16384\n"
    "ds_read_b64_tr_b16 a[226:227], v16 offset:17408\n"
    "v_mul_f32 v22, v22, v167\n"
    "v_mul_f32 v21, v21, v166\n"
    "v_mfma_f32_32x32x16_bf16 v[176:191], a[232:235], a[84:87], v[176:191]\n"
    "ds_read_b64_tr_b16 a[228:229], v17 offset:16384\n"
    "ds_read_b64_tr_b16 a[230:231], v17 offset:17408\n"
    "s_add_u32 m0, %[ldsw], 16384\n"
    "s_mov_b32 s40, 2950016\n"
    "v_exp_f32 v22, v22\n"
    "v_fmaak_f32 v20, v168, v168, 0x409d30fc\n"
    "v_mfma_f32_32x32x16_bf16 v[192:207], a[236:239], a[84:87], v[192:207]\n"
    "s_waitcnt lgkmcnt(4)\n"
    "ds_read_b64_tr_b16 a[232:233], v16 offset:18432\n"
    "ds_read_b64_tr_b16 a[234:235], v16 offset:19456\n"
    "buffer_load_dwordx4 %[w1a], %[srd1], s40 offen lds\n"
    "v_add_f32 v22, 1.0, v22\n"
    "v_rcp_f32 v22, v22\n"
    "v_mfma_f32_32x32x16_bf16 v[176:191], a[240:243], a[88:91], v[176:191]\n"
    "ds_read_b64_tr_b16 a[236:237], v17 offset:18432\n"
    "ds_read_b64_tr_b16 a[238:239], v17 offset:19456\n"
    "v_mul_f32 v20, v20, v168\n"
    "v_mul_f32 v22, v22, v167\n"
    "v_mfma_f32_32x32x16_bf16 v[192:207], a[244:247], a[88:91], v[192:207]\n"
    "ds_read_b64_tr_b16 a[240:241], v16 offset:20480\n"
    "ds_read_b64_tr_b16 a[242:243], v16 offset:21504\n"
    "v_cvt_pk_bf16_f32 v123, v21, v22\n"
    "v_exp_f32 v20, v20\n"
    "v_fmaak_f32 v21, v169, v169, 0x409d30fc\n"
    "v_mfma_f32_32x32x16_bf16 v[176:191], a[248:251], a[92:95], v[176:191]\n"
    "ds_read_b64_tr_b16 a[244:245], v17 offset:20480\n"
    "ds_read_b64_tr_b16 a[246:247], v17 offset:21504\n"
    "v_add_f32 v20, 1.0, v20\n"
    "v_rcp_f32 v20, v20\n"
    "v_mfma_f32_32x32x16_bf16 v[192:207], a[252:255], a[92:95], v[192:207]\n"
    "s_waitcnt lgkmcnt(4)\n"
    "ds_read_b64_tr_b16 a[248:249], v16 offset:22528\n"
    "ds_read_b64_tr_b16 a[250:251], v16 offset:23552\n"
    "buffer_load_dwordx4 %[w1b], %[srd1], s40 offen offset:1024 lds\n"
    "v_mul_f32 v21, v21, v169\n"
    "v_mul_f32 v20, v20, v168\n"
    "v_mfma_f32_32x32x16_bf16 v[176:191], a[224:227], a[96:99], v[176:191]\n"
    "ds_read_b64_tr_b16 a[252:253], v17 offset:22528\n"
    "ds_read_b64_tr_b16 a[254:255], v17 offset:23552\n"
    "v_exp_f32 v21, v21\n"
    "v_fmaak_f32 v22, v170, v170, 0x409d30fc\n"
    "v_mfma_f32_32x32x16_bf16 v[192:207], a[228:231], a[96:99], v[192:207]\n"
    "s_waitcnt vmcnt(18)\n"
    "s_barrier\n"
    "ds_read_b64_tr_b16 a[224:225], v16 offset:24576\n"
    "ds_read_b64_tr_b16 a[226:227], v16 offset:25600\n"
    "v_mfma_f32_32x32x16_bf16 v[176:191], a[232:235], a[100:103], v[176:191]\n"
    "ds_read_b64_tr_b16 a[228:229], v17 offset:24576\n"
    "ds_read_b64_tr_b16 a[230:231], v17 offset:25600\n"
    "s_add_u32 m0, %[ldsw], 24576\n"
    "s_mov_b32 s40, 3539840\n"
    "v_add_f32 v21, 1.0, v21\n"
    "v_rcp_f32 v21, v21\n"
    "v_mfma_f32_32x32x16_bf16 v[192:207], a[236:239], a[100:103], v[192:207]\n"
    "s_waitcnt lgkmcnt(4)\n"
    "ds_read_b64_tr_b16 a[232:233], v16 offset:26624\n"
    "ds_read_b64_tr_b16 a[234:235], v16 offset:27648\n"
    "buffer_load_dwordx4 %[w1a], %[srd1], s40 offen lds\n"
    "v_mul_f32 v22, v22, v170\n"
    "v_mul_f32 v21, v21, v169\n"
    "v_cvt_pk_bf16_f32 v124, v20, v21\n"
    "v_mfma_f32_32x32x16_bf16 v[176:191], a[240:243], a[104:107], v[176:191]\n"
    "ds_read_b64_tr_b16 a[236:237], v17 offset:26624\n"
    "ds_read_b64_tr_b16 a[238:239], v17 offset:27648\n"
    "v_exp_f32 v22, v22\n"
    "v_fmaak_f32 v20, v171, v171, 0x409d30fc\n"
    "v_mfma_f32_32x32x16_bf16 v[192:207], a[244:247], a[104:107], v[192:207]\n"
    "ds_read_b64_tr_b16 a[240:241], v16 offset:28672\n"
    "ds_read_b64_tr_b16 a[242:243], v16 offset:29696\n"
    "v_add_f32 v22, 1.0, v22\n"
    "v_rcp_f32 v22, v22\n"
    "v_mfma_f32_32x32x16_bf16 v[176:191], a[248:251], a[108:111], v[176:191]\n"
    "ds_read_b64_tr_b16 a[244:245], v17 offset:28672\n"
    "ds_read_b64_tr_b16 a[246:247], v17 offset:29696\n"
    "v_mul_f32 v20, v20, v171\n"
    "v_mul_f32 v22, v22, v170\n"
    "v_mfma_f32_32x32x16_bf16 v[192:207], a[252:255], a[108:111], v[192:207]\n"
    "s_waitcnt lgkmcnt(4)\n"
    "ds_read_b64_tr_b16 a[248:249], v16 offset:30720\n"
    "ds_read_b64_tr_b16 a[250:251], v16 offset:31744\n"
    "buffer_load_dwordx4 %[w1b], %[srd1], s40 offen offset:1024 lds\n"
    "v_exp_f32 v20, v20\n"
    "v_fmaak_f32 v21, v172, v172, 0x409d30fc\n"
    "v_add_f32 v20, 1.0, v20\n"
    "v_mfma_f32_32x32x16_bf16 v[176:191], a[224:227], a[112:115], v[176:191]\n"
    "ds_read_b64_tr_b16 a[252:253], v17 offset:30720\n"
    "ds_read_b64_tr_b16 a[254:255], v17 offset:31744\n"
    "v_rcp_f32 v20, v20\n"
    "v_mul_f32 v21, v21, v172\n"
    "v_mfma_f32_32x32x16_bf16 v[192:207], a[228:231], a[112:115], v[192:207]\n"
    "ds_read_b64_tr_b16 a[224:225], v16 offset:32768\n"
    "ds_read_b64_tr_b16 a[226:227], v16 offset:33792\n"
    "v_mul_f32 v20, v20, v171\n"
    "v_cvt_pk_bf16_f32 v125, v22, v20\n"
    "v_mfma_f32_32x32x16_bf16 v[176:191], a[232:235], a[116:119], v[176:191]\n"
    "ds_read_b64_tr_b16 a[228:229], v17 offset:32768\n"
    "ds_read_b64_tr_b16 a[230:231], v17 offset:33792\n"
    "s_add_u32 m0, %[ldsw], 32768\n"
    "s_mov_b32 s40, 4129664\n"
    "v_exp_f32 v21, v21\n"
    "v_fmaak_f32 v22, v173, v173, 0x409d30fc\n"
    "v_mfma_f32_32x32x16_bf16 v[192:207], a[236:239], a[116:119], v[192:207]\n"
    "s_waitcnt lgkmcnt(4)\n"
    "ds_read_b64_tr_b16 a[232:233], v16 offset:34816\n"
    "ds_read_b64_tr_b16 a[234:235], v16 offset:35840\n"
    "buffer_load_dwordx4 %[w1a], %[srd1], s40 offen lds\n"
    "v_add_f32 v21, 1.0, v21\n"
    "v_rcp_f32 v21, v21\n"
    "v_mfma_f32_32x32x16_bf16 v[176:191], a[240:243], a[120:123], v[176:191]\n"
    "ds_read_b64_tr_b16 a[236:237], v17 offset:34816\n"
    "ds_read_b64_tr_b16 a[238:239], v17 offset:35840\n"
    "v_mul_f32 v22, v22, v173\n"
    "v_mul_f32 v21, v21, v172\n"
    "v_exp_f32 v22, v22\n"
    "v_mfma_f32_32x32x16_bf16 v[192:207], a[244:247], a[120:123], v[192:207]\n"
    "ds_read_b64_tr_b16 a[240:241], v16 offset:36864\n"
    "ds_read_b64_tr_b16 a[242:243], v16 offset:37888\n"
    "v_fmaak_f32 v20, v174, v174, 0x409d30fc\n"
    "v_add_f32 v22, 1.0, v22\n"
    "v_mfma_f32_32x32x16_bf16 v[176:191], a[248:251], a[124:127], v[176:191]\n"
    "ds_read_b64_tr_b16 a[244:245], v17 offset:36864\n"
    "ds_read_b64_tr_b16 a[246:247], v17 offset:37888\n"
    "v_rcp_f32 v22, v22\n"
    "v_mul_f32 v20, v20, v174\n"
    "v_mfma_f32_32x32x16_bf16 v[192:207], a[252:255], a[124:127], v[192:207]\n"
    "s_waitcnt lgkmcnt(4)\n"
    "ds_read_b64_tr_b16 a[248:249], v16 offset:38912\n"
    "ds_read_b64_tr_b16 a[250:251], v16 offset:39936\n"
    "buffer_load_dwordx4 %[w1b], %[srd1], s40 offen offset:1024 lds\n"
    "v_mul_f32 v22, v22, v173\n"
    "v_cvt_pk_bf16_f32 v126, v21, v22\n"
    "v_mfma_f32_32x32x16_bf16 v[176:191], a[224:227], a[128:131], v[176:191]\n"
    "ds_read_b64_tr_b16 a[252:253], v17 offset:38912\n"
    "ds_read_b64_tr_b16 a[254:255], v17 offset:39936\n"
    "v_exp_f32 v20, v20\n"
    "v_fmaak_f32 v21, v175, v175, 0x409d30fc\n"
    "v_mfma_f32_32x32x16_bf16 v[192:207], a[228:231], a[128:131], v[192:207]\n"
    "ds_read_b64_tr_b16 a[224:225], v16 offset:40960\n"
    "ds_read_b64_tr_b16 a[226:227], v16 offset:41984\n"
    "v_add_f32 v20, 1.0, v20\n"
    "v_rcp_f32 v20, v20\n"
    "v_mul_f32 v21, v21, v175\n"
    "v_mfma_f32_32x32x16_bf16 v[176:191], a[232:235], a[132:135], v[176:191]\n"
    "ds_read_b64_tr_b16 a[228:229], v17 offset:40960\n"
    "ds_read_b64_tr_b16 a[230:231], v17 offset:41984\n"
    "s_add_u32 m0, %[ldsw], 40960\n"
    "s_mov_b32 s40, 4719488\n"
    "v_mul_f32 v20, v20, v174\n"
    "v_exp_f32 v21, v21\n"
    "v_mfma_f32_32x32x16_bf16 v[192:207], a[236:239], a[132:135], v[192:207]\n"
    "s_waitcnt lgkmcnt(4)\n"
    "ds_read_b64_tr_b16 a[232:233], v16 offset:43008\n"
    "ds_read_b64_tr_b16 a[234:235], v16 offset:44032\n"
    "buffer_load_dwordx4 %[w1a], %[srd1], s40 offen lds\n"
    "s_nop 0\n"
    "v_add_f32 v21, 1.0, v21\n"
    "v_mfma_f32_32x32x16_bf16 v[176:191], a[240:243], a[136:139], v[176:191]\n"
    "ds_read_b64_tr_b16 a[236:237], v17 offset:43008\n"
    "ds_read_b64_tr_b16 a[238:239], v17 offset:44032\n"
    "v_rcp_f32 v21, v21\n"
    "s_nop 0\n"
    "v_mfma_f32_32x32x16_bf16 v[192:207], a[244:247], a[136:139], v[192:207]\n"
    "ds_read_b64_tr_b16 a[240:241], v16 offset:45056\n"
    "ds_read_b64_tr_b16 a[242:243], v16 offset:46080\n"
    "v_mul_f32 v21, v21, v175\n"
    "v_cvt_pk_bf16_f32 v127, v20, v21\n"
    "v_mfma_f32_32x32x16_bf16 v[176:191], a[248:251], a[140:143], v[176:191]\n"
    "ds_read_b64_tr_b16 a[244:245], v17 offset:45056\n"
    "ds_read_b64_tr_b16 a[246:247], v17 offset:46080\n"
    "v_mfma_f32_32x32x16_bf16 v[192:207], a[252:255], a[140:143], v[192:207]\n"
    "s_waitcnt lgkmcnt(4)\n"
    "ds_read_b64_tr_b16 a[248:249], v16 offset:47104\n"
    "ds_read_b64_tr_b16 a[250:251], v16 offset:48128\n"
    "buffer_load_dwordx4 %[w1b], %[srd1], s40 offen offset:1024 lds\n"
    "v_mfma_f32_32x32x16_bf16 v[208:223], a[224:227], v[112:115], v[208:223]\n"
    "ds_read_b64_tr_b16 a[252:253], v17 offset:47104\n"
    "ds_read_b64_tr_b16 a[254:255], v17 offset:48128\n"
    "v_mfma_f32_32x32x16_bf16 v[224:239], a[228:231], v[112:115], v[224:239]\n"
    "s_waitcnt vmcnt(18)\n"
    "s_barrier\n"
    "ds_read_b64_tr_b16 a[224:225], v18 offset:0\n"
    "ds_read_b64_tr_b16 a[226:227], v18 offset:1024\n"
    "v_mfma_f32_32x32x16_bf16 v[208:223], a[232:235], v[116:119], v[208:223]\n"
    "ds_read_b64_tr_b16 a[228:229], v19 offset:0\n"
    "ds_read_b64_tr_b16 a[230:231], v19 offset:1024\n"
    "s_add_u32 m0, %[ldsw], 49152\n"
    "s_mov_b32 s40, 442368\n"
    "ds_read_b128 v[144:147], %[vb] offset:1792\n"
    "v_mfma_f32_32x32x16_bf16 v[224:239], a[236:239], v[116:119], v[224:239]\n"
    "s_waitcnt lgkmcnt(5)\n"
    "ds_read_b64_tr_b16 a[232:233], v18 offset:2048\n"
    "ds_read_b64_tr_b16 a[234:235], v18 offset:3072\n"
    "buffer_load_dwordx4 %[w2a], %[srd2], s40 offen lds\n"
    "v_fmaak_f32 v20, v176, v176, 0x409d30fc\n"
    "v_mul_f32 v20, v20, v176\n"
    "v_exp_f32 v20, v20\n"
    "v_mfma_f32_32x32x16_bf16 v[208:223], a[240:243], v[120:123], v[208:223]\n"
    "ds_read_b64_tr_b16 a[236:237], v19 offset:2048\n"
    "ds_read_b64_tr_b16 a[238:239], v19 offset:3072\n"
    "v_fmaak_f32 v21, v177, v177, 0x409d30fc\n"
    "v_add_f32 v20, 1.0, v20\n"
    "v_mfma_f32_32x32x16_bf16 v[224:239], a[244:247], v[120:123], v[224:239]\n"
    "ds_read_b64_tr_b16 a[240:241], v18 offset:4096\n"
    "ds_read_b64_tr_b16 a[242:243], v18 offset:5120\n"
    "ds_read_b128 v[148:151], %[vb] offset:1824\n"
    "v_rcp_f32 v20, v20\n"
    "v_mul_f32 v21, v21, v177\n"
    "v_mfma_f32_32x32x16_bf16 v[208:223], a[248:251], v[124:127], v[208:223]\n"
    "ds_read_b64_tr_b16 a[244:245], v19 offset:4096\n"
    "ds_read_b64_tr_b16 a[246:247], v19 offset:5120\n"
    "v_mul_f32 v20, v20, v176\n"
    "v_exp_f32 v21, v21\n"
    "v_mfma_f32_32x32x16_bf16 v[224:239], a[252:255], v[124:127], v[224:239]\n"
    "s_waitcnt lgkmcnt(5)\n"
    "ds_read_b64_tr_b16 a[248:249], v18 offset:6144\n"
    "ds_read_b64_tr_b16 a[250:251], v18 offset:7168\n"
    "buffer_load_dwordx4 %[w2b], %[srd2], s40 offen offset:1024 lds\n"
    "v_fmaak_f32 v22, v178, v178, 0x409d30fc\n"
    "v_add_f32 v21, 1.0, v21\n"
    "v_rcp_f32 v21, v21\n"
    "v_mfma_f32_32x32x16_bf16 v[240:255], a[224:227], v[112:115], v[240:255]\n"
    "ds_read_b64_tr_b16 a[252:253], v19 offset:6144\n"
    "ds_read_b64_tr_b16 a[254:255], v19 offset:7168\n"
    "ds_read_b128 v[152:155], %[vb] offset:1856\n"
    "v_mul_f32 v22, v22, v178\n"
    "v_mul_f32 v21, v21, v177\n"
    "v_mfma_f32_32x32x16_bf16 a[144:159], a[228:231], v[112:115], a[144:159]\n"
    "ds_read_b64_tr_b16 a[224:225], v18 offset:8192\n"
    "ds_read_b64_tr_b16 a[226:227], v18 offset:9216\n"
    "v_cvt_pk_bf16_f32 v128, v20, v21\n"
    "v_exp_f32 v22, v22\n"
    "v_mfma_f32_32x32x16_bf16 v[240:255], a[232:235], v[116:119], v[240:255]\n"
    "ds_read_b64_tr_b16 a[228:229], v19 offset:8192\n"
    "ds_read_b64_tr_b16 a[230:231], v19 offset:9216\n"
    "s_add_u32 m0, %[ldsw], 57344\n"
    "s_mov_b32 s40, 442496\n"
    "v_fmaak_f32 v20, v179, v179, 0x409d30fc\n"
    "v_add_f32 v22, 1.0, v22\n"
    "v_mfma_f32_32x32x16_bf16 a[144:159], a[236:239], v[116:119], a[144:159]\n"
    "s_waitcnt lgkmcnt(5)\n"
    "ds_read_b64_tr_b16 a[232:233], v18 offset:10240\n"
    "ds_read_b64_tr_b16 a[234:235], v18 offset:11264\n"
    "buffer_load_dwordx4 %[w2a], %[srd2], s40 offen lds\n"
    "ds_read_b128 v[156:159], %[vb] offset:1888\n"
    "v_rcp_f32 v22, v22\n"
    "v_mul_f32 v20, v20, v179\n"
    "v_mfma_f32_32x32x16_bf16 v[240:255], a[240:243], v[120:123], v[240:255]\n"
    "ds_read_b64_tr_b16 a[236:237], v19 offset:10240\n"
    "ds_read_b64_tr_b16 a[238:239], v19 offset:11264\n"
    "v_mul_f32 v22, v22, v178\n"
    "v_exp_f32 v20, v20\n"
    "v_fmaak_f32 v21, v180, v180, 0x409d30fc\n"
    "v_mfma_f32_32x32x16_bf16 a[144:159], a[244:247], v[120:123], a[144:159]\n"
    "ds_read_b64_tr_b16 a[240:241], v18 offset:12288\n"
    "ds_read_b64_tr_b16 a[242:243], v18 offset:13312\n"
    "v_add_f32 v20, 1.0, v20\n"
    "v_rcp_f32 v20, v20\n"
    "v_mfma_f32_32x32x16_bf16 v[240:255], a[248:251], v[124:127], v[240:255]\n"
    "ds_read_b64_tr_b16 a[244:245], v19 offset:12288\n"
    "ds_read_b64_tr_b16 a[246:247], v19 offset:13312\n"
    "ds_read_b128 v[160:163], %[vb] offset:1920\n"
    "v_mul_f32 v21, v21, v180\n"
    "v_mul_f32 v20, v20, v179\n"
    "v_mfma_f32_32x32x16_bf16 a[144:159], a[252:255], v[124:127], a[144:159]\n"
    "s_waitcnt lgkmcnt(5)\n"
    "ds_read_b64_tr_b16 a[248:249], v18 offset:14336\n"
    "ds_read_b64_tr_b16 a[250:251], v18 offset:15360\n"
    "buffer_load_dwordx4 %[w2b], %[srd2], s40 offen offset:1024 lds\n"
    "v_cvt_pk_bf16_f32 v129, v22, v20\n"
    "v_exp_f32 v21, v21\n"
    "v_mfma_f32_32x32x16_bf16 a[160:175], a[224:227], v[112:115], a[160:175]\n"
    "ds_read_b64_tr_b16 a[252:253], v19 offset:14336\n"
    "ds_read_b64_tr_b16 a[254:255], v19 offset:15360\n"
    "v_fmaak_f32 v22, v181, v181, 0x409d30fc\n"
    "v_add_f32 v21, 1.0, v21\n"
    "v_mfma_f32_32x32x16_bf16 a[176:191], a[228:231], v[112:115], a[176:191]\n"
    "ds_read_b64_tr_b16 a[224:225], v18 offset:16384\n"
    "ds_read_b64_tr_b16 a[226:227], v18 offset:17408\n"
    "ds_read_b128 v[164:167], %[vb] offset:1952\n"
    "v_rcp_f32 v21, v21\n"
    "v_mul_f32 v22, v22, v181\n"
    "v_mul_f32 v21, v21, v180\n"
    "v_mfma_f32_32x32x16_bf16 a[160:175], a[232:235], v[116:119], a[160:175]\n"
    "ds_read_b64_tr_b16 a[228:229], v19 offset:16384\n"
    "ds_read_b64_tr_b16 a[230:231], v19 offset:17408\n"
    "s_add_u32 m0, %[ldsw], 65536\n"
    "s_mov_b32 s40, 442624\n"
    "v_exp_f32 v22, v22\n"
    "v_fmaak_f32 v20, v182, v182, 0x409d30fc\n"
    "v_mfma_f32_32x32x16_bf16 a[176:191], a[236:239], v[116:119], a[176:191]\n"
    "s_waitcnt lgkmcnt(5)\n"
    "ds_read_b64_tr_b16 a[232:233], v18 offset:18432\n"
    "ds_read_b64_tr_b16 a[234:235], v18 offset:19456\n"
    "buffer_load_dwordx4 %[w2a], %[srd2], s40 offen lds\n"
    "v_add_f32 v22, 1.0, v22\n"
    "v_rcp_f32 v22, v22\n"
    "v_mfma_f32_32x32x16_bf16 a[160:175], a[240:243], v[120:123], a[160:175]\n"
    "ds_read_b64_tr_b16 a[236:237], v19 offset:18432\n"
    "ds_read_b64_tr_b16 a[238:239], v19 offset:19456\n"
    "ds_read_b128 v[168:171], %[vb] offset:1984\n"
    "v_mul_f32 v20, v20, v182\n"
    "v_mul_f32 v22, v22, v181\n"
    "v_mfma_f32_32x32x16_bf16 a[176:191], a[244:247], v[120:123], a[176:191]\n"
    "ds_read_b64_tr_b16 a[240:241], v18 offset:20480\n"
    "ds_read_b64_tr_b16 a[242:243], v18 offset:21504\n"
    "v_cvt_pk_bf16_f32 v130, v21, v22\n"
    "v_exp_f32 v20, v20\n"
    "v_mfma_f32_32x32x16_bf16 a[160:175], a[248:251], v[124:127], a[160:175]\n"
    "ds_read_b64_tr_b16 a[244:245], v19 offset:20480\n"
    "ds_read_b64_tr_b16 a[246:247], v19 offset:21504\n"
    "v_fmaak_f32 v21, v183, v183, 0x409d30fc\n"
    "v_add_f32 v20, 1.0, v20\n"
    "v_rcp_f32 v20, v20\n"
    "v_mfma_f32_32x32x16_bf16 a[176:191], a[252:255], v[124:127], a[176:191]\n"
    "s_waitcnt lgkmcnt(5)\n"
    "ds_read_b64_tr_b16 a[248:249], v18 offset:22528\n"
    "ds_read_b64_tr_b16 a[250:251], v18 offset:23552\n"
    "buffer_load_dwordx4 %[w2b], %[srd2], s40 offen offset:1024 lds\n"
    "ds_read_b128 v[172:175], %[vb] offset:2016\n"
    "v_mul_f32 v21, v21, v183\n"
    "v_mul_f32 v20, v20, v182\n"
    "v_mfma_f32_32x32x16_bf16 a[192:207], a[224:227], v[112:115], a[192:207]\n"
    "ds_read_b64_tr_b16 a[252:253], v19 offset:22528\n"
    "ds_read_b64_tr_b16 a[254:255], v19 offset:23552\n"
    "v_exp_f32 v21, v21\n"
    "v_fmaak_f32 v22, v184, v184, 0x409d30fc\n"
    "v_mfma_f32_32x32x16_bf16 a[208:223], a[228:231], v[112:115], a[208:223]\n"
    "s_waitcnt vmcnt(18)\n"
    "s_barrier\n"
    "ds_read_b64_tr_b16 a[224:225], v18 offset:24576\n"
    "ds_read_b64_tr_b16 a[226:227], v18 offset:25600\n"
    "v_mfma_f32_32x32x16_bf16 a[192:207], a[232:235], v[116:119], a[192:207]\n"
    "ds_read_b64_tr_b16 a[228:229], v19 offset:24576\n"
    "ds_read_b64_tr_b16 a[230:231], v19 offset:25600\n"
    "s_add_u32 m0, %[ldsw], 73728\n"
    "s_mov_b32 s40, 442752\n"
    "v_add_f32 v21, 1.0, v21\n"
    "v_rcp_f32 v21, v21\n"
    "v_mfma_f32_32x32x16_bf16 a[208:223], a[236:239], v[116:119], a[208:223]\n"
    "s_waitcnt lgkmcnt(4)\n"
    "ds_read_b64_tr_b16 a[232:233], v18 offset:26624\n"
    "ds_read_b64_tr_b16 a[234:235], v18 offset:27648\n"
    "buffer_load_dwordx4 %[w2a], %[srd2], s40 offen lds\n"
    "v_mul_f32 v22, v22, v184\n"
    "v_mul_f32 v21, v21, v183\n"
    "v_cvt_pk_bf16_f32 v131, v20, v21\n"
    "v_mfma_f32_32x32x16_bf16 a[192:207], a[240:243], v[120:123], a[192:207]\n"
    "ds_read_b64_tr_b16 a[236:237], v19 offset:26624\n"
    "ds_read_b64_tr_b16 a[238:239], v19 offset:27648\n"
    "v_exp_f32 v22, v22\n"
    "v_fmaak_f32 v20, v185, v185, 0x409d30fc\n"
    "v_mfma_f32_32x32x16_bf16 a[208:223], a[244:247], v[120:123], a[208:223]\n"
    "ds_read_b64_tr_b16 a[240:241], v18 offset:28672\n"
    "ds_read_b64_tr_b16 a[242:243], v18 offset:29696\n"
    "v_add_f32 v22, 1.0, v22\n"
    "v_rcp_f32 v22, v22\n"
    "v_mfma_f32_32x32x16_bf16 a[192:207], a[248:251], v[124:127], a[192:207]\n"
    "ds_read_b64_tr_b16 a[244:245], v19 offset:28672\n"
    "ds_read_b64_tr_b16 a[246:247], v19 offset:29696\n"
    "v_mul_f32 v20, v20, v185\n"
    "v_mul_f32 v22, v22, v184\n"
    "v_mfma_f32_32x32x16_bf16 a[208:223], a[252:255], v[124:127], a[208:223]\n"
    "s_waitcnt lgkmcnt(4)\n"
    "ds_read_b64_tr_b16 a[248:249], v18 offset:30720\n"
    "ds_read_b64_tr_b16 a[250:251], v18 offset:31744\n"
    "buffer_load_dwordx4 %[w2b], %[srd2], s40 offen offset:1024 lds\n"
    "v_exp_f32 v20, v20\n"
    "v_fmaak_f32 v21, v186, v186, 0x409d30fc\n"
    "v_mfma_f32_32x32x16_bf16 v[144:159], a[224:227], a[0:3], v[144:159]\n"
    "ds_read_b64_tr_b16 a[252:253], v19 offset:30720\n"
    "ds_read_b64_tr_b16 a[254:255], v19 offset:31744\n"
    "v_add_f32 v20, 1.0, v20\n"
    "v_rcp_f32 v20, v20\n"
    "v_mul_f32 v21, v21, v186\n"
    "v_mfma_f32_32x32x16_bf16 v[160:175], a[228:231], a[0:3], v[160:175]\n"
    "ds_read_b64_tr_b16 a[224:225], v18 offset:32768\n"
    "ds_read_b64_tr_b16 a[226:227], v18 offset:33792\n"
    "v_mul_f32 v20, v20, v185\n"
    "v_cvt_pk_bf16_f32 v132, v22, v20\n"
    "v_mfma_f32_32x32x16_bf16 v[144:159], a[232:235], a[4:7], v[144:159]\n"
    "ds_read_b64_tr_b16 a[228:229], v19 offset:32768\n"
    "ds_read_b64_tr_b16 a[230:231], v19 offset:33792\n"
    "s_add_u32 m0, %[ldsw], 81920\n"
    "s_mov_b32 s40, 1024\n"
    "v_exp_f32 v21, v21\n"
    "v_fmaak_f32 v22, v187, v187, 0x409d30fc\n"
    "v_mfma_f32_32x32x16_bf16 v[160:175], a[236:239], a[4:7], v[160:175]\n"
    "s_waitcnt lgkmcnt(4)\n"
    "ds_read_b64_tr_b16 a[232:233], v18 offset:34816\n"
    "ds_read_b64_tr_b16 a[234:235], v18 offset:35840\n"
    "buffer_load_dwordx4 %[w1a], %[srd1], s40 offen lds\n"
    "v_add_f32 v21, 1.0, v21\n"
    "v_rcp_f32 v21, v21\n"
    "v_mfma_f32_32x32x16_bf16 v[144:159], a[240:243], a[8:11], v[144:159]\n"
    "ds_read_b64_tr_b16 a[236:237], v19 offset:34816\n"
    "ds_read_b64_tr_b16 a[238:239], v19 offset:35840\n"
    "v_mul_f32 v22, v22, v187\n"
    "v_mul_f32 v21, v21, v186\n"
    "v_mfma_f32_32x32x16_bf16 v[160:175], a[244:247], a[8:11], v[160:175]\n"
    "ds_read_b64_tr_b16 a[240:241], v18 offset:36864\n"
    "ds_read_b64_tr_b16 a[242:243], v18 offset:37888\n"
    "v_exp_f32 v22, v22\n"
    "v_fmaak_f32 v20, v188, v188, 0x409d30fc\n"
    "v_add_f32 v22, 1.0, v22\n"
    "v_mfma_f32_32x32x16_bf16 v[144:159], a[248:251], a[12:15], v[144:159]\n"
    "ds_read_b64_tr_b16 a[244:245], v19 offset:36864\n"
    "ds_read_b64_tr_b16 a[246:247], v19 offset:37888\n"
    "v_rcp_f32 v22, v22\n"
    "v_mul_f32 v20, v20, v188\n"
    "v_mfma_f32_32x32x16_bf16 v[160:175], a[252:255], a[12:15], v[160:175]\n"
    "s_waitcnt lgkmcnt(4)\n"
    "ds_read_b64_tr_b16 a[248:249], v18 offset:38912\n"
    "ds_read_b64_tr_b16 a[250:251], v18 offset:39936\n"
    "buffer_load_dwordx4 %[w1b], %[srd1], s40 offen offset:1024 lds\n"
    "v_mul_f32 v22, v22, v187\n"
    "v_cvt_pk_bf16_f32 v133, v21, v22\n"
    "v_mfma_f32_32x32x16_bf16 v[144:159], a[224:227], a[16:19], v[144:159]\n"
    "ds_read_b64_tr_b16 a[252:253], v19 offset:38912\n"
    "ds_read_b64_tr_b16 a[254:255], v19 offset:39936\n"
    "v_exp_f32 v20, v20\n"
    "v_fmaak_f32 v21, v189, v189, 0x409d30fc\n"
    "v_mfma_f32_32x32x16_bf16 v[160:175], a[228:231], a[16:19], v[160:175]\n"
    "ds_read_b64_tr_b16 a[224:225], v18 offset:40960\n"
    "ds_read_b64_tr_b16 a[226:227], v18 offset:41984\n"
    "v_add_f32 v20, 1.0, v20\n"
    "v_rcp_f32 v20, v20\n"
    "v_mfma_f32_32x32x16_bf16 v[144:159], a[232:235], a[20:23], v[144:159]\n"
    "ds_read_b64_tr_b16 a[228:229], v19 offset:40960\n"
    "ds_read_b64_tr_b16 a[230:231], v19 offset:41984\n"
    "s_add_u32 m0, %[ldsw], 90112\n"
    "s_mov_b32 s40, 590848\n"
    "v_mul_f32 v21, v21, v189\n"
    "v_mul_f32 v20, v20, v188\n"
    "v_exp_f32 v21, v21\n"
    "v_mfma_f32_32x32x16_bf16 v[160:175], a[236:239], a[20:23], v[160:175]\n"
    "s_waitcnt lgkmcnt(4)\n"
    "ds_read_b64_tr_b16 a[232:233], v18 offset:43008\n"
    "ds_read_b64_tr_b16 a[234:235], v18 offset:44032\n"
    "buffer_load_dwordx4 %[w1a], %[srd1], s40 offen lds\n"
    "v_fmaak_f32 v22, v190, v190, 0x409d30fc\n"
    "v_add_f32 v21, 1.0, v21\n"
    "v_mfma_f32_32x32x16_bf16 v[144:159], a[240:243], a[24:27], v[144:159]\n"
    "ds_read_b64_tr_b16 a[236:237], v19 offset:43008\n"
    "ds_read_b64_tr_b16 a[238:239], v19 offset:44032\n"
    "v_rcp_f32 v21, v21\n"
    "v_mul_f32 v22, v22, v190\n"
    "v_mfma_f32_32x32x16_bf16 v[160:175], a[244:247], a[24:27], v[160:175]\n"
    "ds_read_b64_tr_b16 a[240:241], v18 offset:45056\n"
    "ds_read_b64_tr_b16 a[242:243], v18 offset:46080\n"
    "v_mul_f32 v21, v21, v189\n"
    "v_cvt_pk_bf16_f32 v134, v20, v21\n"
    "v_mfma_f32_32x32x16_bf16 v[144:159], a[248:251], a[28:31], v[144:159]\n"
    "ds_read_b64_tr_b16 a[244:245], v19 offset:45056\n"
    "ds_read_b64_tr_b16 a[246:247], v19 offset:46080\n"
    "v_exp_f32 v22, v22\n"
    "v_fmaak_f32 v20, v191, v191, 0x409d30fc\n"
    "v_add_f32 v22, 1.0, v22\n"
    "v_mfma_f32_32x32x16_bf16 v[160:175], a[252:255], a[28:31], v[160:175]\n"
    "s_waitcnt lgkmcnt(4)\n"
    "ds_read_b64_tr_b16 a[248:249], v18 offset:47104\n"
    "ds_read_b64_tr_b16 a[250:251], v18 offset:48128\n"
    "buffer_load_dwordx4 %[w1b], %[srd1], s40 offen offset:1024 lds\n"
    "v_rcp_f32 v22, v22\n"
    "v_mul_f32 v20, v20, v191\n"
    "v_mfma_f32_32x32x16_bf16 v[144:159], a[224:227], a[32:35], v[144:159]\n"
    "ds_read_b64_tr_b16 a[252:253], v19 offset:47104\n"
    "ds_read_b64_tr_b16 a[254:255], v19 offset:48128\n"
    "v_mul_f32 v22, v22, v190\n"
    "v_exp_f32 v20, v20\n"
    "v_mfma_f32_32x32x16_bf16 v[160:175], a[228:231], a[32:35], v[160:175]\n"
    "s_waitcnt vmcnt(18)\n"
    "s_barrier\n"
    "ds_read_b64_tr_b16 a[224:225], %[rd0] offset:0\n"
    "ds_read_b64_tr_b16 a[226:227], %[rd0] offset:1024\n"
    "v_mfma_f32_32x32x16_bf16 v[144:159], a[232:235], a[36:39], v[144:159]\n"
    "ds_read_b64_tr_b16 a[228:229], %[rd1] offset:0\n"
    "ds_read_b64_tr_b16 a[230:231], %[rd1] offset:1024\n"
    "s_add_u32 m0, %[ldsw], 98304\n"
    "s_mov_b32 s40, 1180672\n"
    "v_fmaak_f32 v21, v192, v192, 0x409d30fc\n"
    "v_add_f32 v20, 1.0, v20\n"
    "v_mfma_f32_32x32x16_bf16 v[160:175], a[236:239], a[36:39], v[160:175]\n"
    "s_waitcnt lgkmcnt(4)\n"
    "ds_read_b64_tr_b16 a[232:233], %[rd0] offset:2048\n"
    "ds_read_b64_tr_b16 a[234:235], %[rd0] offset:3072\n"
    "buffer_load_dwordx4 %[w1a], %[srd1], s40 offen lds\n"
    "v_rcp_f32 v20, v20\n"
    "v_mul_f32 v21, v21, v192\n"
    "v_mfma_f32_32x32x16_bf16 v[144:159], a[240:243], a[40:43], v[144:159]\n"
    "ds_read_b64_tr_b16 a[236:237], %[rd1] offset:2048\n"
    "ds_read_b64_tr_b16 a[238:239], %[rd1] offset:3072\n"
    "v_mul_f32 v20, v20, v191\n"
    "v_cvt_pk_bf16_f32 v135, v22, v20\n"
    "v_exp_f32 v21, v21\n"
    "v_mfma_f32_32x32x16_bf16 v[160:175], a[244:247], a[40:43], v[160:175]\n"
    "ds_read_b64_tr_b16 a[240:241], %[rd0] offset:4096\n"
    "ds_read_b64_tr_b16 a[242:243], %[rd0] offset:5120\n"
    "v_fmaak_f32 v22, v193, v193, 0x409d30fc\n"
    "v_add_f32 v21, 1.0, v21\n"
    "v_mfma_f32_32x32x16_bf16 v[144:159], a[248:251], a[44:47], v[144:159]\n"
    "ds_read_b64_tr_b16 a[244:245], %[rd1] offset:4096\n"
    "ds_read_b64_tr_b16 a[246:247], %[rd1] offset:5120\n"
    "v_rcp_f32 v21, v21\n"
    "v_mul_f32 v22, v22, v193\n"
    "v_mfma_f32_32x32x16_bf16 v[160:175], a[252:255], a[44:47], v[160:175]\n"
    "s_waitcnt lgkmcnt(4)\n"
    "ds_read_b64_tr_b16 a[248:249], %[rd0] offset:6144\n"
    "ds_read_b64_tr_b16 a[250:251], %[rd0] offset:7168\n"
    "buffer_load_dwordx4 %[w1b], %[srd1], s40 offen offset:1024 lds\n"
    "v_mul_f32 v21, v21, v192\n"
    "v_exp_f32 v22, v22\n"
    "v_mfma_f32_32x32x16_bf16 v[144:159], a[224:227], a[48:51], v[144:159]\n"
    "ds_read_b64_tr_b16 a[252:253], %[rd1] offset:6144\n"
    "ds_read_b64_tr_b16 a[254:255], %[rd1] offset:7168\n"
    "v_fmaak_f32 v20, v194, v194, 0x409d30fc\n"
    "v_add_f32 v22, 1.0, v22\n"
    "v_mfma_f32_32x32x16_bf16 v[160:175], a[228:231], a[48:51], v[160:175]\n"
    "ds_read_b64_tr_b16 a[224:225], %[rd0] offset:8192\n"
    "ds_read_b64_tr_b16 a[226:227], %[rd0] offset:9216\n"
    "v_rcp_f32 v22, v22\n"
    "v_mul_f32 v20, v20, v194\n"
    "v_mul_f32 v22, v22, v193\n"
    "v_mfma_f32_32x32x16_bf16 v[144:159], a[232:235], a[52:55], v[144:159]\n"
    "ds_read_b64_tr_b16 a[228:229], %[rd1] offset:8192\n"
    "ds_read_b64_tr_b16 a[230:231], %[rd1] offset:9216\n"
    "s_add_u32 m0, %[ldsw], 106496\n"
    "s_mov_b32 s40, 1770496\n"
    "v_cvt_pk_bf16_f32 v136, v21, v22\n"
    "v_exp_f32 v20, v20\n"
    "v_mfma_f32_32x32x16_bf16 v[160:175], a[236:239], a[52:55], v[160:175]\n"
    "s_waitcnt lgkmcnt(4)\n"
    "ds_read_b64_tr_b16 a[232:233], %[rd0] offset:10240\n"
    "ds_read_b64_tr_b16 a[234:235], %[rd0] offset:11264\n"
    "buffer_load_dwordx4 %[w1a], %[srd1], s40 offen lds\n"
    "v_fmaak_f32 v21, v195, v195, 0x409d30fc\n"
    "v_add_f32 v20, 1.0, v20\n"
    "v_mfma_f32_32x32x16_bf16 v[144:159], a[240:243], a[56:59], v[144:159]\n"
    "ds_read_b64_tr_b16 a[236:237], %[rd1] offset:10240\n"
    "ds_read_b64_tr_b16 a[238:239], %[rd1] offset:11264\n"
    "v_rcp_f32 v20, v20\n"
    "v_mul_f32 v21, v21, v195\n"
    "v_mfma_f32_32x32x16_bf16 v[160:175], a[244:247], a[56:59], v[160:175]\n"
    "ds_read_b64_tr_b16 a[240:241], %[rd0] offset:12288\n"
    "ds_read_b64_tr_b16 a[242:243], %[rd0] offset:13312\n"
    "v_mul_f32 v20, v20, v194\n"
    "v_exp_f32 v21, v21\n"
    "v_mfma_f32_32x32x16_bf16 v[144:159], a[248:251], a[60:63], v[144:159]\n"
    "ds_read_b64_tr_b16 a[244:245], %[rd1] offset:12288\n"
    "ds_read_b64_tr_b16 a[246:247], %[rd1] offset:13312\n"
    "v_fmaak_f32 v22, v196, v196, 0x409d30fc\n"
    "v_add_f32 v21, 1.0, v21\n"
    "v_rcp_f32 v21, v21\n"
    "v_mfma_f32_32x32x16_bf16 v[160:175], a[252:255], a[60:63], v[160:175]\n"
    "s_waitcnt lgkmcnt(4)\n"
    "ds_read_b64_tr_b16 a[248:249], %[rd0] offset:14336\n"
    "ds_read_b64_tr_b16 a[250:251], %[rd0] offset:15360\n"
    "buffer_load_dwordx4 %[w1b], %[srd1], s40 offen offset:1024 lds\n"
    "v_mul_f32 v22, v22, v196\n"
    "v_mul_f32 v21, v21, v195\n"
    "v_mfma_f32_32x32x16_bf16 v[144:159], a[224:227], a[64:67], v[144:159]\n"
    "ds_read_b64_tr_b16 a[252:253], %[rd1] offset:14336\n"
    "ds_read_b64_tr_b16 a[254:255], %[rd1] offset:15360\n"
    "v_cvt_pk_bf16_f32 v137, v20, v21\n"
    "v_exp_f32 v22, v22\n"
    "v_mfma_f32_32x32x16_bf16 v[160:175], a[228:231], a[64:67], v[160:175]\n"
    "ds_read_b64_tr_b16 a[224:225], %[rd0] offset:16384\n"
    "ds_read_b64_tr_b16 a[226:227], %[rd0] offset:17408\n"
    "v_fmaak_f32 v20, v197, v197, 0x409d30fc\n"
    "v_add_f32 v22, 1.0, v22\n"
    "v_mfma_f32_32x32x16_bf16 v[144:159], a[232:235], a[68:71], v[144:159]\n"
    "ds_read_b64_tr_b16 a[228:229], %[rd1] offset:16384\n"
    "ds_read_b64_tr_b16 a[230:231], %[rd1] offset:17408\n"
    "s_add_u32 m0, %[ldsw], 114688\n"
    "s_mov_b32 s40, 2360320\n"
    "v_rcp_f32 v22, v22\n"
    "v_mul_f32 v20, v20, v197\n"
    "v_mul_f32 v22, v22, v196\n"
    "v_mfma_f32_32x32x16_bf16 v[160:175], a[236:239], a[68:71], v[160:175]\n"
    "s_waitcnt lgkmcnt(4)\n"
    "ds_read_b64_tr_b16 a[232:233], %[rd0] offset:18432\n"
    "ds_read_b64_tr_b16 a[234:235], %[rd0] offset:19456\n"
    "buffer_load_dwordx4 %[w1a], %[srd1], s40 offen lds\n"
    "v_exp_f32 v20, v20\n"
    "v_fmaak_f32 v21, v198, v198, 0x409d30fc\n"
    "v_mfma_f32_32x32x16_bf16 v[144:159], a[240:243], a[72:75], v[144:159]\n"
    "ds_read_b64_tr_b16 a[236:237], %[rd1] offset:18432\n"
    "ds_read_b64_tr_b16 a[238:239], %[rd1] offset:19456\n"
    "v_add_f32 v20, 1.0, v20\n"
    "v_rcp_f32 v20, v20\n"
    "v_mfma_f32_32x32x16_bf16 v[160:175], a[244:247], a[72:75], v[160:175]\n"
    "ds_read_b64_tr_b16 a[240:241], %[rd0] offset:20480\n"
    "ds_read_b64_tr_b16 a[242:243], %[rd0] offset:21504\n"
    "v_mul_f32 v21, v21, v198\n"
    "v_mul_f32 v20, v20, v197\n"
    "v_mfma_f32_32x32x16_bf16 v[144:159], a[248:251], a[76:79], v[144:159]\n"
    "ds_read_b64_tr_b16 a[244:245], %[rd1] offset:20480\n"
    "ds_read_b64_tr_b16 a[246:247], %[rd1] offset:21504\n"
    "v_cvt_pk_bf16_f32 v138, v22, v20\n"
    "v_exp_f32 v21, v21\n"
    "v_mfma_f32_32x32x16_bf16 v[160:175], a[252:255], a[76:79], v[160:175]\n"
    "s_waitcnt lgkmcnt(4)\n"
    "ds_read_b64_tr_b16 a[248:249], %[rd0] offset:22528\n"
    "ds_read_b64_tr_b16 a[250:251], %[rd0] offset:23552\n"
    "buffer_load_dwordx4 %[w1b], %[srd1], s40 offen offset:1024 lds\n"
    "v_fmaak_f32 v22, v199, v199, 0x409d30fc\n"
    "v_add_f32 v21, 1.0, v21\n"
    "v_rcp_f32 v21, v21\n"
    "v_mfma_f32_32x32x16_bf16 v[144:159], a[224:227], a[80:83], v[144:159]\n"
    "ds_read_b64_tr_b16 a[252:253], %[rd1] offset:22528\n"
    "ds_read_b64_tr_b16 a[254:255], %[rd1] offset:23552\n"
    "v_mul_f32 v22, v22, v199\n"
    "v_mul_f32 v21, v21, v198\n"
    "v_mfma_f32_32x32x16_bf16 v[160:175], a[228:231], a[80:83], v[160:175]\n"
    "s_waitcnt vmcnt(18)\n"
    "s_barrier\n"
    "ds_read_b64_tr_b16 a[224:225], %[rd0] offset:24576\n"
    "ds_read_b64_tr_b16 a[226:227], %[rd0] offset:25600\n"
    "v_mfma_f32_32x32x16_bf16 v[144:159], a[232:235], a[84:87], v[144:159]\n"
    "ds_read_b64_tr_b16 a[228:229], %[rd1] offset:24576\n"
    "ds_read_b64_tr_b16 a[230:231], %[rd1] offset:25600\n"
    "s_add_u32 m0, %[ldsw], 122880\n"
    "s_mov_b32 s40, 2950144\n"
    "v_exp_f32 v22, v22\n"
    "v_fmaak_f32 v20, v200, v200, 0x409d30fc\n"
    "v_mfma_f32_32x32x16_bf16 v[160:175], a[236:239], a[84:87], v[160:175]\n"
    "s_waitcnt lgkmcnt(4)\n"
    "ds_read_b64_tr_b16 a[232:233], %[rd0] offset:26624\n"
    "ds_read_b64_tr_b16 a[234:235], %[rd0] offset:27648\n"
    "buffer_load_dwordx4 %[w1a], %[srd1], s40 offen lds\n"
    "v_add_f32 v22, 1.0, v22\n"
    "v_rcp_f32 v22, v22\n"
    "v_mfma_f32_32x32x16_bf16 v[144:159], a[240:243], a[88:91], v[144:159]\n"
    "ds_read_b64_tr_b16 a[236:237], %[rd1] offset:26624\n"
    "ds_read_b64_tr_b16 a[238:239], %[rd1] offset:27648\n"
    "v_mul_f32 v20, v20, v200\n"
    "v_mul_f32 v22, v22, v199\n"
    "v_mfma_f32_32x32x16_bf16 v[160:175], a[244:247], a[88:91], v[160:175]\n"
    "ds_read_b64_tr_b16 a[240:241], %[rd0] offset:28672\n"
    "ds_read_b64_tr_b16 a[242:243], %[rd0] offset:29696\n"
    "v_cvt_pk_bf16_f32 v139, v21, v22\n"
    "v_exp_f32 v20, v20\n"
    "v_fmaak_f32 v21, v201, v201, 0x409d30fc\n"
    "v_mfma_f32_32x32x16_bf16 v[144:159], a[248:251], a[92:95], v[144:159]\n"
    "ds_read_b64_tr_b16 a[244:245], %[rd1] offset:28672\n"
    "ds_read_b64_tr_b16 a[246:247], %[rd1] offset:29696\n"
    "v_add_f32 v20, 1.0, v20\n"
    "v_rcp_f32 v20, v20\n"
    "v_mfma_f32_32x32x16_bf16 v[160:175], a[252:255], a[92:95], v[160:175]\n"
    "s_waitcnt lgkmcnt(4)\n"
    "ds_read_b64_tr_b16 a[248:249], %[rd0] offset:30720\n"
    "ds_read_b64_tr_b16 a[250:251], %[rd0] offset:31744\n"
    "buffer_load_dwordx4 %[w1b], %[srd1], s40 offen offset:1024 lds\n"
    "v_mul_f32 v21, v21, v201\n"
    "v_mul_f32 v20, v20, v200\n"
    "v_mfma_f32_32x32x16_bf16 v[144:159], a[224:227], a[96:99], v[144:159]\n"
    "ds_read_b64_tr_b16 a[252:253], %[rd1] offset:30720\n"
    "ds_read_b64_tr_b16 a[254:255], %[rd1] offset:31744\n"
    "v_exp_f32 v21, v21\n"
    "v_fmaak_f32 v22, v202, v202, 0x409d30fc\n"
    "v_mfma_f32_32x32x16_bf16 v[160:175], a[228:231], a[96:99], v[160:175]\n"
    "ds_read_b64_tr_b16 a[224:225], %[rd0] offset:32768\n"
    "ds_read_b64_tr_b16 a[226:227], %[rd0] offset:33792\n"
    "v_add_f32 v21, 1.0, v21\n"
    "v_rcp_f32 v21, v21\n"
    "v_mfma_f32_32x32x16_bf16 v[144:159], a[232:235], a[100:103], v[144:159]\n"
    "ds_read_b64_tr_b16 a[228:229], %[rd1] offset:32768\n"
    "ds_read_b64_tr_b16 a[230:231], %[rd1] offset:33792\n"
    "s_add_u32 m0, %[ldsw], 131072\n"
    "s_mov_b32 s40, 3539968\n"
    "v_mul_f32 v22, v22, v202\n"
    "v_mul_f32 v21, v21, v201\n"
    "v_cvt_pk_bf16_f32 v140, v20, v21\n"
    "v_mfma_f32_32x32x16_bf16 v[160:175], a[236:239], a[100:103], v[160:175]\n"
    "s_waitcnt lgkmcnt(4)\n"
    "ds_read_b64_tr_b16 a[232:233], %[rd0] offset:34816\n"
    "ds_read_b64_tr_b16 a[234:235], %[rd0] offset:35840\n"
    "buffer_load_dwordx4 %[w1a], %[srd1], s40 offen lds\n"
    "v_exp_f32 v22, v22\n"
    "v_fmaak_f32 v20, v203, v203, 0x409d30fc\n"
    "v_mfma_f32_32x32x16_bf16 v[144:159], a[240:243], a[104:107], v[144:159]\n"
    "ds_read_b64_tr_b16 a[236:237], %[rd1] offset:34816\n"
    "ds_read_b64_tr_b16 a[238:239], %[rd1] offset:35840\n"
    "v_add_f32 v22, 1.0, v22\n"
    "v_rcp_f32 v22, v22\n"
    "v_mfma_f32_32x32x16_bf16 v[160:175], a[244:247], a[104:107], v[160:175]\n"
    "ds_read_b64_tr_b16 a[240:241], %[rd0] offset:36864\n"
    "ds_read_b64_tr_b16 a[242:243], %[rd0] offset:37888\n"
    "v_mul_f32 v20, v20, v203\n"
    "v_mul_f32 v22, v22, v202\n"
    "v_mfma_f32_32x32x16_bf16 v[144:159], a[248:251], a[108:111], v[144:159]\n"
    "ds_read_b64_tr_b16 a[244:245], %[rd1] offset:36864\n"
    "ds_read_b64_tr_b16 a[246:247], %[rd1] offset:37888\n"
    "v_exp_f32 v20, v20\n"
    "v_fmaak_f32 v21, v204, v204, 0x409d30fc\n"
    "v_add_f32 v20, 1.0, v20\n"
    "v_mfma_f32_32x32x16_bf16 v[160:175], a[252:255], a[108:111], v[160:175]\n"
    "s_waitcnt lgkmcnt(4)\n"
    "ds_read_b64_tr_b16 a[248:249], %[rd0] offset:38912\n"
    "ds_read_b64_tr_b16 a[250:251], %[rd0] offset:39936\n"
    "buffer_load_dwordx4 %[w1b], %[srd1], s40 offen offset:1024 lds\n"
    "v_rcp_f32 v20, v20\n"
    "v_mul_f32 v21, v21, v204\n"
    "v_mfma_f32_32x32x16_bf16 v[144:159], a[224:227], a[112:115], v[144:159]\n"
    "ds_read_b64_tr_b16 a[252:253], %[rd1] offset:38912\n"
    "ds_read_b64_tr_b16 a[254:255], %[rd1] offset:39936\n"
    "v_mul_f32 v20, v20, v203\n"
    "v_cvt_pk_bf16_f32 v141, v22, v20\n"
    "v_mfma_f32_32x32x16_bf16 v[160:175], a[228:231], a[112:115], v[160:175]\n"
    "ds_read_b64_tr_b16 a[224:225], %[rd0] offset:40960\n"
    "ds_read_b64_tr_b16 a[226:227], %[rd0] offset:41984\n"
    "v_exp_f32 v21, v21\n"
    "v_fmaak_f32 v22, v205, v205, 0x409d30fc\n"
    "v_mfma_f32_32x32x16_bf16 v[144:159], a[232:235], a[116:119], v[144:159]\n"
    "ds_read_b64_tr_b16 a[228:229], %[rd1] offset:40960\n"
    "ds_read_b64_tr_b16 a[230:231], %[rd1] offset:41984\n"
    "s_add_u32 m0, %[ldsw], 139264\n"
    "s_mov_b32 s40, 4129792\n"
    "v_add_f32 v21, 1.0, v21\n"
    "v_rcp_f32 v21, v21\n"
    "v_mfma_f32_32x32x16_bf16 v[160:175], a[236:239], a[116:119], v[160:175]\n"
    "s_waitcnt lgkmcnt(4)\n"
    "ds_read_b64_tr_b16 a[232:233], %[rd0] offset:43008\n"
    "ds_read_b64_tr_b16 a[234:235], %[rd0] offset:44032\n"
    "buffer_load_dwordx4 %[w1a], %[srd1], s40 offen lds\n"
    "v_mul_f32 v22, v22, v205\n"
    "v_mul_f32 v21, v21, v204\n"
    "v_exp_f32 v22, v22\n"
    "v_mfma_f32_32x32x16_bf16 v[144:159], a[240:243], a[120:123], v[144:159]\n"
    "ds_read_b64_tr_b16 a[236:237], %[rd1] offset:43008\n"
    "ds_read_b64_tr_b16 a[238:239], %[rd1] offset:44032\n"
    "v_fmaak_f32 v20, v206, v206, 0x409d30fc\n"
    "v_add_f32 v22, 1.0, v22\n"
    "v_mfma_f32_32x32x16_bf16 v[160:175], a[244:247], a[120:123], v[160:175]\n"
    "ds_read_b64_tr_b16 a[240:241], %[rd0] offset:45056\n"
    "ds_read_b64_tr_b16 a[242:243], %[rd0] offset:46080\n"
    "v_rcp_f32 v22, v22\n"
    "v_mul_f32 v20, v20, v206\n"
    "v_mfma_f32_32x32x16_bf16 v[144:159], a[248:251], a[124:127], v[144:159]\n"
    "ds_read_b64_tr_b16 a[244:245], %[rd1] offset:45056\n"
    "ds_read_b64_tr_b16 a[246:247], %[rd1] offset:46080\n"
    "v_mul_f32 v22, v22, v205\n"
    "v_cvt_pk_bf16_f32 v142, v21, v22\n"
    "v_mfma_f32_32x32x16_bf16 v[160:175], a[252:255], a[124:127], v[160:175]\n"
    "s_waitcnt lgkmcnt(4)\n"
    "ds_read_b64_tr_b16 a[248:249], %[rd0] offset:47104\n"
    "ds_read_b64_tr_b16 a[250:251], %[rd0] offset:48128\n"
    "buffer_load_dwordx4 %[w1b], %[srd1], s40 offen offset:1024 lds\n"
    "v_exp_f32 v20, v20\n"
    "v_fmaak_f32 v21, v207, v207, 0x409d30fc\n"
    "v_mfma_f32_32x32x16_bf16 v[144:159], a[224:227], a[128:131], v[144:159]\n"
    "ds_read_b64_tr_b16 a[252:253], %[rd1] offset:47104\n"
    "ds_read_b64_tr_b16 a[254:255], %[rd1] offset:48128\n"
    "v_add_f32 v20, 1.0, v20\n"
    "v_rcp_f32 v20, v20\n"
    "v_mul_f32 v21, v21, v207\n"
    "v_mfma_f32_32x32x16_bf16 v[160:175], a[228:231], a[128:131], v[160:175]\n"
    "s_waitcnt vmcnt(18)\n"
    "s_barrier\n"
    "ds_read_b64_tr_b16 a[224:225], v16 offset:0\n"
    "ds_read_b64_tr_b16 a[226:227], v16 offset:1024\n"
    "v_mfma_f32_32x32x16_bf16 v[144:159], a[232:235], a[132:135], v[144:159]\n"
    "ds_read_b64_tr_b16 a[228:229], v17 offset:0\n"
    "ds_read_b64_tr_b16 a[230:231], v17 offset:1024\n"
    "s_add_u32 m0, %[ldsw], 0\n"
    "s_mov_b32 s40, 4719616\n"
    "v_mul_f32 v20, v20, v206\n"
    "v_exp_f32 v21, v21\n"
    "v_mfma_f32_32x32x16_bf16 v[160:175], a[236:239], a[132:135], v[160:175]\n"
    "s_waitcnt lgkmcnt(4)\n"
    "ds_read_b64_tr_b16 a[232:233], v16 offset:2048\n"
    "ds_read_b64_tr_b16 a[234:235], v16 offset:3072\n"
    "buffer_load_dwordx4 %[w1a], %[srd1], s40 offen lds\n"
    "s_nop 0\n"
    "v_add_f32 v21, 1.0, v21\n"
    "v_mfma_f32_32x32x16_bf16 v[144:159], a[240:243], a[136:139], v[144:159]\n"
    "ds_read_b64_tr_b16 a[236:237], v17 offset:2048\n"
    "ds_read_b64_tr_b16 a[238:239], v17 offset:3072\n"
    "v_rcp_f32 v21, v21\n"
    "s_nop 0\n"
    "v_mfma_f32_32x32x16_bf16 v[160:175], a[244:247], a[136:139], v[160:175]\n"
    "ds_read_b64_tr_b16 a[240:241], v16 offset:4096\n"
    "ds_read_b64_tr_b16 a[242:243], v16 offset:5120\n"
    "v_mul_f32 v21, v21, v207\n"
    "v_cvt_pk_bf16_f32 v143, v20, v21\n"
    "v_mfma_f32_32x32x16_bf16 v[144:159], a[248:251], a[140:143], v[144:159]\n"
    "ds_read_b64_tr_b16 a[244:245], v17 offset:4096\n"
    "ds_read_b64_tr_b16 a[246:247], v17 offset:5120\n"
    "v_mfma_f32_32x32x16_bf16 v[160:175], a[252:255], a[140:143], v[160:175]\n"
    "s_waitcnt lgkmcnt(4)\n"
    "ds_read_b64_tr_b16 a[248:249], v16 offset:6144\n"
    "ds_read_b64_tr_b16 a[250:251], v16 offset:7168\n"
    "buffer_load_dwordx4 %[w1b], %[srd1], s40 offen offset:1024 lds\n"
    "v_mfma_f32_32x32x16_bf16 v[208:223], a[224:227], v[128:131], v[208:223]\n"
    "ds_read_b64_tr_b16 a[252:253], v17 offset:6144\n"
    "ds_read_b64_tr_b16 a[254:255], v17 offset:7168\n"
    "v_mfma_f32_32x32x16_bf16 v[224:239], a[228:231], v[128:131], v[224:239]\n"
    "ds_read_b64_tr_b16 a[224:225], v16 offset:8192\n"
    "ds_read_b64_tr_b16 a[226:227], v16 offset:9216\n"
    "v_mfma_f32_32x32x16_bf16 v[208:223], a[232:235], v[132:135], v[208:223]\n"
    "ds_read_b64_tr_b16 a[228:229], v17 offset:8192\n"
    "ds_read_b64_tr_b16 a[230:231], v17 offset:9216\n"
    "s_add_u32 m0, %[ldsw], 8192\n"
    "s_mov_b32 s40, 516096\n"
    "ds_read_b128 v[176:179], %[vb] offset:2048\n"
    "v_mfma_f32_32x32x16_bf16 v[224:239], a[236:239], v[132:135], v[224:239]\n"
    "s_waitcnt lgkmcnt(5)\n"
    "ds_read_b64_tr_b16 a[232:233], v16 offset:10240\n"
    "ds_read_b64_tr_b16 a[234:235], v16 offset:11264\n"
    "buffer_load_dwordx4 %[w2a], %[srd2], s40 offen lds\n"
    "v_fmaak_f32 v20, v144, v144, 0x409d30fc\n"
    "v_mul_f32 v20, v20, v144\n"
    "v_exp_f32 v20, v20\n"
    "v_mfma_f32_32x32x16_bf16 v[208:223], a[240:243], v[136:139], v[208:223]\n"
    "ds_read_b64_tr_b16 a[236:237], v17 offset:10240\n"
    "ds_read_b64_tr_b16 a[238:239], v17 offset:11264\n"
    "v_fmaak_f32 v21, v145, v145, 0x409d30fc\n"
    "v_add_f32 v20, 1.0, v20\n"
    "v_mfma_f32_32x32x16_bf16 v[224:239], a[244:247], v[136:139], v[224:239]\n"
    "ds_read_b64_tr_b16 a[240:241], v16 offset:12288\n"
    "ds_read_b64_tr_b16 a[242:243], v16 offset:13312\n"
    "ds_read_b128 v[180:183], %[vb] offset:2080\n"
    "v_rcp_f32 v20, v20\n"
    "v_mul_f32 v21, v21, v145\n"
    "v_mfma_f32_32x32x16_bf16 v[208:223], a[248:251], v[140:143], v[208:223]\n"
    "ds_read_b64_tr_b16 a[244:245], v17 offset:12288\n"
    "ds_read_b64_tr_b16 a[246:247], v17 offset:13312\n"
    "v_mul_f32 v20, v20, v144\n"
    "v_exp_f32 v21, v21\n"
    "v_mfma_f32_32x32x16_bf16 v[224:239], a[252:255], v[140:143], v[224:239]\n"
    "s_waitcnt lgkmcnt(5)\n"
    "ds_read_b64_tr_b16 a[248:249], v16 offset:14336\n"
    "ds_read_b64_tr_b16 a[250:251], v16 offset:15360\n"
    "buffer_load_dwordx4 %[w2b], %[srd2], s40 offen offset:1024 lds\n"
    "v_fmaak_f32 v22, v146, v146, 0x409d30fc\n"
    "v_add_f32 v21, 1.0, v21\n"
    "v_rcp_f32 v21, v21\n"
    "v_mfma_f32_32x32x16_bf16 v[240:255], a[224:227], v[128:131], v[240:255]\n"
    "ds_read_b64_tr_b16 a[252:253], v17 offset:14336\n"
    "ds_read_b64_tr_b16 a[254:255], v17 offset:15360\n"
    "ds_read_b128 v[184:187], %[vb] offset:2112\n"
    "v_mul_f32 v22, v22, v146\n"
    "v_mul_f32 v21, v21, v145\n"
    "v_mfma_f32_32x32x16_bf16 a[144:159], a[228:231], v[128:131], a[144:159]\n"
    "ds_read_b64_tr_b16 a[224:225], v16 offset:16384\n"
    "ds_read_b64_tr_b16 a[226:227], v16 offset:17408\n"
    "v_cvt_pk_bf16_f32 v144, v20, v21\n"
    "v_exp_f32 v22, v22\n"
    "v_mfma_f32_32x32x16_bf16 v[240:255], a[232:235], v[132:135], v[240:255]\n"
    "ds_read_b64_tr_b16 a[228:229], v17 offset:16384\n"
    "ds_read_b64_tr_b16 a[230:231], v17 offset:17408\n"
    "s_add_u32 m0, %[ldsw], 16384\n"
    "s_mov_b32 s40, 516224\n"
    "v_fmaak_f32 v20, v147, v147, 0x409d30fc\n"
    "v_add_f32 v22, 1.0, v22\n"
    "v_mfma_f32_32x32x16_bf16 a[144:159], a[236:239], v[132:135], a[144:159]\n"
    "s_waitcnt lgkmcnt(5)\n"
    "ds_read_b64_tr_b16 a[232:233], v16 offset:18432\n"
    "ds_read_b64_tr_b16 a[234:235], v16 offset:19456\n"
    "buffer_load_dwordx4 %[w2a], %[srd2], s40 offen lds\n"
    "ds_read_b128 v[188:191], %[vb] offset:2144\n"
    "v_rcp_f32 v22, v22\n"
    "v_mul_f32 v20, v20, v147\n"
    "v_mfma_f32_32x32x16_bf16 v[240:255], a[240:243], v[136:139], v[240:255]\n"
    "ds_read_b64_tr_b16 a[236:237], v17 offset:18432\n"
    "ds_read_b64_tr_b16 a[238:239], v17 offset:19456\n"
    "v_mul_f32 v22, v22, v146\n"
    "v_exp_f32 v20, v20\n"
    "v_fmaak_f32 v21, v148, v148, 0x409d30fc\n"
    "v_mfma_f32_32x32x16_bf16 a[144:159], a[244:247], v[136:139], a[144:159]\n"
    "ds_read_b64_tr_b16 a[240:241], v16 offset:20480\n"
    "ds_read_b64_tr_b16 a[242:243], v16 offset:21504\n"
    "v_add_f32 v20, 1.0, v20\n"
    "v_rcp_f32 v20, v20\n"
    "v_mfma_f32_32x32x16_bf16 v[240:255], a[248:251], v[140:143], v[240:255]\n"
    "ds_read_b64_tr_b16 a[244:245], v17 offset:20480\n"
    "ds_read_b64_tr_b16 a[246:247], v17 offset:21504\n"
    "ds_read_b128 v[192:195], %[vb] offset:2176\n"
    "v_mul_f32 v21, v21, v148\n"
    "v_mul_f32 v20, v20, v147\n"
    "v_mfma_f32_32x32x16_bf16 a[144:159], a[252:255], v[140:143], a[144:159]\n"
    "s_waitcnt lgkmcnt(5)\n"
    "ds_read_b64_tr_b16 a[248:249], v16 offset:22528\n"
    "ds_read_b64_tr_b16 a[250:251], v16 offset:23552\n"
    "buffer_load_dwordx4 %[w2b], %[srd2], s40 offen offset:1024 lds\n"
    "v_cvt_pk_bf16_f32 v145, v22, v20\n"
    "v_exp_f32 v21, v21\n"
    "v_mfma_f32_32x32x16_bf16 a[160:175], a[224:227], v[128:131], a[160:175]\n"
    "ds_read_b64_tr_b16 a[252:253], v17 offset:22528\n"
    "ds_read_b64_tr_b16 a[254:255], v17 offset:23552\n"
    "v_fmaak_f32 v22, v149, v149, 0x409d30fc\n"
    "v_add_f32 v21, 1.0, v21\n"
    "v_mfma_f32_32x32x16_bf16 a[176:191], a[228:231], v[128:131], a[176:191]\n"
    "s_waitcnt vmcnt(18)\n"
    "s_barrier\n"
    "ds_read_b64_tr_b16 a[224:225], v16 offset:24576\n"
    "ds_read_b64_tr_b16 a[226:227], v16 offset:25600\n"
    "v_mfma_f32_32x32x16_bf16 a[160:175], a[232:235], v[132:135], a[160:175]\n"
    "ds_read_b64_tr_b16 a[228:229], v17 offset:24576\n"
    "ds_read_b64_tr_b16 a[230:231], v17 offset:25600\n"
    "s_add_u32 m0, %[ldsw], 24576\n"
    "s_mov_b32 s40, 516352\n"
    "ds_read_b128 v[196:199], %[vb] offset:2208\n"
    "v_rcp_f32 v21, v21\n"
    "v_mul_f32 v22, v22, v149\n"
    "v_mul_f32 v21, v21, v148\n"
    "v_mfma_f32_32x32x16_bf16 a[176:191], a[236:239], v[132:135], a[176:191]\n"
    "s_waitcnt lgkmcnt(5)\n"
    "ds_read_b64_tr_b16 a[232:233], v16 offset:26624\n"
    "ds_read_b64_tr_b16 a[234:235], v16 offset:27648\n"
    "buffer_load_dwordx4 %[w2a], %[srd2], s40 offen lds\n"
    "v_exp_f32 v22, v22\n"
    "v_fmaak_f32 v20, v150, v150, 0x409d30fc\n"
    "v_mfma_f32_32x32x16_bf16 a[160:175], a[240:243], v[136:139], a[160:175]\n"
    "ds_read_b64_tr_b16 a[236:237], v17 offset:26624\n"
    "ds_read_b64_tr_b16 a[238:239], v17 offset:27648\n"
    "v_add_f32 v22, 1.0, v22\n"
    "v_rcp_f32 v22, v22\n"
    "v_mfma_f32_32x32x16_bf16 a[176:191], a[244:247], v[136:139], a[176:191]\n"
    "ds_read_b64_tr_b16 a[240:241], v16 offset:28672\n"
    "ds_read_b64_tr_b16 a[242:243], v16 offset:29696\n"
    "ds_read_b128 v[200:203], %[vb] offset:2240\n"
    "v_mul_f32 v20, v20, v150\n"
    "v_mul_f32 v22, v22, v149\n"
    "v_mfma_f32_32x32x16_bf16 a[160:175], a[248:251], v[140:143], a[160:175]\n"
    "ds_read_b64_tr_b16 a[244:245], v17 offset:28672\n"
    "ds_read_b64_tr_b16 a[246:247], v17 offset:29696\n"
    "v_cvt_pk_bf16_f32 v146, v21, v22\n"
    "v_exp_f32 v20, v20\n"
    "v_mfma_f32_32x32x16_bf16 a[176:191], a[252:255], v[140:143], a[176:191]\n"
    "s_waitcnt lgkmcnt(5)\n"
    "ds_read_b64_tr_b16 a[248:249], v16 offset:30720\n"
    "ds_read_b64_tr_b16 a[250:251], v16 offset:31744\n"
    "buffer_load_dwordx4 %[w2b], %[srd2], s40 offen offset:1024 lds\n"
    "v_fmaak_f32 v21, v151, v151, 0x409d30fc\n"
    "v_add_f32 v20, 1.0, v20\n"
    "v_rcp_f32 v20, v20\n"
    "v_mfma_f32_32x32x16_bf16 a[192:207], a[224:227], v[128:131], a[192:207]\n"
    "ds_read_b64_tr_b16 a[252:253], v17 offset:30720\n"
    "ds_read_b64_tr_b16 a[254:255], v17 offset:31744\n"
    "ds_read_b128 v[204:207], %[vb] offset:2272\n"
    "v_mul_f32 v21, v21, v151\n"
    "v_mul_f32 v20, v20, v150\n"
    "v_mfma_f32_32x32x16_bf16 a[208:223], a[228:231], v[128:131], a[208:223]\n"
    "ds_read_b64_tr_b16 a[224:225], v16 offset:32768\n"
    "ds_read_b64_tr_b16 a[226:227], v16 offset:33792\n"
    "v_exp_f32 v21, v21\n"
    "v_fmaak_f32 v22, v152, v152, 0x409d30fc\n"
    "v_mfma_f32_32x32x16_bf16 a[192:207], a[232:235], v[132:135], a[192:207]\n"
    "ds_read_b64_tr_b16 a[228:229], v17 offset:32768\n"
    "ds_read_b64_tr_b16 a[230:231], v17 offset:33792\n"
    "s_add_u32 m0, %[ldsw], 32768\n"
    "s_mov_b32 s40, 516480\n"
    "v_add_f32 v21, 1.0, v21\n"
    "v_rcp_f32 v21, v21\n"
    "v_mfma_f32_32x32x16_bf16 a[208:223], a[236:239], v[132:135], a[208:223]\n"
    "s_waitcnt lgkmcnt(5)\n"
    "ds_read_b64_tr_b16 a[232:233], v16 offset:34816\n"
    "ds_read_b64_tr_b16 a[234:235], v16 offset:35840\n"
    "buffer_load_dwordx4 %[w2a], %[srd2], s40 offen lds\n"
    "v_mul_f32 v22, v22, v152\n"
    "v_mul_f32 v21, v21, v151\n"
    "v_cvt_pk_bf16_f32 v147, v20, v21\n"
    "v_mfma_f32_32x32x16_bf16 a[192:207], a[240:243], v[136:139], a[192:207]\n"
    "ds_read_b64_tr_b16 a[236:237], v17 offset:34816\n"
    "ds_read_b64_tr_b16 a[238:239], v17 offset:35840\n"
    "v_exp_f32 v22, v22\n"
    "v_fmaak_f32 v20, v153, v153, 0x409d30fc\n"
    "v_mfma_f32_32x32x16_bf16 a[208:223], a[244:247], v[136:139], a[208:223]\n"
    "ds_read_b64_tr_b16 a[240:241], v16 offset:36864\n"
    "ds_read_b64_tr_b16 a[242:243], v16 offset:37888\n"
    "v_add_f32 v22, 1.0, v22\n"
    "v_rcp_f32 v22, v22\n"
    "v_mfma_f32_32x32x16_bf16 a[192:207], a[248:251], v[140:143], a[192:207]\n"
    "ds_read_b64_tr_b16 a[244:245], v17 offset:36864\n"
    "ds_read_b64_tr_b16 a[246:247], v17 offset:37888\n"
    "v_mul_f32 v20, v20, v153\n"
    "v_mul_f32 v22, v22, v152\n"
    "v_mfma_f32_32x32x16_bf16 a[208:223], a[252:255], v[140:143], a[208:223]\n"
    "s_waitcnt lgkmcnt(4)\n"
    "ds_read_b64_tr_b16 a[248:249], v16 offset:38912\n"
    "ds_read_b64_tr_b16 a[250:251], v16 offset:39936\n"
    "buffer_load_dwordx4 %[w2b], %[srd2], s40 offen offset:1024 lds\n"
    "v_exp_f32 v20, v20\n"
    "v_fmaak_f32 v21, v154, v154, 0x409d30fc\n"
    "v_mfma_f32_32x32x16_bf16 v[176:191], a[224:227], a[0:3], v[176:191]\n"
    "ds_read_b64_tr_b16 a[252:253], v17 offset:38912\n"
    "ds_read_b64_tr_b16 a[254:255], v17 offset:39936\n"
    "v_add_f32 v20, 1.0, v20\n"
    "v_rcp_f32 v20, v20\n"
    "v_mul_f32 v21, v21, v154\n"
    "v_mfma_f32_32x32x16_bf16 v[192:207], a[228:231], a[0:3], v[192:207]\n"
    "ds_read_b64_tr_b16 a[224:225], v16 offset:40960\n"
    "ds_read_b64_tr_b16 a[226:227], v16 offset:41984\n"
    "v_mul_f32 v20, v20, v153\n"
    "v_cvt_pk_bf16_f32 v148, v22, v20\n"
    "v_mfma_f32_32x32x16_bf16 v[176:191], a[232:235], a[4:7], v[176:191]\n"
    "ds_read_b64_tr_b16 a[228:229], v17 offset:40960\n"
    "ds_read_b64_tr_b16 a[230:231], v17 offset:41984\n"
    "s_add_u32 m0, %[ldsw], 40960\n"
    "s_mov_b32 s40, 512\n"
    "v_exp_f32 v21, v21\n"
    "v_fmaak_f32 v22, v155, v155, 0x409d30fc\n"
    "v_mfma_f32_32x32x16_bf16 v[192:207], a[236:239], a[4:7], v[192:207]\n"
    "s_waitcnt lgkmcnt(4)\n"
    "ds_read_b64_tr_b16 a[232:233], v16 offset:43008\n"
    "ds_read_b64_tr_b16 a[234:235], v16 offset:44032\n"
    "buffer_load_dwordx4 %[w2a], %[srd2], s40 offen lds\n"
    "v_add_f32 v21, 1.0, v21\n"
    "v_rcp_f32 v21, v21\n"
    "v_mfma_f32_32x32x16_bf16 v[176:191], a[240:243], a[8:11], v[176:191]\n"
    "ds_read_b64_tr_b16 a[236:237], v17 offset:43008\n"
    "ds_read_b64_tr_b16 a[238:239], v17 offset:44032\n"
    "v_mul_f32 v22, v22, v155\n"
    "v_mul_f32 v21, v21, v154\n"
    "v_mfma_f32_32x32x16_bf16 v[192:207], a[244:247], a[8:11], v[192:207]\n"
    "ds_read_b64_tr_b16 a[240:241], v16 offset:45056\n"
    "ds_read_b64_tr_b16 a[242:243], v16 offset:46080\n"
    "v_exp_f32 v22, v22\n"
    "v_fmaak_f32 v20, v156, v156, 0x409d30fc\n"
    "v_add_f32 v22, 1.0, v22\n"
    "v_mfma_f32_32x32x16_bf16 v[176:191], a[248:251], a[12:15], v[176:191]\n"
    "ds_read_b64_tr_b16 a[244:245], v17 offset:45056\n"
    "ds_read_b64_tr_b16 a[246:247], v17 offset:46080\n"
    "v_rcp_f32 v22, v22\n"
    "v_mul_f32 v20, v20, v156\n"
    "v_mfma_f32_32x32x16_bf16 v[192:207], a[252:255], a[12:15], v[192:207]\n"
    "s_waitcnt lgkmcnt(4)\n"
    "ds_read_b64_tr_b16 a[248:249], v16 offset:47104\n"
    "ds_read_b64_tr_b16 a[250:251], v16 offset:48128\n"
    "buffer_load_dwordx4 %[w2b], %[srd2], s40 offen offset:1024 lds\n"
    "v_mul_f32 v22, v22, v155\n"
    "v_cvt_pk_bf16_f32 v149, v21, v22\n"
    "v_mfma_f32_32x32x16_bf16 v[176:191], a[224:227], a[16:19], v[176:191]\n"
    "ds_read_b64_tr_b16 a[252:253], v17 offset:47104\n"
    "ds_read_b64_tr_b16 a[254:255], v17 offset:48128\n"
    "v_exp_f32 v20, v20\n"
    "v_fmaak_f32 v21, v157, v157, 0x409d30fc\n"
    "v_mfma_f32_32x32x16_bf16 v[192:207], a[228:231], a[16:19], v[192:207]\n"
    "s_waitcnt vmcnt(18)\n"
    "s_barrier\n"
    "ds_read_b64_tr_b16 a[224:225], v18 offset:0\n"
    "ds_read_b64_tr_b16 a[226:227], v18 offset:1024\n"
    "v_mfma_f32_32x32x16_bf16 v[176:191], a[232:235], a[20:23], v[176:191]\n"
    "ds_read_b64_tr_b16 a[228:229], v19 offset:0\n"
    "ds_read_b64_tr_b16 a[230:231], v19 offset:1024\n"
    "s_add_u32 m0, %[ldsw], 49152\n"
    "s_mov_b32 s40, 74240\n"
    "v_add_f32 v20, 1.0, v20\n"
    "v_rcp_f32 v20, v20\n"
    "v_mfma_f32_32x32x16_bf16 v[192:207], a[236:239], a[20:23], v[192:207]\n"
    "s_waitcnt lgkmcnt(4)\n"
    "ds_read_b64_tr_b16 a[232:233], v18 offset:2048\n"
    "ds_read_b64_tr_b16 a[234:235], v18 offset:3072\n"
    "buffer_load_dwordx4 %[w2a], %[srd2], s40 offen lds\n"
    "v_mul_f32 v21, v21, v157\n"
    "v_mul_f32 v20, v20, v156\n"
    "v_exp_f32 v21, v21\n"
    "v_mfma_f32_32x32x16_bf16 v[176:191], a[240:243], a[24:27], v[176:191]\n"
    "ds_read_b64_tr_b16 a[236:237], v19 offset:2048\n"
    "ds_read_b64_tr_b16 a[238:239], v19 offset:3072\n"
    "v_fmaak_f32 v22, v158, v158, 0x409d30fc\n"
    "v_add_f32 v21, 1.0, v21\n"
    "v_mfma_f32_32x32x16_bf16 v[192:207], a[244:247], a[24:27], v[192:207]\n"
    "ds_read_b64_tr_b16 a[240:241], v18 offset:4096\n"
    "ds_read_b64_tr_b16 a[242:243], v18 offset:5120\n"
    "v_rcp_f32 v21, v21\n"
    "v_mul_f32 v22, v22, v158\n"
    "v_mfma_f32_32x32x16_bf16 v[176:191], a[248:251], a[28:31], v[176:191]\n"
    "ds_read_b64_tr_b16 a[244:245], v19 offset:4096\n"
    "ds_read_b64_tr_b16 a[246:247], v19 offset:5120\n"
    "v_mul_f32 v21, v21, v157\n"
    "v_cvt_pk_bf16_f32 v150, v20, v21\n"
    "v_mfma_f32_32x32x16_bf16 v[192:207], a[252:255], a[28:31], v[192:207]\n"
    "s_waitcnt lgkmcnt(4)\n"
    "ds_read_b64_tr_b16 a[248:249], v18 offset:6144\n"
    "ds_read_b64_tr_b16 a[250:251], v18 offset:7168\n"
    "buffer_load_dwordx4 %[w2b], %[srd2], s40 offen offset:1024 lds\n"
    "v_exp_f32 v22, v22\n"
    "v_fmaak_f32 v20, v159, v159, 0x409d30fc\n"
    "v_add_f32 v22, 1.0, v22\n"
    "v_mfma_f32_32x32x16_bf16 v[176:191], a[224:227], a[32:35], v[176:191]\n"
    "ds_read_b64_tr_b16 a[252:253], v19 offset:6144\n"
    "ds_read_b64_tr_b16 a[254:255], v19 offset:7168\n"
    "v_rcp_f32 v22, v22\n"
    "v_mul_f32 v20, v20, v159\n"
    "v_mfma_f32_32x32x16_bf16 v[192:207], a[228:231], a[32:35], v[192:207]\n"
    "ds_read_b64_tr_b16 a[224:225], v18 offset:8192\n"
    "ds_read_b64_tr_b16 a[226:227], v18 offset:9216\n"
    "v_mul_f32 v22, v22, v158\n"
    "v_exp_f32 v20, v20\n"
    "v_mfma_f32_32x32x16_bf16 v[176:191], a[232:235], a[36:39], v[176:191]\n"
    "ds_read_b64_tr_b16 a[228:229], v19 offset:8192\n"
    "ds_read_b64_tr_b16 a[230:231], v19 offset:9216\n"
    "s_add_u32 m0, %[ldsw], 57344\n"
    "s_mov_b32 s40, 147968\n"
    "v_fmaak_f32 v21, v160, v160, 0x409d30fc\n"
    "v_add_f32 v20, 1.0, v20\n"
    "v_mfma_f32_32x32x16_bf16 v[192:207], a[236:239], a[36:39], v[192:207]\n"
    "s_waitcnt lgkmcnt(4)\n"
    "ds_read_b64_tr_b16 a[232:233], v18 offset:10240\n"
    "ds_read_b64_tr_b16 a[234:235], v18 offset:11264\n"
    "buffer_load_dwordx4 %[w2a], %[srd2], s40 offen lds\n"
    "v_rcp_f32 v20, v20\n"
    "v_mul_f32 v21, v21, v160\n"
    "v_mfma_f32_32x32x16_bf16 v[176:191], a[240:243], a[40:43], v[176:191]\n"
    "ds_read_b64_tr_b16 a[236:237], v19 offset:10240\n"
    "ds_read_b64_tr_b16 a[238:239], v19 offset:11264\n"
    "v_mul_f32 v20, v20, v159\n"
    "v_cvt_pk_bf16_f32 v151, v22, v20\n"
    "v_exp_f32 v21, v21\n"
    "v_mfma_f32_32x32x16_bf16 v[192:207], a[244:247], a[40:43], v[192:207]\n"
    "ds_read_b64_tr_b16 a[240:241], v18 offset:12288\n"
    "ds_read_b64_tr_b16 a[242:243], v18 offset:13312\n"
    "v_fmaak_f32 v22, v161, v161, 0x409d30fc\n"
    "v_add_f32 v21, 1.0, v21\n"
    "v_mfma_f32_32x32x16_bf16 v[176:191], a[248:251], a[44:47], v[176:191]\n"
    "ds_read_b64_tr_b16 a[244:245], v19 offset:12288\n"
    "ds_read_b64_tr_b16 a[246:247], v19 offset:13312\n"
    "v_rcp_f32 v21, v21\n"
    "v_mul_f32 v22, v22, v161\n"
    "v_mfma_f32_32x32x16_bf16 v[192:207], a[252:255], a[44:47], v[192:207]\n"
    "s_waitcnt lgkmcnt(4)\n"
    "ds_read_b64_tr_b16 a[248:249], v18 offset:14336\n"
    "ds_read_b64_tr_b16 a[250:251], v18 offset:15360\n"
    "buffer_load_dwordx4 %[w2b], %[srd2], s40 offen offset:1024 lds\n"
    "v_mul_f32 v21, v21, v160\n"
    "v_exp_f32 v22, v22\n"
    "v_mfma_f32_32x32x16_bf16 v[176:191], a[224:227], a[48:51], v[176:191]\n"
    "ds_read_b64_tr_b16 a[252:253], v19 offset:14336\n"
    "ds_read_b64_tr_b16 a[254:255], v19 offset:15360\n"
    "v_fmaak_f32 v20, v162, v162, 0x409d30fc\n"
    "v_add_f32 v22, 1.0, v22\n"
    "v_mfma_f32_32x32x16_bf16 v[192:207], a[228:231], a[48:51], v[192:207]\n"
    "ds_read_b64_tr_b16 a[224:225], v18 offset:16384\n"
    "ds_read_b64_tr_b16 a[226:227], v18 offset:17408\n"
    "v_rcp_f32 v22, v22\n"
    "v_mul_f32 v20, v20, v162\n"
    "v_mul_f32 v22, v22, v161\n"
    "v_mfma_f32_32x32x16_bf16 v[176:191], a[232:235], a[52:55], v[176:191]\n"
    "ds_read_b64_tr_b16 a[228:229], v19 offset:16384\n"
    "ds_read_b64_tr_b16 a[230:231], v19 offset:17408\n"
    "s_add_u32 m0, %[ldsw], 65536\n"
    "s_mov_b32 s40, 221696\n"
    "v_cvt_pk_bf16_f32 v152, v21, v22\n"
    "v_exp_f32 v20, v20\n"
    "v_mfma_f32_32x32x16_bf16 v[192:207], a[236:239], a[52:55], v[192:207]\n"
    "s_waitcnt lgkmcnt(4)\n"
    "ds_read_b64_tr_b16 a[232:233], v18 offset:18432\n"
    "ds_read_b64_tr_b16 a[234:235], v18 offset:19456\n"
    "buffer_load_dwordx4 %[w2a], %[srd2], s40 offen lds\n"
    "v_fmaak_f32 v21, v163, v163, 0x409d30fc\n"
    "v_add_f32 v20, 1.0, v20\n"
    "v_mfma_f32_32x32x16_bf16 v[176:191], a[240:243], a[56:59], v[176:191]\n"
    "ds_read_b64_tr_b16 a[236:237], v19 offset:18432\n"
    "ds_read_b64_tr_b16 a[238:239], v19 offset:19456\n"
    "v_rcp_f32 v20, v20\n"
    "v_mul_f32 v21, v21, v163\n"
    "v_mfma_f32_32x32x16_bf16 v[192:207], a[244:247], a[56:59], v[192:207]\n"
    "ds_read_b64_tr_b16 a[240:241], v18 offset:20480\n"
    "ds_read_b64_tr_b16 a[242:243], v18 offset:21504\n"
    "v_mul_f32 v20, v20, v162\n"
    "v_exp_f32 v21, v21\n"
    "v_mfma_f32_32x32x16_bf16 v[176:191], a[248:251], a[60:63], v[176:191]\n"
    "ds_read_b64_tr_b16 a[244:245], v19 offset:20480\n"
    "ds_read_b64_tr_b16 a[246:247], v19 offset:21504\n"
    "v_fmaak_f32 v22, v164, v164, 0x409d30fc\n"
    "v_add_f32 v21, 1.0, v21\n"
    "v_rcp_f32 v21, v21\n"
    "v_mfma_f32_32x32x16_bf16 v[192:207], a[252:255], a[60:63], v[192:207]\n"
    "s_waitcnt lgkmcnt(4)\n"
    "ds_read_b64_tr_b16 a[248:249], v18 offset:22528\n"
    "ds_read_b64_tr_b16 a[250:251], v18 offset:23552\n"
    "buffer_load_dwordx4 %[w2b], %[srd2], s40 offen offset:1024 lds\n"
    "v_mul_f32 v22, v22, v164\n"
    "v_mul_f32 v21, v21, v163\n"
    "v_mfma_f32_32x32x16_bf16 v[176:191], a[224:227], a[64:67], v[176:191]\n"
    "ds_read_b64_tr_b16 a[252:253], v19 offset:22528\n"
    "ds_read_b64_tr_b16 a[254:255], v19 offset:23552\n"
    "v_cvt_pk_bf16_f32 v153, v20, v21\n"
    "v_exp_f32 v22, v22\n"
    "v_mfma_f32_32x32x16_bf16 v[192:207], a[228:231], a[64:67], v[192:207]\n"
    "s_waitcnt vmcnt(18)\n"
    "s_barrier\n"
    "ds_read_b64_tr_b16 a[224:225], v18 offset:24576\n"
    "ds_read_b64_tr_b16 a[226:227], v18 offset:25600\n"
    "v_mfma_f32_32x32x16_bf16 v[176:191], a[232:235], a[68:71], v[176:191]\n"
    "ds_read_b64_tr_b16 a[228:229], v19 offset:24576\n"
    "ds_read_b64_tr_b16 a[230:231], v19 offset:25600\n"
    "s_add_u32 m0, %[ldsw], 73728\n"
    "s_mov_b32 s40, 295424\n"
    "v_fmaak_f32 v20, v165, v165, 0x409d30fc\n"
    "v_add_f32 v22, 1.0, v22\n"
    "v_mfma_f32_32x32x16_bf16 v[192:207], a[236:239], a[68:71], v[192:207]\n"
    "s_waitcnt lgkmcnt(4)\n"
    "ds_read_b64_tr_b16 a[232:233], v18 offset:26624\n"
    "ds_read_b64_tr_b16 a[234:235], v18 offset:27648\n"
    "buffer_load_dwordx4 %[w2a], %[srd2], s40 offen lds\n"
    "v_rcp_f32 v22, v22\n"
    "v_mul_f32 v20, v20, v165\n"
    "v_mul_f32 v22, v22, v164\n"
    "v_mfma_f32_32x32x16_bf16 v[176:191], a[240:243], a[72:75], v[176:191]\n"
    "ds_read_b64_tr_b16 a[236:237], v19 offset:26624\n"
    "ds_read_b64_tr_b16 a[238:239], v19 offset:27648\n"
    "v_exp_f32 v20, v20\n"
    "v_fmaak_f32 v21, v166, v166, 0x409d30fc\n"
    "v_mfma_f32_32x32x16_bf16 v[192:207], a[244:247], a[72:75], v[192:207]\n"
    "ds_read_b64_tr_b16 a[240:241], v18 offset:28672\n"
    "ds_read_b64_tr_b16 a[242:243], v18 offset:29696\n"
    "v_add_f32 v20, 1.0, v20\n"
    "v_rcp_f32 v20, v20\n"
    "v_mfma_f32_32x32x16_bf16 v[176:191], a[248:251], a[76:79], v[176:191]\n"
    "ds_read_b64_tr_b16 a[244:245], v19 offset:28672\n"
    "ds_read_b64_tr_b16 a[246:247], v19 offset:29696\n"
    "v_mul_f32 v21, v21, v166\n"
    "v_mul_f32 v20, v20, v165\n"
    "v_mfma_f32_32x32x16_bf16 v[192:207], a[252:255], a[76:79], v[192:207]\n"
    "s_waitcnt lgkmcnt(4)\n"
    "ds_read_b64_tr_b16 a[248:249], v18 offset:30720\n"
    "ds_read_b64_tr_b16 a[250:251], v18 offset:31744\n"
    "buffer_load_dwordx4 %[w2b], %[srd2], s40 offen offset:1024 lds\n"
    "v_cvt_pk_bf16_f32 v154, v22, v20\n"
    "v_exp_f32 v21, v21\n"
    "v_mfma_f32_32x32x16_bf16 v[176:191], a[224:227], a[80:83], v[176:191]\n"
    "ds_read_b64_tr_b16 a[252:253], v19 offset:30720\n"
    "ds_read_b64_tr_b16 a[254:255], v19 offset:31744\n"
    "v_fmaak_f32 v22, v167, v167, 0x409d30fc\n"
    "v_add_f32 v21, 1.0, v21\n"
    "v_rcp_f32 v21, v21\n"
    "v_mfma_f32_32x32x16_bf16 v[192:207], a[228:231], a[80:83], v[192:207]\n"
    "ds_read_b64_tr_b16 a[224:225], v18 offset:32768\n"
    "ds_read_b64_tr_b16 a[226:227], v18 offset:33792\n"
    "v_mul_f32 v22, v22, v167\n"
    "v_mul_f32 v21, v21, v166\n"
    "v_mfma_f32_32x32x16_bf16 v[176:191], a[232:235], a[84:87], v[176:191]\n"
    "ds_read_b64_tr_b16 a[228:229], v19 offset:32768\n"
    "ds_read_b64_tr_b16 a[230:231], v19 offset:33792\n"
    "s_add_u32 m0, %[ldsw], 81920\n"
    "s_mov_b32 s40, 369152\n"
    "v_exp_f32 v22, v22\n"
    "v_fmaak_f32 v20, v168, v168, 0x409d30fc\n"
    "v_mfma_f32_32x32x16_bf16 v[192:207], a[236:239], a[84:87], v[192:207]\n"
    "s_waitcnt lgkmcnt(4)\n"
    "ds_read_b64_tr_b16 a[232:233], v18 offset:34816\n"
    "ds_read_b64_tr_b16 a[234:235], v18 offset:35840\n"
    "buffer_load_dwordx4 %[w2a], %[srd2], s40 offen lds\n"
    "v_add_f32 v22, 1.0, v22\n"
    "v_rcp_f32 v22, v22\n"
    "v_mfma_f32_32x32x16_bf16 v[176:191], a[240:243], a[88:91], v[176:191]\n"
    "ds_read_b64_tr_b16 a[236:237], v19 offset:34816\n"
    "ds_read_b64_tr_b16 a[238:239], v19 offset:35840\n"
    "v_mul_f32 v20, v20, v168\n"
    "v_mul_f32 v22, v22, v167\n"
    "v_mfma_f32_32x32x16_bf16 v[192:207], a[244:247], a[88:91], v[192:207]\n"
    "ds_read_b64_tr_b16 a[240:241], v18 offset:36864\n"
    "ds_read_b64_tr_b16 a[242:243], v18 offset:37888\n"
    "v_cvt_pk_bf16_f32 v155, v21, v22\n"
    "v_exp_f32 v20, v20\n"
    "v_fmaak_f32 v21, v169, v169, 0x409d30fc\n"
    "v_mfma_f32_32x32x16_bf16 v[176:191], a[248:251], a[92:95], v[176:191]\n"
    "ds_read_b64_tr_b16 a[244:245], v19 offset:36864\n"
    "ds_read_b64_tr_b16 a[246:247], v19 offset:37888\n"
    "v_add_f32 v20, 1.0, v20\n"
    "v_rcp_f32 v20, v20\n"
    "v_mfma_f32_32x32x16_bf16 v[192:207], a[252:255], a[92:95], v[192:207]\n"
    "s_waitcnt lgkmcnt(4)\n"
    "ds_read_b64_tr_b16 a[248:249], v18 offset:38912\n"
    "ds_read_b64_tr_b16 a[250:251], v18 offset:39936\n"
    "buffer_load_dwordx4 %[w2b], %[srd2], s40 offen offset:1024 lds\n"
    "v_mul_f32 v21, v21, v169\n"
    "v_mul_f32 v20, v20, v168\n"
    "v_mfma_f32_32x32x16_bf16 v[176:191], a[224:227], a[96:99], v[176:191]\n"
    "ds_read_b64_tr_b16 a[252:253], v19 offset:38912\n"
    "ds_read_b64_tr_b16 a[254:255], v19 offset:39936\n"
    "v_exp_f32 v21, v21\n"
    "v_fmaak_f32 v22, v170, v170, 0x409d30fc\n"
    "v_mfma_f32_32x32x16_bf16 v[192:207], a[228:231], a[96:99], v[192:207]\n"
    "ds_read_b64_tr_b16 a[224:225], v18 offset:40960\n"
    "ds_read_b64_tr_b16 a[226:227], v18 offset:41984\n"
    "v_add_f32 v21, 1.0, v21\n"
    "v_rcp_f32 v21, v21\n"
    "v_mfma_f32_32x32x16_bf16 v[176:191], a[232:235], a[100:103], v[176:191]\n"
    "ds_read_b64_tr_b16 a[228:229], v19 offset:40960\n"
    "ds_read_b64_tr_b16 a[230:231], v19 offset:41984\n"
    "s_add_u32 m0, %[ldsw], 90112\n"
    "s_mov_b32 s40, 442880\n"
    "v_mul_f32 v22, v22, v170\n"
    "v_mul_f32 v21, v21, v169\n"
    "v_cvt_pk_bf16_f32 v156, v20, v21\n"
    "v_mfma_f32_32x32x16_bf16 v[192:207], a[236:239], a[100:103], v[192:207]\n"
    "s_waitcnt lgkmcnt(4)\n"
    "ds_read_b64_tr_b16 a[232:233], v18 offset:43008\n"
    "ds_read_b64_tr_b16 a[234:235], v18 offset:44032\n"
    "buffer_load_dwordx4 %[w2a], %[srd2], s40 offen lds\n"
    "v_exp_f32 v22, v22\n"
    "v_fmaak_f32 v20, v171, v171, 0x409d30fc\n"
    "v_mfma_f32_32x32x16_bf16 v[176:191], a[240:243], a[104:107], v[176:191]\n"
    "ds_read_b64_tr_b16 a[236:237], v19 offset:43008\n"
    "ds_read_b64_tr_b16 a[238:239], v19 offset:44032\n"
    "v_add_f32 v22, 1.0, v22\n"
    "v_rcp_f32 v22, v22\n"
    "v_mfma_f32_32x32x16_bf16 v[192:207], a[244:247], a[104:107], v[192:207]\n"
    "ds_read_b64_tr_b16 a[240:241], v18 offset:45056\n"
    "ds_read_b64_tr_b16 a[242:243], v18 offset:46080\n"
    "v_mul_f32 v20, v20, v171\n"
    "v_mul_f32 v22, v22, v170\n"
    "v_mfma_f32_32x32x16_bf16 v[176:191], a[248:251], a[108:111], v[176:191]\n"
    "ds_read_b64_tr_b16 a[244:245], v19 offset:45056\n"
    "ds_read_b64_tr_b16 a[246:247], v19 offset:46080\n"
    "v_exp_f32 v20, v20\n"
    "v_fmaak_f32 v21, v172, v172, 0x409d30fc\n"
    "v_add_f32 v20, 1.0, v20\n"
    "v_mfma_f32_32x32x16_bf16 v[192:207], a[252:255], a[108:111], v[192:207]\n"
    "s_waitcnt lgkmcnt(4)\n"
    "ds_read_b64_tr_b16 a[248:249], v18 offset:47104\n"
    "ds_read_b64_tr_b16 a[250:251], v18 offset:48128\n"
    "buffer_load_dwordx4 %[w2b], %[srd2], s40 offen offset:1024 lds\n"
    "v_rcp_f32 v20, v20\n"
    "v_mul_f32 v21, v21, v172\n"
    "v_mfma_f32_32x32x16_bf16 v[176:191], a[224:227], a[112:115], v[176:191]\n"
    "ds_read_b64_tr_b16 a[252:253], v19 offset:47104\n"
    "ds_read_b64_tr_b16 a[254:255], v19 offset:48128\n"
    "v_mul_f32 v20, v20, v171\n"
    "v_cvt_pk_bf16_f32 v157, v22, v20\n"
    "v_mfma_f32_32x32x16_bf16 v[192:207], a[228:231], a[112:115], v[192:207]\n"
    "s_waitcnt vmcnt(18)\n"
    "s_barrier\n"
    "ds_read_b64_tr_b16 a[224:225], %[rd0] offset:0\n"
    "ds_read_b64_tr_b16 a[226:227], %[rd0] offset:1024\n"
    "v_mfma_f32_32x32x16_bf16 v[176:191], a[232:235], a[116:119], v[176:191]\n"
    "ds_read_b64_tr_b16 a[228:229], %[rd1] offset:0\n"
    "ds_read_b64_tr_b16 a[230:231], %[rd1] offset:1024\n"
    "s_add_u32 m0, %[ldsw], 98304\n"
    "s_mov_b32 s40, 516608\n"
    "v_exp_f32 v21, v21\n"
    "v_fmaak_f32 v22, v173, v173, 0x409d30fc\n"
    "v_mfma_f32_32x32x16_bf16 v[192:207], a[236:239], a[116:119], v[192:207]\n"
    "s_waitcnt lgkmcnt(4)\n"
    "ds_read_b64_tr_b16 a[232:233], %[rd0] offset:2048\n"
    "ds_read_b64_tr_b16 a[234:235], %[rd0] offset:3072\n"
    "buffer_load_dwordx4 %[w2a], %[srd2], s40 offen lds\n"
    "v_add_f32 v21, 1.0, v21\n"
    "v_rcp_f32 v21, v21\n"
    "v_mfma_f32_32x32x16_bf16 v[176:191], a[240:243], a[120:123], v[176:191]\n"
    "ds_read_b64_tr_b16 a[236:237], %[rd1] offset:2048\n"
    "ds_read_b64_tr_b16 a[238:239], %[rd1] offset:3072\n"
    "v_mul_f32 v22, v22, v173\n"
    "v_mul_f32 v21, v21, v172\n"
    "v_exp_f32 v22, v22\n"
    "v_mfma_f32_32x32x16_bf16 v[192:207], a[244:247], a[120:123], v[192:207]\n"
    "ds_read_b64_tr_b16 a[240:241], %[rd0] offset:4096\n"
    "ds_read_b64_tr_b16 a[242:243], %[rd0] offset:5120\n"
    "v_fmaak_f32 v20, v174, v174, 0x409d30fc\n"
    "v_add_f32 v22, 1.0, v22\n"
    "v_mfma_f32_32x32x16_bf16 v[176:191], a[248:251], a[124:127], v[176:191]\n"
    "ds_read_b64_tr_b16 a[244:245], %[rd1] offset:4096\n"
    "ds_read_b64_tr_b16 a[246:247], %[rd1] offset:5120\n"
    "v_rcp_f32 v22, v22\n"
    "v_mul_f32 v20, v20, v174\n"
    "v_mfma_f32_32x32x16_bf16 v[192:207], a[252:255], a[124:127], v[192:207]\n"
    "s_waitcnt lgkmcnt(4)\n"
    "ds_read_b64_tr_b16 a[248:249], %[rd0] offset:6144\n"
    "ds_read_b64_tr_b16 a[250:251], %[rd0] offset:7168\n"
    "buffer_load_dwordx4 %[w2b], %[srd2], s40 offen offset:1024 lds\n"
    "v_mul_f32 v22, v22, v173\n"
    "v_cvt_pk_bf16_f32 v158, v21, v22\n"
    "v_mfma_f32_32x32x16_bf16 v[176:191], a[224:227], a[128:131], v[176:191]\n"
    "ds_read_b64_tr_b16 a[252:253], %[rd1] offset:6144\n"
    "ds_read_b64_tr_b16 a[254:255], %[rd1] offset:7168\n"
    "v_exp_f32 v20, v20\n"
    "v_fmaak_f32 v21, v175, v175, 0x409d30fc\n"
    "v_mfma_f32_32x32x16_bf16 v[192:207], a[228:231], a[128:131], v[192:207]\n"
    "ds_read_b64_tr_b16 a[224:225], %[rd0] offset:8192\n"
    "ds_read_b64_tr_b16 a[226:227], %[rd0] offset:9216\n"
    "v_add_f32 v20, 1.0, v20\n"
    "v_rcp_f32 v20, v20\n"
    "v_mul_f32 v21, v21, v175\n"
    "v_mfma_f32_32x32x16_bf16 v[176:191], a[232:235], a[132:135], v[176:191]\n"
    "ds_read_b64_tr_b16 a[228:229], %[rd1] offset:8192\n"
    "ds_read_b64_tr_b16 a[230:231], %[rd1] offset:9216\n"
    "s_add_u32 m0, %[ldsw], 106496\n"
    "s_mov_b32 s40, 590336\n"
    "v_mul_f32 v20, v20, v174\n"
    "v_exp_f32 v21, v21\n"
    "v_mfma_f32_32x32x16_bf16 v[192:207], a[236:239], a[132:135], v[192:207]\n"
    "s_waitcnt lgkmcnt(4)\n"
    "ds_read_b64_tr_b16 a[232:233], %[rd0] offset:10240\n"
    "ds_read_b64_tr_b16 a[234:235], %[rd0] offset:11264\n"
    "buffer_load_dwordx4 %[w2a], %[srd2], s40 offen lds\n"
    "s_nop 0\n"
    "v_add_f32 v21, 1.0, v21\n"
    "v_mfma_f32_32x32x16_bf16 v[176:191], a[240:243], a[136:139], v[176:191]\n"
    "ds_read_b64_tr_b16 a[236:237], %[rd1] offset:10240\n"
    "ds_read_b64_tr_b16 a[238:239], %[rd1] offset:11264\n"
    "v_rcp_f32 v21, v21\n"
    "s_nop 0\n"
    "v_mfma_f32_32x32x16_bf16 v[192:207], a[244:247], a[136:139], v[192:207]\n"
    "ds_read_b64_tr_b16 a[240:241], %[rd0] offset:12288\n"
    "ds_read_b64_tr_b16 a[242:243], %[rd0] offset:13312\n"
    "v_mul_f32 v21, v21, v175\n"
    "v_cvt_pk_bf16_f32 v159, v20, v21\n"
    "v_mfma_f32_32x32x16_bf16 v[176:191], a[248:251], a[140:143], v[176:191]\n"
    "ds_read_b64_tr_b16 a[244:245], %[rd1] offset:12288\n"
    "ds_read_b64_tr_b16 a[246:247], %[rd1] offset:13312\n"
    "v_mfma_f32_32x32x16_bf16 v[192:207], a[252:255], a[140:143], v[192:207]\n"
    "s_waitcnt lgkmcnt(4)\n"
    "ds_read_b64_tr_b16 a[248:249], %[rd0] offset:14336\n"
    "ds_read_b64_tr_b16 a[250:251], %[rd0] offset:15360\n"
    "buffer_load_dwordx4 %[w2b], %[srd2], s40 offen offset:1024 lds\n"
    "v_mfma_f32_32x32x16_bf16 v[208:223], a[224:227], v[144:147], v[208:223]\n"
    "ds_read_b64_tr_b16 a[252:253], %[rd1] offset:14336\n"
    "ds_read_b64_tr_b16 a[254:255], %[rd1] offset:15360\n"
    "v_mfma_f32_32x32x16_bf16 v[224:239], a[228:231], v[144:147], v[224:239]\n"
    "ds_read_b64_tr_b16 a[224:225], %[rd0] offset:16384\n"
    "ds_read_b64_tr_b16 a[226:227], %[rd0] offset:17408\n"
    "v_mfma_f32_32x32x16_bf16 v[208:223], a[232:235], v[148:151], v[208:223]\n"
    "ds_read_b64_tr_b16 a[228:229], %[rd1] offset:16384\n"
    "ds_read_b64_tr_b16 a[230:231], %[rd1] offset:17408\n"
    "s_add_u32 m0, %[ldsw], 114688\n"
    "s_mov_b32 s40, 589824\n"
    "v_mfma_f32_32x32x16_bf16 v[224:239], a[236:239], v[148:151], v[224:239]\n"
    "s_waitcnt lgkmcnt(4)\n"
    "ds_read_b64_tr_b16 a[232:233], %[rd0] offset:18432\n"
    "ds_read_b64_tr_b16 a[234:235], %[rd0] offset:19456\n"
    "buffer_load_dwordx4 %[w2a], %[srd2], s40 offen lds\n"
    "v_fmaak_f32 v20, v176, v176, 0x409d30fc\n"
    "v_mul_f32 v20, v20, v176\n"
    "v_exp_f32 v20, v20\n"
    "v_mfma_f32_32x32x16_bf16 v[208:223], a[240:243], v[152:155], v[208:223]\n"
    "ds_read_b64_tr_b16 a[236:237], %[rd1] offset:18432\n"
    "ds_read_b64_tr_b16 a[238:239], %[rd1] offset:19456\n"
    "v_fmaak_f32 v21, v177, v177, 0x409d30fc\n"
    "v_add_f32 v20, 1.0, v20\n"
    "v_mfma_f32_32x32x16_bf16 v[224:239], a[244:247], v[152:155], v[224:239]\n"
    "ds_read_b64_tr_b16 a[240:241], %[rd0] offset:20480\n"
    "ds_read_b64_tr_b16 a[242:243], %[rd0] offset:21504\n"
    "v_rcp_f32 v20, v20\n"
    "v_mul_f32 v21, v21, v177\n"
    "v_mul_f32 v20, v20, v176\n"
    "v_mfma_f32_32x32x16_bf16 v[208:223], a[248:251], v[156:159], v[208:223]\n"
    "ds_read_b64_tr_b16 a[244:245], %[rd1] offset:20480\n"
    "ds_read_b64_tr_b16 a[246:247], %[rd1] offset:21504\n"
    "v_exp_f32 v21, v21\n"
    "v_fmaak_f32 v22, v178, v178, 0x409d30fc\n"
    "v_mfma_f32_32x32x16_bf16 v[224:239], a[252:255], v[156:159], v[224:239]\n"
    "s_waitcnt lgkmcnt(4)\n"
    "ds_read_b64_tr_b16 a[248:249], %[rd0] offset:22528\n"
    "ds_read_b64_tr_b16 a[250:251], %[rd0] offset:23552\n"
    "buffer_load_dwordx4 %[w2b], %[srd2], s40 offen offset:1024 lds\n"
    "v_add_f32 v21, 1.0, v21\n"
    "v_rcp_f32 v21, v21\n"
    "v_mul_f32 v22, v22, v178\n"
    "v_mfma_f32_32x32x16_bf16 v[240:255], a[224:227], v[144:147], v[240:255]\n"
    "ds_read_b64_tr_b16 a[252:253], %[rd1] offset:22528\n"
    "ds_read_b64_tr_b16 a[254:255], %[rd1] offset:23552\n"
    "v_mul_f32 v21, v21, v177\n"
    "v_cvt_pk_bf16_f32 v160, v20, v21\n"
    "v_mfma_f32_32x32x16_bf16 a[144:159], a[228:231], v[144:147], a[144:159]\n"
    "s_waitcnt vmcnt(18)\n"
    "s_barrier\n"
    "ds_read_b64_tr_b16 a[224:225], %[rd0] offset:24576\n"
    "ds_read_b64_tr_b16 a[226:227], %[rd0] offset:25600\n"
    "v_mfma_f32_32x32x16_bf16 v[240:255], a[232:235], v[148:151], v[240:255]\n"
    "ds_read_b64_tr_b16 a[228:229], %[rd1] offset:24576\n"
    "ds_read_b64_tr_b16 a[230:231], %[rd1] offset:25600\n"
    "s_add_u32 m0, %[ldsw], 122880\n"
    "s_mov_b32 s40, 589952\n"
    "v_exp_f32 v22, v22\n"
    "v_fmaak_f32 v20, v179, v179, 0x409d30fc\n"
    "v_mfma_f32_32x32x16_bf16 a[144:159], a[236:239], v[148:151], a[144:159]\n"
    "s_waitcnt lgkmcnt(4)\n"
    "ds_read_b64_tr_b16 a[232:233], %[rd0] offset:26624\n"
    "ds_read_b64_tr_b16 a[234:235], %[rd0] offset:27648\n"
    "buffer_load_dwordx4 %[w2a], %[srd2], s40 offen lds\n"
    "v_add_f32 v22, 1.0, v22\n"
    "v_rcp_f32 v22, v22\n"
    "v_mul_f32 v20, v20, v179\n"
    "v_mfma_f32_32x32x16_bf16 v[240:255], a[240:243], v[152:155], v[240:255]\n"
    "ds_read_b64_tr_b16 a[236:237], %[rd1] offset:26624\n"
    "ds_read_b64_tr_b16 a[238:239], %[rd1] offset:27648\n"
    "v_mul_f32 v22, v22, v178\n"
    "v_exp_f32 v20, v20\n"
    "v_mfma_f32_32x32x16_bf16 a[144:159], a[244:247], v[152:155], a[144:159]\n"
    "ds_read_b64_tr_b16 a[240:241], %[rd0] offset:28672\n"
    "ds_read_b64_tr_b16 a[242:243], %[rd0] offset:29696\n"
    "v_fmaak_f32 v21, v180, v180, 0x409d30fc\n"
    "v_add_f32 v20, 1.0, v20\n"
    "v_rcp_f32 v20, v20\n"
    "v_mfma_f32_32x32x16_bf16 v[240:255], a[248:251], v[156:159], v[240:255]\n"
    "ds_read_b64_tr_b16 a[244:245], %[rd1] offset:28672\n"
    "ds_read_b64_tr_b16 a[246:247], %[rd1] offset:29696\n"
    "v_mul_f32 v21, v21, v180\n"
    "v_mul_f32 v20, v20, v179\n"
    "v_mfma_f32_32x32x16_bf16 a[144:159], a[252:255], v[156:159], a[144:159]\n"
    "s_waitcnt lgkmcnt(4)\n"
    "ds_read_b64_tr_b16 a[248:249], %[rd0] offset:30720\n"
    "ds_read_b64_tr_b16 a[250:251], %[rd0] offset:31744\n"
    "buffer_load_dwordx4 %[w2b], %[srd2], s40 offen offset:1024 lds\n"
    "v_cvt_pk_bf16_f32 v161, v22, v20\n"
    "v_exp_f32 v21, v21\n"
    "v_mfma_f32_32x32x16_bf16 a[160:175], a[224:227], v[144:147], a[160:175]\n"
    "ds_read_b64_tr_b16 a[252:253], %[rd1] offset:30720\n"
    "ds_read_b64_tr_b16 a[254:255], %[rd1] offset:31744\n"
    "v_fmaak_f32 v22, v181, v181, 0x409d30fc\n"
    "v_add_f32 v21, 1.0, v21\n"
    "v_rcp_f32 v21, v21\n"
    "v_mfma_f32_32x32x16_bf16 a[176:191], a[228:231], v[144:147], a[176:191]\n"
    "ds_read_b64_tr_b16 a[224:225], %[rd0] offset:32768\n"
    "ds_read_b64_tr_b16 a[226:227], %[rd0] offset:33792\n"
    "v_mul_f32 v22, v22, v181\n"
    "v_mul_f32 v21, v21, v180\n"
    "v_mfma_f32_32x32x16_bf16 a[160:175], a[232:235], v[148:151], a[160:175]\n"
    "ds_read_b64_tr_b16 a[228:229], %[rd1] offset:32768\n"
    "ds_read_b64_tr_b16 a[230:231], %[rd1] offset:33792\n"
    "s_add_u32 m0, %[ldsw], 131072\n"
    "s_mov_b32 s40, 590080\n"
    "v_exp_f32 v22, v22\n"
    "v_fmaak_f32 v20, v182, v182, 0x409d30fc\n"
    "v_add_f32 v22, 1.0, v22\n"
    "v_mfma_f32_32x32x16_bf16 a[176:191], a[236:239], v[148:151], a[176:191]\n"
    "s_waitcnt lgkmcnt(4)\n"
    "ds_read_b64_tr_b16 a[232:233], %[rd0] offset:34816\n"
    "ds_read_b64_tr_b16 a[234:235], %[rd0] offset:35840\n"
    "buffer_load_dwordx4 %[w2a], %[srd2], s40 offen lds\n"
    "v_rcp_f32 v22, v22\n"
    "v_mul_f32 v20, v20, v182\n"
    "v_mfma_f32_32x32x16_bf16 a[160:175], a[240:243], v[152:155], a[160:175]\n"
    "ds_read_b64_tr_b16 a[236:237], %[rd1] offset:34816\n"
    "ds_read_b64_tr_b16 a[238:239], %[rd1] offset:35840\n"
    "v_mul_f32 v22, v22, v181\n"
    "v_cvt_pk_bf16_f32 v162, v21, v22\n"
    "v_exp_f32 v20, v20\n"
    "v_mfma_f32_32x32x16_bf16 a[176:191], a[244:247], v[152:155], a[176:191]\n"
    "ds_read_b64_tr_b16 a[240:241], %[rd0] offset:36864\n"
    "ds_read_b64_tr_b16 a[242:243], %[rd0] offset:37888\n"
    "v_fmaak_f32 v21, v183, v183, 0x409d30fc\n"
    "v_add_f32 v20, 1.0, v20\n"
    "v_mfma_f32_32x32x16_bf16 a[160:175], a[248:251], v[156:159], a[160:175]\n"
    "ds_read_b64_tr_b16 a[244:245], %[rd1] offset:36864\n"
    "ds_read_b64_tr_b16 a[246:247], %[rd1] offset:37888\n"
    "v_rcp_f32 v20, v20\n"
    "v_mul_f32 v21, v21, v183\n"
    "v_mfma_f32_32x32x16_bf16 a[176:191], a[252:255], v[156:159], a[176:191]\n"
    "s_waitcnt lgkmcnt(4)\n"
    "ds_read_b64_tr_b16 a[248:249], %[rd0] offset:38912\n"
    "ds_read_b64_tr_b16 a[250:251], %[rd0] offset:39936\n"
    "buffer_load_dwordx4 %[w2b], %[srd2], s40 offen offset:1024 lds\n"
    "v_mul_f32 v20, v20, v182\n"
    "v_exp_f32 v21, v21\n"
    "v_fmaak_f32 v22, v184, v184, 0x409d30fc\n"
    "v_mfma_f32_32x32x16_bf16 a[192:207], a[224:227], v[144:147], a[192:207]\n"
    "ds_read_b64_tr_b16 a[252:253], %[rd1] offset:38912\n"
    "ds_read_b64_tr_b16 a[254:255], %[rd1] offset:39936\n"
    "v_add_f32 v21, 1.0, v21\n"
    "v_rcp_f32 v21, v21\n"
    "v_mfma_f32_32x32x16_bf16 a[208:223], a[228:231], v[144:147], a[208:223]\n"
    "ds_read_b64_tr_b16 a[224:225], %[rd0] offset:40960\n"
    "ds_read_b64_tr_b16 a[226:227], %[rd0] offset:41984\n"
    "v_mul_f32 v22, v22, v184\n"
    "v_mul_f32 v21, v21, v183\n"
    "v_cvt_pk_bf16_f32 v163, v20, v21\n"
    "v_mfma_f32_32x32x16_bf16 a[192:207], a[232:235], v[148:151], a[192:207]\n"
    "ds_read_b64_tr_b16 a[228:229], %[rd1] offset:40960\n"
    "ds_read_b64_tr_b16 a[230:231], %[rd1] offset:41984\n"
    "s_add_u32 m0, %[ldsw], 139264\n"
    "s_mov_b32 s40, 590208\n"
    "v_exp_f32 v22, v22\n"
    "v_fmaak_f32 v20, v185, v185, 0x409d30fc\n"
    "v_mfma_f32_32x32x16_bf16 a[208:223], a[236:239], v[148:151], a[208:223]\n"
    "s_waitcnt lgkmcnt(4)\n"
    "ds_read_b64_tr_b16 a[232:233], %[rd0] offset:43008\n"
    "ds_read_b64_tr_b16 a[234:235], %[rd0] offset:44032\n"
    "buffer_load_dwordx4 %[w2a], %[srd2], s40 offen lds\n"
    "v_add_f32 v22, 1.0, v22\n"
    "v_rcp_f32 v22, v22\n"
    "v_mfma_f32_32x32x16_bf16 a[192:207], a[240:243], v[152:155], a[192:207]\n"
    "ds_read_b64_tr_b16 a[236:237], %[rd1] offset:43008\n"
    "ds_read_b64_tr_b16 a[238:239], %[rd1] offset:44032\n"
    "v_mul_f32 v20, v20, v185\n"
    "v_mul_f32 v22, v22, v184\n"
    "v_exp_f32 v20, v20\n"
    "v_mfma_f32_32x32x16_bf16 a[208:223], a[244:247], v[152:155], a[208:223]\n"
    "ds_read_b64_tr_b16 a[240:241], %[rd0] offset:45056\n"
    "ds_read_b64_tr_b16 a[242:243], %[rd0] offset:46080\n"
    "v_fmaak_f32 v21, v186, v186, 0x409d30fc\n"
    "v_add_f32 v20, 1.0, v20\n"
    "v_mfma_f32_32x32x16_bf16 a[192:207], a[248:251], v[156:159], a[192:207]\n"
    "ds_read_b64_tr_b16 a[244:245], %[rd1] offset:45056\n"
    "ds_read_b64_tr_b16 a[246:247], %[rd1] offset:46080\n"
    "v_rcp_f32 v20, v20\n"
    "v_mul_f32 v21, v21, v186\n"
    "v_mul_f32 v20, v20, v185\n"
    "v_mfma_f32_32x32x16_bf16 a[208:223], a[252:255], v[156:159], a[208:223]\n"
    "s_waitcnt lgkmcnt(4)\n"
    "ds_read_b64_tr_b16 a[248:249], %[rd0] offset:47104\n"
    "ds_read_b64_tr_b16 a[250:251], %[rd0] offset:48128\n"
    "buffer_load_dwordx4 %[w2b], %[srd2], s40 offen offset:1024 lds\n"
    "v_cvt_pk_bf16_f32 v164, v22, v20\n"
    "v_exp_f32 v21, v21\n"
    "v_mfma_f32_32x32x16_bf16 a[0:15], a[224:227], v[32:35], 0\n"
    "ds_read_b64_tr_b16 a[252:253], %[rd1] offset:47104\n"
    "ds_read_b64_tr_b16 a[254:255], %[rd1] offset:48128\n"
    "v_fmaak_f32 v22, v187, v187, 0x409d30fc\n"
    "v_add_f32 v21, 1.0, v21\n"
    "v_mfma_f32_32x32x16_bf16 a[16:31], a[228:231], v[32:35], 0\n"
    "s_waitcnt vmcnt(18)\n"
    "s_barrier\n"
    "ds_read_b64_tr_b16 a[224:225], v16 offset:0\n"
    "ds_read_b64_tr_b16 a[226:227], v16 offset:1024\n"
    "v_mfma_f32_32x32x16_bf16 a[0:15], a[232:235], v[36:39], a[0:15]\n"
    "ds_read_b64_tr_b16 a[228:229], v17 offset:0\n"
    "ds_read_b64_tr_b16 a[230:231], v17 offset:1024\n"
    "s_add_u32 m0, %[ldsw], 0\n"
    "s_mov_b32 s40, 640\n"
    "v_rcp_f32 v21, v21\n"
    "v_mul_f32 v22, v22, v187\n"
    "v_mul_f32 v21, v21, v186\n"
    "v_mfma_f32_32x32x16_bf16 a[16:31], a[236:239], v[36:39], a[16:31]\n"
    "s_waitcnt lgkmcnt(4)\n"
    "ds_read_b64_tr_b16 a[232:233], v16 offset:2048\n"
    "ds_read_b64_tr_b16 a[234:235], v16 offset:3072\n"
    "buffer_load_dwordx4 %[w2a], %[srd2], s40 offen lds\n"
    "v_exp_f32 v22, v22\n"
    "v_fmaak_f32 v20, v188, v188, 0x409d30fc\n"
    "v_mfma_f32_32x32x16_bf16 a[0:15], a[240:243], v[40:43], a[0:15]\n"
    "ds_read_b64_tr_b16 a[236:237], v17 offset:2048\n"
    "ds_read_b64_tr_b16 a[238:239], v17 offset:3072\n"
    "v_add_f32 v22, 1.0, v22\n"
    "v_rcp_f32 v22, v22\n"
    "v_mul_f32 v20, v20, v188\n"
    "v_mfma_f32_32x32x16_bf16 a[16:31], a[244:247], v[40:43], a[16:31]\n"
    "ds_read_b64_tr_b16 a[240:241], v16 offset:4096\n"
    "ds_read_b64_tr_b16 a[242:243], v16 offset:5120\n"
    "v_mul_f32 v22, v22, v187\n"
    "v_cvt_pk_bf16_f32 v165, v21, v22\n"
    "v_mfma_f32_32x32x16_bf16 a[0:15], a[248:251], v[44:47], a[0:15]\n"
    "ds_read_b64_tr_b16 a[244:245], v17 offset:4096\n"
    "ds_read_b64_tr_b16 a[246:247], v17 offset:5120\n"
    "v_exp_f32 v20, v20\n"
    "v_fmaak_f32 v21, v189, v189, 0x409d30fc\n"
    "v_add_f32 v20, 1.0, v20\n"
    "v_mfma_f32_32x32x16_bf16 a[16:31], a[252:255], v[44:47], a[16:31]\n"
    "s_waitcnt lgkmcnt(4)\n"
    "ds_read_b64_tr_b16 a[248:249], v16 offset:6144\n"
    "ds_read_b64_tr_b16 a[250:251], v16 offset:7168\n"
    "buffer_load_dwordx4 %[w2b], %[srd2], s40 offen offset:1024 lds\n"
    "v_rcp_f32 v20, v20\n"
    "v_mul_f32 v21, v21, v189\n"
    "v_mfma_f32_32x32x16_bf16 a[0:15], a[224:227], v[48:51], a[0:15]\n"
    "ds_read_b64_tr_b16 a[252:253], v17 offset:6144\n"
    "ds_read_b64_tr_b16 a[254:255], v17 offset:7168\n"
    "v_mul_f32 v20, v20, v188\n"
    "v_exp_f32 v21, v21\n"
    "v_mfma_f32_32x32x16_bf16 a[16:31], a[228:231], v[48:51], a[16:31]\n"
    "ds_read_b64_tr_b16 a[224:225], v16 offset:8192\n"
    "ds_read_b64_tr_b16 a[226:227], v16 offset:9216\n"
    "v_fmaak_f32 v22, v190, v190, 0x409d30fc\n"
    "v_add_f32 v21, 1.0, v21\n"
    "v_rcp_f32 v21, v21\n"
    "v_mfma_f32_32x32x16_bf16 a[0:15], a[232:235], v[52:55], a[0:15]\n"
    "ds_read_b64_tr_b16 a[228:229], v17 offset:8192\n"
    "ds_read_b64_tr_b16 a[230:231], v17 offset:9216\n"
    "s_add_u32 m0, %[ldsw], 8192\n"
    "s_mov_b32 s40, 74368\n"
    "v_mul_f32 v22, v22, v190\n"
    "v_mul_f32 v21, v21, v189\n"
    "v_mfma_f32_32x32x16_bf16 a[16:31], a[236:239], v[52:55], a[16:31]\n"
    "s_waitcnt lgkmcnt(4)\n"
    "ds_read_b64_tr_b16 a[232:233], v16 offset:10240\n"
    "ds_read_b64_tr_b16 a[234:235], v16 offset:11264\n"
    "buffer_load_dwordx4 %[w2a], %[srd2], s40 offen lds\n"
    "v_cvt_pk_bf16_f32 v166, v20, v21\n"
    "v_exp_f32 v22, v22\n"
    "v_fmaak_f32 v20, v191, v191, 0x409d30fc\n"
    "v_mfma_f32_32x32x16_bf16 a[0:15], a[240:243], v[56:59], a[0:15]\n"
    "ds_read_b64_tr_b16 a[236:237], v17 offset:10240\n"
    "ds_read_b64_tr_b16 a[238:239], v17 offset:11264\n"
    "v_add_f32 v22, 1.0, v22\n"
    "v_rcp_f32 v22, v22\n"
    "v_mfma_f32_32x32x16_bf16 a[16:31], a[244:247], v[56:59], a[16:31]\n"
    "ds_read_b64_tr_b16 a[240:241], v16 offset:12288\n"
    "ds_read_b64_tr_b16 a[242:243], v16 offset:13312\n"
    "v_mul_f32 v20, v20, v191\n"
    "v_mul_f32 v22, v22, v190\n"
    "v_mfma_f32_32x32x16_bf16 a[0:15], a[248:251], v[60:63], a[0:15]\n"
    "ds_read_b64_tr_b16 a[244:245], v17 offset:12288\n"
    "ds_read_b64_tr_b16 a[246:247], v17 offset:13312\n"
    "v_exp_f32 v20, v20\n"
    "v_fmaak_f32 v21, v192, v192, 0x409d30fc\n"
    "v_add_f32 v20, 1.0, v20\n"
    "v_mfma_f32_32x32x16_bf16 a[16:31], a[252:255], v[60:63], a[16:31]\n"
    "s_waitcnt lgkmcnt(4)\n"
    "ds_read_b64_tr_b16 a[248:249], v16 offset:14336\n"
    "ds_read_b64_tr_b16 a[250:251], v16 offset:15360\n"
    "buffer_load_dwordx4 %[w2b], %[srd2], s40 offen offset:1024 lds\n"
    "v_rcp_f32 v20, v20\n"
    "v_mul_f32 v21, v21, v192\n"
    "v_mfma_f32_32x32x16_bf16 a[0:15], a[224:227], v[64:67], a[0:15]\n"
    "ds_read_b64_tr_b16 a[252:253], v17 offset:14336\n"
    "ds_read_b64_tr_b16 a[254:255], v17 offset:15360\n"
    "v_mul_f32 v20, v20, v191\n"
    "v_cvt_pk_bf16_f32 v167, v22, v20\n"
    "v_exp_f32 v21, v21\n"
    "v_mfma_f32_32x32x16_bf16 a[16:31], a[228:231], v[64:67], a[16:31]\n"
    "ds_read_b64_tr_b16 a[224:225], v16 offset:16384\n"
    "ds_read_b64_tr_b16 a[226:227], v16 offset:17408\n"
    "v_fmaak_f32 v22, v193, v193, 0x409d30fc\n"
    "v_add_f32 v21, 1.0, v21\n"
    "v_mfma_f32_32x32x16_bf16 a[0:15], a[232:235], v[68:71], a[0:15]\n"
    "ds_read_b64_tr_b16 a[228:229], v17 offset:16384\n"
    "ds_read_b64_tr_b16 a[230:231], v17 offset:17408\n"
    "s_add_u32 m0, %[ldsw], 16384\n"
    "s_mov_b32 s40, 148096\n"
    "v_rcp_f32 v21, v21\n"
    "v_mul_f32 v22, v22, v193\n"
    "v_mul_f32 v21, v21, v192\n"
    "v_mfma_f32_32x32x16_bf16 a[16:31], a[236:239], v[68:71], a[16:31]\n"
    "s_waitcnt lgkmcnt(4)\n"
    "ds_read_b64_tr_b16 a[232:233], v16 offset:18432\n"
    "ds_read_b64_tr_b16 a[234:235], v16 offset:19456\n"
    "buffer_load_dwordx4 %[w2a], %[srd2], s40 offen lds\n"
    "v_exp_f32 v22, v22\n"
    "v_fmaak_f32 v20, v194, v194, 0x409d30fc\n"
    "v_mfma_f32_32x32x16_bf16 a[0:15], a[240:243], v[72:75], a[0:15]\n"
    "ds_read_b64_tr_b16 a[236:237], v17 offset:18432\n"
    "ds_read_b64_tr_b16 a[238:239], v17 offset:19456\n"
    "v_add_f32 v22, 1.0, v22\n"
    "v_rcp_f32 v22, v22\n"
    "v_mfma_f32_32x32x16_bf16 a[16:31], a[244:247], v[72:75], a[16:31]\n"
    "ds_read_b64_tr_b16 a[240:241], v16 offset:20480\n"
    "ds_read_b64_tr_b16 a[242:243], v16 offset:21504\n"
    "v_mul_f32 v20, v20, v194\n"
    "v_mul_f32 v22, v22, v193\n"
    "v_cvt_pk_bf16_f32 v168, v21, v22\n"
    "v_mfma_f32_32x32x16_bf16 a[0:15], a[248:251], v[76:79], a[0:15]\n"
    "ds_read_b64_tr_b16 a[244:245], v17 offset:20480\n"
    "ds_read_b64_tr_b16 a[246:247], v17 offset:21504\n"
    "v_exp_f32 v20, v20\n"
    "v_fmaak_f32 v21, v195, v195, 0x409d30fc\n"
    "v_mfma_f32_32x32x16_bf16 a[16:31], a[252:255], v[76:79], a[16:31]\n"
    "s_waitcnt lgkmcnt(4)\n"
    "ds_read_b64_tr_b16 a[248:249], v16 offset:22528\n"
    "ds_read_b64_tr_b16 a[250:251], v16 offset:23552\n"
    "buffer_load_dwordx4 %[w2b], %[srd2], s40 offen offset:1024 lds\n"
    "v_add_f32 v20, 1.0, v20\n"
    "v_rcp_f32 v20, v20\n"
    "v_mul_f32 v21, v21, v195\n"
    "v_mfma_f32_32x32x16_bf16 a[0:15], a[224:227], v[80:83], a[0:15]\n"
    "ds_read_b64_tr_b16 a[252:253], v17 offset:22528\n"
    "ds_read_b64_tr_b16 a[254:255], v17 offset:23552\n"
    "v_mul_f32 v20, v20, v194\n"
    "v_exp_f32 v21, v21\n"
    "v_mfma_f32_32x32x16_bf16 a[16:31], a[228:231], v[80:83], a[16:31]\n"
    "s_waitcnt vmcnt(18)\n"
    "s_barrier\n"
    "ds_read_b64_tr_b16 a[224:225], v16 offset:24576\n"
    "ds_read_b64_tr_b16 a[226:227], v16 offset:25600\n"
    "v_mfma_f32_32x32x16_bf16 a[0:15], a[232:235], v[84:87], a[0:15]\n"
    "ds_read_b64_tr_b16 a[228:229], v17 offset:24576\n"
    "ds_read_b64_tr_b16 a[230:231], v17 offset:25600\n"
    "s_add_u32 m0, %[ldsw], 24576\n"
    "s_mov_b32 s40, 221824\n"
    "v_fmaak_f32 v22, v196, v196, 0x409d30fc\n"
    "v_add_f32 v21, 1.0, v21\n"
    "v_mfma_f32_32x32x16_bf16 a[16:31], a[236:239], v[84:87], a[16:31]\n"
    "s_waitcnt lgkmcnt(4)\n"
    "ds_read_b64_tr_b16 a[232:233], v16 offset:26624\n"
    "ds_read_b64_tr_b16 a[234:235], v16 offset:27648\n"
    "buffer_load_dwordx4 %[w2a], %[srd2], s40 offen lds\n"
    "v_rcp_f32 v21, v21\n"
    "v_mul_f32 v22, v22, v196\n"
    "v_mul_f32 v21, v21, v195\n"
    "v_mfma_f32_32x32x16_bf16 a[0:15], a[240:243], v[88:91], a[0:15]\n"
    "ds_read_b64_tr_b16 a[236:237], v17 offset:26624\n"
    "ds_read_b64_tr_b16 a[238:239], v17 offset:27648\n"
    "v_cvt_pk_bf16_f32 v169, v20, v21\n"
    "v_exp_f32 v22, v22\n"
    "v_mfma_f32_32x32x16_bf16 a[16:31], a[244:247], v[88:91], a[16:31]\n"
    "ds_read_b64_tr_b16 a[240:241], v16 offset:28672\n"
    "ds_read_b64_tr_b16 a[242:243], v16 offset:29696\n"
    "v_fmaak_f32 v20, v197, v197, 0x409d30fc\n"
    "v_add_f32 v22, 1.0, v22\n"
    "v_rcp_f32 v22, v22\n"
    "v_mfma_f32_32x32x16_bf16 a[0:15], a[248:251], v[92:95], a[0:15]\n"
    "ds_read_b64_tr_b16 a[244:245], v17 offset:28672\n"
    "ds_read_b64_tr_b16 a[246:247], v17 offset:29696\n"
    "v_mul_f32 v20, v20, v197\n"
    "v_mul_f32 v22, v22, v196\n"
    "v_mfma_f32_32x32x16_bf16 a[16:31], a[252:255], v[92:95], a[16:31]\n"
    "s_waitcnt lgkmcnt(4)\n"
    "ds_read_b64_tr_b16 a[248:249], v16 offset:30720\n"
    "ds_read_b64_tr_b16 a[250:251], v16 offset:31744\n"
    "buffer_load_dwordx4 %[w2b], %[srd2], s40 offen offset:1024 lds\n"
    "v_exp_f32 v20, v20\n"
    "v_fmaak_f32 v21, v198, v198, 0x409d30fc\n"
    "v_mfma_f32_32x32x16_bf16 a[0:15], a[224:227], v[96:99], a[0:15]\n"
    "ds_read_b64_tr_b16 a[252:253], v17 offset:30720\n"
    "ds_read_b64_tr_b16 a[254:255], v17 offset:31744\n"
    "v_add_f32 v20, 1.0, v20\n"
    "v_rcp_f32 v20, v20\n"
    "v_mul_f32 v21, v21, v198\n"
    "v_mfma_f32_32x32x16_bf16 a[16:31], a[228:231], v[96:99], a[16:31]\n"
    "ds_read_b64_tr_b16 a[224:225], v16 offset:32768\n"
    "ds_read_b64_tr_b16 a[226:227], v16 offset:33792\n"
    "v_mul_f32 v20, v20, v197\n"
    "v_cvt_pk_bf16_f32 v170, v22, v20\n"
    "v_mfma_f32_32x32x16_bf16 a[0:15], a[232:235], v[100:103], a[0:15]\n"
    "ds_read_b64_tr_b16 a[228:229], v17 offset:32768\n"
    "ds_read_b64_tr_b16 a[230:231], v17 offset:33792\n"
    "s_add_u32 m0, %[ldsw], 32768\n"
    "s_mov_b32 s40, 295552\n"
    "v_exp_f32 v21, v21\n"
    "v_fmaak_f32 v22, v199, v199, 0x409d30fc\n"
    "v_add_f32 v21, 1.0, v21\n"
    "v_mfma_f32_32x32x16_bf16 a[16:31], a[236:239], v[100:103], a[16:31]\n"
    "s_waitcnt lgkmcnt(4)\n"
    "ds_read_b64_tr_b16 a[232:233], v16 offset:34816\n"
    "ds_read_b64_tr_b16 a[234:235], v16 offset:35840\n"
    "buffer_load_dwordx4 %[w2a], %[srd2], s40 offen lds\n"
    "v_rcp_f32 v21, v21\n"
    "v_mul_f32 v22, v22, v199\n"
    "v_mfma_f32_32x32x16_bf16 a[0:15], a[240:243], v[104:107], a[0:15]\n"
    "ds_read_b64_tr_b16 a[236:237], v17 offset:34816\n"
    "ds_read_b64_tr_b16 a[238:239], v17 offset:35840\n"
    "v_mul_f32 v21, v21, v198\n"
    "v_exp_f32 v22, v22\n"
    "v_fmaak_f32 v20, v200, v200, 0x409d30fc\n"
    "v_mfma_f32_32x32x16_bf16 a[16:31], a[244:247], v[104:107], a[16:31]\n"
    "ds_read_b64_tr_b16 a[240:241], v16 offset:36864\n"
    "ds_read_b64_tr_b16 a[242:243], v16 offset:37888\n"
    "v_add_f32 v22, 1.0, v22\n"
    "v_rcp_f32 v22, v22\n"
    "v_mfma_f32_32x32x16_bf16 a[0:15], a[248:251], v[108:111], a[0:15]\n"
    "ds_read_b64_tr_b16 a[244:245], v17 offset:36864\n"
    "ds_read_b64_tr_b16 a[246:247], v17 offset:37888\n"
    "v_mul_f32 v20, v20, v200\n"
    "v_mul_f32 v22, v22, v199\n"
    "v_mfma_f32_32x32x16_bf16 a[16:31], a[252:255], v[108:111], a[16:31]\n"
    "s_waitcnt lgkmcnt(4)\n"
    "ds_read_b64_tr_b16 a[248:249], v16 offset:38912\n"
    "ds_read_b64_tr_b16 a[250:251], v16 offset:39936\n"
    "buffer_load_dwordx4 %[w2b], %[srd2], s40 offen offset:1024 lds\n"
    "v_cvt_pk_bf16_f32 v171, v21, v22\n"
    "v_exp_f32 v20, v20\n"
    "v_fmaak_f32 v21, v201, v201, 0x409d30fc\n"
    "v_mfma_f32_32x32x16_bf16 a[0:15], a[224:227], v[112:115], a[0:15]\n"
    "ds_read_b64_tr_b16 a[252:253], v17 offset:38912\n"
    "ds_read_b64_tr_b16 a[254:255], v17 offset:39936\n"
    "v_add_f32 v20, 1.0, v20\n"
    "v_rcp_f32 v20, v20\n"
    "v_mfma_f32_32x32x16_bf16 a[16:31], a[228:231], v[112:115], a[16:31]\n"
    "ds_read_b64_tr_b16 a[224:225], v16 offset:40960\n"
    "ds_read_b64_tr_b16 a[226:227], v16 offset:41984\n"
    "v_mul_f32 v21, v21, v201\n"
    "v_mul_f32 v20, v20, v200\n"
    "v_exp_f32 v21, v21\n"
    "v_mfma_f32_32x32x16_bf16 a[0:15], a[232:235], v[116:119], a[0:15]\n"
    "ds_read_b64_tr_b16 a[228:229], v17 offset:40960\n"
    "ds_read_b64_tr_b16 a[230:231], v17 offset:41984\n"
    "s_add_u32 m0, %[ldsw], 40960\n"
    "s_mov_b32 s40, 369280\n"
    "v_fmaak_f32 v22, v202, v202, 0x409d30fc\n"
    "v_add_f32 v21, 1.0, v21\n"
    "v_mfma_f32_32x32x16_bf16 a[16:31], a[236:239], v[116:119], a[16:31]\n"
    "s_waitcnt lgkmcnt(4)\n"
    "ds_read_b64_tr_b16 a[232:233], v16 offset:43008\n"
    "ds_read_b64_tr_b16 a[234:235], v16 offset:44032\n"
    "buffer_load_dwordx4 %[w2a], %[srd2], s40 offen lds\n"
    "v_rcp_f32 v21, v21\n"
    "v_mul_f32 v22, v22, v202\n"
    "v_mfma_f32_32x32x16_bf16 a[0:15], a[240:243], v[120:123], a[0:15]\n"
    "ds_read_b64_tr_b16 a[236:237], v17 offset:43008\n"
    "ds_read_b64_tr_b16 a[238:239], v17 offset:44032\n"
    "v_mul_f32 v21, v21, v201\n"
    "v_cvt_pk_bf16_f32 v172, v20, v21\n"
    "v_exp_f32 v22, v22\n"
    "v_mfma_f32_32x32x16_bf16 a[16:31], a[244:247], v[120:123], a[16:31]\n"
    "ds_read_b64_tr_b16 a[240:241], v16 offset:45056\n"
    "ds_read_b64_tr_b16 a[242:243], v16 offset:46080\n"
    "v_fmaak_f32 v20, v203, v203, 0x409d30fc\n"
    "v_add_f32 v22, 1.0, v22\n"
    "v_mfma_f32_32x32x16_bf16 a[0:15], a[248:251], v[124:127], a[0:15]\n"
    "ds_read_b64_tr_b16 a[244:245], v17 offset:45056\n"
    "ds_read_b64_tr_b16 a[246:247], v17 offset:46080\n"
    "v_rcp_f32 v22, v22\n"
    "v_mul_f32 v20, v20, v203\n"
    "v_mul_f32 v22, v22, v202\n"
    "v_mfma_f32_32x32x16_bf16 a[16:31], a[252:255], v[124:127], a[16:31]\n"
    "s_waitcnt lgkmcnt(4)\n"
    "ds_read_b64_tr_b16 a[248:249], v16 offset:47104\n"
    "ds_read_b64_tr_b16 a[250:251], v16 offset:48128\n"
    "buffer_load_dwordx4 %[w2b], %[srd2], s40 offen offset:1024 lds\n"
    "v_exp_f32 v20, v20\n"
    "v_fmaak_f32 v21, v204, v204, 0x409d30fc\n"
    "v_mfma_f32_32x32x16_bf16 a[0:15], a[224:227], v[128:131], a[0:15]\n"
    "ds_read_b64_tr_b16 a[252:253], v17 offset:47104\n"
    "ds_read_b64_tr_b16 a[254:255], v17 offset:48128\n"
    "v_add_f32 v20, 1.0, v20\n"
    "v_rcp_f32 v20, v20\n"
    "v_mul_f32 v21, v21, v204\n"
    "v_mfma_f32_32x32x16_bf16 a[16:31], a[228:231], v[128:131], a[16:31]\n"
    "s_waitcnt vmcnt(18)\n"
    "s_barrier\n"
    "ds_read_b64_tr_b16 a[224:225], v18 offset:0\n"
    "ds_read_b64_tr_b16 a[226:227], v18 offset:1024\n"
    "v_mfma_f32_32x32x16_bf16 a[0:15], a[232:235], v[132:135], a[0:15]\n"
    "ds_read_b64_tr_b16 a[228:229], v19 offset:0\n"
    "ds_read_b64_tr_b16 a[230:231], v19 offset:1024\n"
    "s_add_u32 m0, %[ldsw], 49152\n"
    "s_mov_b32 s40, 443008\n"
    "v_mul_f32 v20, v20, v203\n"
    "v_cvt_pk_bf16_f32 v173, v22, v20\n"
    "v_mfma_f32_32x32x16_bf16 a[16:31], a[236:239], v[132:135], a[16:31]\n"
    "s_waitcnt lgkmcnt(4)\n"
    "ds_read_b64_tr_b16 a[232:233], v18 offset:2048\n"
    "ds_read_b64_tr_b16 a[234:235], v18 offset:3072\n"
    "buffer_load_dwordx4 %[w2a], %[srd2], s40 offen lds\n"
    "v_exp_f32 v21, v21\n"
    "v_fmaak_f32 v22, v205, v205, 0x409d30fc\n"
    "v_mfma_f32_32x32x16_bf16 a[0:15], a[240:243], v[136:139], a[0:15]\n"
    "ds_read_b64_tr_b16 a[236:237], v19 offset:2048\n"
    "ds_read_b64_tr_b16 a[238:239], v19 offset:3072\n"
    "v_add_f32 v21, 1.0, v21\n"
    "v_rcp_f32 v21, v21\n"
    "v_mul_f32 v22, v22, v205\n"
    "v_mfma_f32_32x32x16_bf16 a[16:31], a[244:247], v[136:139], a[16:31]\n"
    "ds_read_b64_tr_b16 a[240:241], v18 offset:4096\n"
    "ds_read_b64_tr_b16 a[242:243], v18 offset:5120\n"
    "v_mul_f32 v21, v21, v204\n"
    "v_exp_f32 v22, v22\n"
    "v_mfma_f32_32x32x16_bf16 a[0:15], a[248:251], v[140:143], a[0:15]\n"
    "ds_read_b64_tr_b16 a[244:245], v19 offset:4096\n"
    "ds_read_b64_tr_b16 a[246:247], v19 offset:5120\n"
    "v_fmaak_f32 v20, v206, v206, 0x409d30fc\n"
    "v_add_f32 v22, 1.0, v22\n"
    "v_rcp_f32 v22, v22\n"
    "v_mfma_f32_32x32x16_bf16 a[16:31], a[252:255], v[140:143], a[16:31]\n"
    "s_waitcnt lgkmcnt(4)\n"
    "ds_read_b64_tr_b16 a[248:249], v18 offset:6144\n"
    "ds_read_b64_tr_b16 a[250:251], v18 offset:7168\n"
    "buffer_load_dwordx4 %[w2b], %[srd2], s40 offen offset:1024 lds\n"
    "v_mul_f32 v20, v20, v206\n"
    "v_mul_f32 v22, v22, v205\n"
    "v_mfma_f32_32x32x16_bf16 a[0:15], a[224:227], v[144:147], a[0:15]\n"
    "ds_read_b64_tr_b16 a[252:253], v19 offset:6144\n"
    "ds_read_b64_tr_b16 a[254:255], v19 offset:7168\n"
    "v_cvt_pk_bf16_f32 v174, v21, v22\n"
    "v_exp_f32 v20, v20\n"
    "v_mfma_f32_32x32x16_bf16 a[16:31], a[228:231], v[144:147], a[16:31]\n"
    "ds_read_b64_tr_b16 a[224:225], v18 offset:8192\n"
    "ds_read_b64_tr_b16 a[226:227], v18 offset:9216\n"
    "v_fmaak_f32 v21, v207, v207, 0x409d30fc\n"
    "v_add_f32 v20, 1.0, v20\n"
    "v_rcp_f32 v20, v20\n"
    "v_mfma_f32_32x32x16_bf16 a[0:15], a[232:235], v[148:151], a[0:15]\n"
    "ds_read_b64_tr_b16 a[228:229], v19 offset:8192\n"
    "ds_read_b64_tr_b16 a[230:231], v19 offset:9216\n"
    "s_add_u32 m0, %[ldsw], 57344\n"
    "s_mov_b32 s40, 516736\n"
    "v_mul_f32 v21, v21, v207\n"
    "v_mul_f32 v20, v20, v206\n"
    "v_mfma_f32_32x32x16_bf16 a[16:31], a[236:239], v[148:151], a[16:31]\n"
    "s_waitcnt lgkmcnt(4)\n"
    "ds_read_b64_tr_b16 a[232:233], v18 offset:10240\n"
    "ds_read_b64_tr_b16 a[234:235], v18 offset:11264\n"
    "buffer_load_dwordx4 %[w2a], %[srd2], s40 offen lds\n"
    "v_exp_f32 v21, v21\n"
    "s_nop 0\n"
    "v_add_f32 v21, 1.0, v21\n"
    "v_mfma_f32_32x32x16_bf16 a[0:15], a[240:243], v[152:155], a[0:15]\n"
    "ds_read_b64_tr_b16 a[236:237], v19 offset:10240\n"
    "ds_read_b64_tr_b16 a[238:239], v19 offset:11264\n"
    "v_rcp_f32 v21, v21\n"
    "s_nop 0\n"
    "v_mfma_f32_32x32x16_bf16 a[16:31], a[244:247], v[152:155], a[16:31]\n"
    "ds_read_b64_tr_b16 a[240:241], v18 offset:12288\n"
    "ds_read_b64_tr_b16 a[242:243], v18 offset:13312\n"
    "v_mul_f32 v21, v21, v207\n"
    "v_cvt_pk_bf16_f32 v175, v20, v21\n"
    "v_mfma_f32_32x32x16_bf16 a[0:15], a[248:251], v[156:159], a[0:15]\n"
    "ds_read_b64_tr_b16 a[244:245], v19 offset:12288\n"
    "ds_read_b64_tr_b16 a[246:247], v19 offset:13312\n"
    "v_mfma_f32_32x32x16_bf16 a[16:31], a[252:255], v[156:159], a[16:31]\n"
    "s_waitcnt lgkmcnt(4)\n"
    "ds_read_b64_tr_b16 a[248:249], v18 offset:14336\n"
    "ds_read_b64_tr_b16 a[250:251], v18 offset:15360\n"
    "buffer_load_dwordx4 %[w2b], %[srd2], s40 offen offset:1024 lds\n"
    "v_mfma_f32_32x32x16_bf16 a[0:15], a[224:227], v[160:163], a[0:15]\n"
    "ds_read_b64_tr_b16 a[252:253], v19 offset:14336\n"
    "ds_read_b64_tr_b16 a[254:255], v19 offset:15360\n"
    "v_mfma_f32_32x32x16_bf16 a[16:31], a[228:231], v[160:163], a[16:31]\n"
    "ds_read_b64_tr_b16 a[224:225], v18 offset:16384\n"
    "ds_read_b64_tr_b16 a[226:227], v18 offset:17408\n"
    "v_mfma_f32_32x32x16_bf16 a[0:15], a[232:235], v[164:167], a[0:15]\n"
    "ds_read_b64_tr_b16 a[228:229], v19 offset:16384\n"
    "ds_read_b64_tr_b16 a[230:231], v19 offset:17408\n"
    "s_add_u32 m0, %[ldsw], 65536\n"
    "s_mov_b32 s40, 590464\n"
    "v_mfma_f32_32x32x16_bf16 a[16:31], a[236:239], v[164:167], a[16:31]\n"
    "s_waitcnt lgkmcnt(4)\n"
    "ds_read_b64_tr_b16 a[232:233], v18 offset:18432\n"
    "ds_read_b64_tr_b16 a[234:235], v18 offset:19456\n"
    "buffer_load_dwordx4 %[w2a], %[srd2], s40 offen lds\n"
    "v_mfma_f32_32x32x16_bf16 a[0:15], a[240:243], v[168:171], a[0:15]\n"
    "ds_read_b64_tr_b16 a[236:237], v19 offset:18432\n"
    "ds_read_b64_tr_b16 a[238:239], v19 offset:19456\n"
    "v_mfma_f32_32x32x16_bf16 a[16:31], a[244:247], v[168:171], a[16:31]\n"
    "ds_read_b64_tr_b16 a[240:241], v18 offset:20480\n"
    "ds_read_b64_tr_b16 a[242:243], v18 offset:21504\n"
    "v_mfma_f32_32x32x16_bf16 a[0:15], a[248:251], v[172:175], a[0:15]\n"
    "ds_read_b64_tr_b16 a[244:245], v19 offset:20480\n"
    "ds_read_b64_tr_b16 a[246:247], v19 offset:21504\n"
    "v_mfma_f32_32x32x16_bf16 a[16:31], a[252:255], v[172:175], a[16:31]\n"
    "s_waitcnt lgkmcnt(4)\n"
    "ds_read_b64_tr_b16 a[248:249], v18 offset:22528\n"
    "ds_read_b64_tr_b16 a[250:251], v18 offset:23552\n"
    "buffer_load_dwordx4 %[w2b], %[srd2], s40 offen offset:1024 lds\n"
    "v_mfma_f32_32x32x16_bf16 v[208:223], a[224:227], v[160:163], v[208:223]\n"
    "ds_read_b64_tr_b16 a[252:253], v19 offset:22528\n"
    "ds_read_b64_tr_b16 a[254:255], v19 offset:23552\n"
    "v_mfma_f32_32x32x16_bf16 v[224:239], a[228:231], v[160:163], v[224:239]\n"
    "s_waitcnt vmcnt(18)\n"
    "s_barrier\n"
    "ds_read_b64_tr_b16 a[224:225], v18 offset:24576\n"
    "ds_read_b64_tr_b16 a[226:227], v18 offset:25600\n"
    "v_mfma_f32_32x32x16_bf16 v[208:223], a[232:235], v[164:167], v[208:223]\n"
    "ds_read_b64_tr_b16 a[228:229], v19 offset:24576\n"
    "ds_read_b64_tr_b16 a[230:231], v19 offset:25600\n"
    "s_add_u32 m0, %[ldsw], 73728\n"
    "s_mov_b32 s40, 768\n"
    "v_mfma_f32_32x32x16_bf16 v[224:239], a[236:239], v[164:167], v[224:239]\n"
    "s_waitcnt lgkmcnt(4)\n"
    "ds_read_b64_tr_b16 a[232:233], v18 offset:26624\n"
    "ds_read_b64_tr_b16 a[234:235], v18 offset:27648\n"
    "buffer_load_dwordx4 %[w2a], %[srd2], s40 offen lds\n"
    "v_mfma_f32_32x32x16_bf16 v[208:223], a[240:243], v[168:171], v[208:223]\n"
    "ds_read_b64_tr_b16 a[236:237], v19 offset:26624\n"
    "ds_read_b64_tr_b16 a[238:239], v19 offset:27648\n"
    "v_mfma_f32_32x32x16_bf16 v[224:239], a[244:247], v[168:171], v[224:239]\n"
    "ds_read_b64_tr_b16 a[240:241], v18 offset:28672\n"
    "ds_read_b64_tr_b16 a[242:243], v18 offset:29696\n"
    "v_mfma_f32_32x32x16_bf16 v[208:223], a[248:251], v[172:175], v[208:223]\n"
    "ds_read_b64_tr_b16 a[244:245], v19 offset:28672\n"
    "ds_read_b64_tr_b16 a[246:247], v19 offset:29696\n"
    "v_mfma_f32_32x32x16_bf16 v[224:239], a[252:255], v[172:175], v[224:239]\n"
    "s_waitcnt lgkmcnt(4)\n"
    "ds_read_b64_tr_b16 a[248:249], v18 offset:30720\n"
    "ds_read_b64_tr_b16 a[250:251], v18 offset:31744\n"
    "buffer_load_dwordx4 %[w2b], %[srd2], s40 offen offset:1024 lds\n"
    "v_mfma_f32_32x32x16_bf16 v[240:255], a[224:227], v[160:163], v[240:255]\n"
    "ds_read_b64_tr_b16 a[252:253], v19 offset:30720\n"
    "ds_read_b64_tr_b16 a[254:255], v19 offset:31744\n"
    "v_mfma_f32_32x32x16_bf16 a[144:159], a[228:231], v[160:163], a[144:159]\n"
    "ds_read_b64_tr_b16 a[224:225], v18 offset:32768\n"
    "ds_read_b64_tr_b16 a[226:227], v18 offset:33792\n"
    "v_mfma_f32_32x32x16_bf16 v[240:255], a[232:235], v[164:167], v[240:255]\n"
    "ds_read_b64_tr_b16 a[228:229], v19 offset:32768\n"
    "ds_read_b64_tr_b16 a[230:231], v19 offset:33792\n"
    "s_add_u32 m0, %[ldsw], 81920\n"
    "s_mov_b32 s40, 74496\n"
    "v_mfma_f32_32x32x16_bf16 a[144:159], a[236:239], v[164:167], a[144:159]\n"
    "s_waitcnt lgkmcnt(4)\n"
    "ds_read_b64_tr_b16 a[232:233], v18 offset:34816\n"
    "ds_read_b64_tr_b16 a[234:235], v18 offset:35840\n"
    "buffer_load_dwordx4 %[w2a], %[srd2], s40 offen lds\n"
    "v_mfma_f32_32x32x16_bf16 v[240:255], a[240:243], v[168:171], v[240:255]\n"
    "ds_read_b64_tr_b16 a[236:237], v19 offset:34816\n"
    "ds_read_b64_tr_b16 a[238:239], v19 offset:35840\n"
    "v_mfma_f32_32x32x16_bf16 a[144:159], a[244:247], v[168:171], a[144:159]\n"
    "ds_read_b64_tr_b16 a[240:241], v18 offset:36864\n"
    "ds_read_b64_tr_b16 a[242:243], v18 offset:37888\n"
    "v_mfma_f32_32x32x16_bf16 v[240:255], a[248:251], v[172:175], v[240:255]\n"
    "ds_read_b64_tr_b16 a[244:245], v19 offset:36864\n"
    "ds_read_b64_tr_b16 a[246:247], v19 offset:37888\n"
    "v_mfma_f32_32x32x16_bf16 a[144:159], a[252:255], v[172:175], a[144:159]\n"
    "s_waitcnt lgkmcnt(4)\n"
    "ds_read_b64_tr_b16 a[248:249], v18 offset:38912\n"
    "ds_read_b64_tr_b16 a[250:251], v18 offset:39936\n"
    "buffer_load_dwordx4 %[w2b], %[srd2], s40 offen offset:1024 lds\n"
    "v_mfma_f32_32x32x16_bf16 a[160:175], a[224:227], v[160:163], a[160:175]\n"
    "ds_read_b64_tr_b16 a[252:253], v19 offset:38912\n"
    "ds_read_b64_tr_b16 a[254:255], v19 offset:39936\n"
    "v_mfma_f32_32x32x16_bf16 a[176:191], a[228:231], v[160:163], a[176:191]\n"
    "ds_read_b64_tr_b16 a[224:225], v18 offset:40960\n"
    "ds_read_b64_tr_b16 a[226:227], v18 offset:41984\n"
    "v_mfma_f32_32x32x16_bf16 a[160:175], a[232:235], v[164:167], a[160:175]\n"
    "ds_read_b64_tr_b16 a[228:229], v19 offset:40960\n"
    "ds_read_b64_tr_b16 a[230:231], v19 offset:41984\n"
    "s_add_u32 m0, %[ldsw], 90112\n"
    "s_mov_b32 s40, 148224\n"
    "v_mfma_f32_32x32x16_bf16 a[176:191], a[236:239], v[164:167], a[176:191]\n"
    "s_waitcnt lgkmcnt(4)\n"
    "ds_read_b64_tr_b16 a[232:233], v18 offset:43008\n"
    "ds_read_b64_tr_b16 a[234:235], v18 offset:44032\n"
    "buffer_load_dwordx4 %[w2a], %[srd2], s40 offen lds\n"
    "v_mfma_f32_32x32x16_bf16 a[160:175], a[240:243], v[168:171], a[160:175]\n"
    "ds_read_b64_tr_b16 a[236:237], v19 offset:43008\n"
    "ds_read_b64_tr_b16 a[238:239], v19 offset:44032\n"
    "v_mfma_f32_32x32x16_bf16 a[176:191], a[244:247], v[168:171], a[176:191]\n"
    "ds_read_b64_tr_b16 a[240:241], v18 offset:45056\n"
    "ds_read_b64_tr_b16 a[242:243], v18 offset:46080\n"
    "v_mfma_f32_32x32x16_bf16 a[160:175], a[248:251], v[172:175], a[160:175]\n"
    "ds_read_b64_tr_b16 a[244:245], v19 offset:45056\n"
    "ds_read_b64_tr_b16 a[246:247], v19 offset:46080\n"
    "v_mfma_f32_32x32x16_bf16 a[176:191], a[252:255], v[172:175], a[176:191]\n"
    "s_waitcnt lgkmcnt(4)\n"
    "ds_read_b64_tr_b16 a[248:249], v18 offset:47104\n"
    "ds_read_b64_tr_b16 a[250:251], v18 offset:48128\n"
    "buffer_load_dwordx4 %[w2b], %[srd2], s40 offen offset:1024 lds\n"
    "v_mfma_f32_32x32x16_bf16 a[192:207], a[224:227], v[160:163], a[192:207]\n"
    "ds_read_b64_tr_b16 a[252:253], v19 offset:47104\n"
    "ds_read_b64_tr_b16 a[254:255], v19 offset:48128\n"
    "v_mfma_f32_32x32x16_bf16 a[208:223], a[228:231], v[160:163], a[208:223]\n"
    "s_waitcnt vmcnt(18)\n"
    "s_barrier\n"
    "ds_read_b64_tr_b16 a[224:225], %[rd0] offset:0\n"
    "ds_read_b64_tr_b16 a[226:227], %[rd0] offset:1024\n"
    "v_mfma_f32_32x32x16_bf16 a[192:207], a[232:235], v[164:167], a[192:207]\n"
    "ds_read_b64_tr_b16 a[228:229], %[rd1] offset:0\n"
    "ds_read_b64_tr_b16 a[230:231], %[rd1] offset:1024\n"
    "s_add_u32 m0, %[ldsw], 98304\n"
    "s_mov_b32 s40, 221952\n"
    "v_mfma_f32_32x32x16_bf16 a[208:223], a[236:239], v[164:167], a[208:223]\n"
    "s_waitcnt lgkmcnt(4)\n"
    "ds_read_b64_tr_b16 a[232:233], %[rd0] offset:2048\n"
    "ds_read_b64_tr_b16 a[234:235], %[rd0] offset:3072\n"
    "buffer_load_dwordx4 %[w2a], %[srd2], s40 offen lds\n"
    "v_mfma_f32_32x32x16_bf16 a[192:207], a[240:243], v[168:171], a[192:207]\n"
    "ds_read_b64_tr_b16 a[236:237], %[rd1] offset:2048\n"
    "ds_read_b64_tr_b16 a[238:239], %[rd1] offset:3072\n"
    "v_mfma_f32_32x32x16_bf16 a[208:223], a[244:247], v[168:171], a[208:223]\n"
    "ds_read_b64_tr_b16 a[240:241], %[rd0] offset:4096\n"
    "ds_read_b64_tr_b16 a[242:243], %[rd0] offset:5120\n"
    "v_mfma_f32_32x32x16_bf16 a[192:207], a[248:251], v[172:175], a[192:207]\n"
    "ds_read_b64_tr_b16 a[244:245], %[rd1] offset:4096\n"
    "ds_read_b64_tr_b16 a[246:247], %[rd1] offset:5120\n"
    "v_mfma_f32_32x32x16_bf16 a[208:223], a[252:255], v[172:175], a[208:223]\n"
    "s_waitcnt lgkmcnt(4)\n"
    "ds_read_b64_tr_b16 a[248:249], %[rd0] offset:6144\n"
    "ds_read_b64_tr_b16 a[250:251], %[rd0] offset:7168\n"
    "buffer_load_dwordx4 %[w2b], %[srd2], s40 offen offset:1024 lds\n"
    "v_mfma_f32_32x32x16_bf16 v[176:191], a[224:227], v[32:35], 0\n"
    "ds_read_b64_tr_b16 a[252:253], %[rd1] offset:6144\n"
    "ds_read_b64_tr_b16 a[254:255], %[rd1] offset:7168\n"
    "v_mfma_f32_32x32x16_bf16 v[192:207], a[228:231], v[32:35], 0\n"
    "ds_read_b64_tr_b16 a[224:225], %[rd0] offset:8192\n"
    "ds_read_b64_tr_b16 a[226:227], %[rd0] offset:9216\n"
    "v_mfma_f32_32x32x16_bf16 v[176:191], a[232:235], v[36:39], v[176:191]\n"
    "ds_read_b64_tr_b16 a[228:229], %[rd1] offset:8192\n"
    "ds_read_b64_tr_b16 a[230:231], %[rd1] offset:9216\n"
    "s_add_u32 m0, %[ldsw], 106496\n"
    "s_mov_b32 s40, 295680\n"
    "v_mfma_f32_32x32x16_bf16 v[192:207], a[236:239], v[36:39], v[192:207]\n"
    "s_waitcnt lgkmcnt(4)\n"
    "ds_read_b64_tr_b16 a[232:233], %[rd0] offset:10240\n"
    "ds_read_b64_tr_b16 a[234:235], %[rd0] offset:11264\n"
    "buffer_load_dwordx4 %[w2a], %[srd2], s40 offen lds\n"
    "s_mov_b32 s41, 16384\n"
    "v_accvgpr_read_b32 v20, a0\n"
    "v_mfma_f32_32x32x16_bf16 v[176:191], a[240:243], v[40:43], v[176:191]\n"
    "ds_read_b64_tr_b16 a[236:237], %[rd1] offset:10240\n"
    "ds_read_b64_tr_b16 a[238:239], %[rd1] offset:11264\n"
    "v_accvgpr_read_b32 v21, a1\n"
    "v_cvt_pk_bf16_f32 v24, v20, v21\n"
    "v_mfma_f32_32x32x16_bf16 v[192:207], a[244:247], v[40:43], v[192:207]\n"
    "ds_read_b64_tr_b16 a[240:241], %[rd0] offset:12288\n"
    "ds_read_b64_tr_b16 a[242:243], %[rd0] offset:13312\n"
    "v_accvgpr_read_b32 v22, a2\n"
    "v_accvgpr_read_b32 v23, a3\n"
    "v_mfma_f32_32x32x16_bf16 v[176:191], a[248:251], v[44:47], v[176:191]\n"
    "ds_read_b64_tr_b16 a[244:245], %[rd1] offset:12288\n"
    "ds_read_b64_tr_b16 a[246:247], %[rd1] offset:13312\n"
    "v_cvt_pk_bf16_f32 v25, v22, v23\n"
    "v_mfma_f32_32x32x16_bf16 v[192:207], a[252:255], v[44:47], v[192:207]\n"
    "s_waitcnt lgkmcnt(4)\n"
    "ds_read_b64_tr_b16 a[248:249], %[rd0] offset:14336\n"
    "ds_read_b64_tr_b16 a[250:251], %[rd0] offset:15360\n"
    "buffer_load_dwordx4 %[w2b], %[srd2], s40 offen offset:1024 lds\n"
    "v_accvgpr_read_b32 v20, a4\n"
    "v_accvgpr_read_b32 v21, a5\n"
    "v_mfma_f32_32x32x16_bf16 v[176:191], a[224:227], v[48:51], v[176:191]\n"
    "ds_read_b64_tr_b16 a[252:253], %[rd1] offset:14336\n"
    "ds_read_b64_tr_b16 a[254:255], %[rd1] offset:15360\n"
    "v_cvt_pk_bf16_f32 v26, v20, v21\n"
    "v_accvgpr_read_b32 v22, a6\n"
    "v_mfma_f32_32x32x16_bf16 v[192:207], a[228:231], v[48:51], v[192:207]\n"
    "ds_read_b64_tr_b16 a[224:225], %[rd0] offset:16384\n"
    "ds_read_b64_tr_b16 a[226:227], %[rd0] offset:17408\n"
    "v_accvgpr_read_b32 v23, a7\n"
    "v_cvt_pk_bf16_f32 v27, v22, v23\n"
    "v_mfma_f32_32x32x16_bf16 v[176:191], a[232:235], v[52:55], v[176:191]\n"
    "ds_read_b64_tr_b16 a[228:229], %[rd1] offset:16384\n"
    "ds_read_b64_tr_b16 a[230:231], %[rd1] offset:17408\n"
    "s_add_u32 m0, %[ldsw], 114688\n"
    "s_mov_b32 s40, 369408\n"
    "buffer_store_dwordx4 v[24:27], %[vl], %[srdp], s41 offen offset:0\n"
    "v_mfma_f32_32x32x16_bf16 v[192:207], a[236:239], v[52:55], v[192:207]\n"
    "s_waitcnt lgkmcnt(4)\n"
    "ds_read_b64_tr_b16 a[232:233], %[rd0] offset:18432\n"
    "ds_read_b64_tr_b16 a[234:235], %[rd0] offset:19456\n"
    "buffer_load_dwordx4 %[w2a], %[srd2], s40 offen lds\n"
    "v_accvgpr_read_b32 v20, a8\n"
    "v_accvgpr_read_b32 v21, a9\n"
    "v_mfma_f32_32x32x16_bf16 v[176:191], a[240:243], v[56:59], v[176:191]\n"
    "ds_read_b64_tr_b16 a[236:237], %[rd1] offset:18432\n"
    "ds_read_b64_tr_b16 a[238:239], %[rd1] offset:19456\n"
    "v_cvt_pk_bf16_f32 v28, v20, v21\n"
    "v_accvgpr_read_b32 v22, a10\n"
    "v_mfma_f32_32x32x16_bf16 v[192:207], a[244:247], v[56:59], v[192:207]\n"
    "ds_read_b64_tr_b16 a[240:241], %[rd0] offset:20480\n"
    "ds_read_b64_tr_b16 a[242:243], %[rd0] offset:21504\n"
    "v_accvgpr_read_b32 v23, a11\n"
    "v_mfma_f32_32x32x16_bf16 v[176:191], a[248:251], v[60:63], v[176:191]\n"
    "ds_read_b64_tr_b16 a[244:245], %[rd1] offset:20480\n"
    "ds_read_b64_tr_b16 a[246:247], %[rd1] offset:21504\n"
    "v_cvt_pk_bf16_f32 v29, v22, v23\n"
    "v_accvgpr_read_b32 v20, a12\n"
    "v_mfma_f32_32x32x16_bf16 v[192:207], a[252:255], v[60:63], v[192:207]\n"
    "s_waitcnt lgkmcnt(4)\n"
    "ds_read_b64_tr_b16 a[248:249], %[rd0] offset:22528\n"
    "ds_read_b64_tr_b16 a[250:251], %[rd0] offset:23552\n"
    "buffer_load_dwordx4 %[w2b], %[srd2], s40 offen offset:1024 lds\n"
    "v_accvgpr_read_b32 v21, a13\n"
    "v_cvt_pk_bf16_f32 v30, v20, v21\n"
    "v_mfma_f32_32x32x16_bf16 v[176:191], a[224:227], v[64:67], v[176:191]\n"
    "ds_read_b64_tr_b16 a[252:253], %[rd1] offset:22528\n"
    "ds_read_b64_tr_b16 a[254:255], %[rd1] offset:23552\n"
    "v_accvgpr_read_b32 v22, a14\n"
    "v_accvgpr_read_b32 v23, a15\n"
    "v_mfma_f32_32x32x16_bf16 v[192:207], a[228:231], v[64:67], v[192:207]\n"
    "s_waitcnt vmcnt(19)\n"
    "s_barrier\n"
    "ds_read_b64_tr_b16 a[224:225], %[rd0] offset:24576\n"
    "ds_read_b64_tr_b16 a[226:227], %[rd0] offset:25600\n"
    "v_mfma_f32_32x32x16_bf16 v[176:191], a[232:235], v[68:71], v[176:191]\n"
    "ds_read_b64_tr_b16 a[228:229], %[rd1] offset:24576\n"
    "ds_read_b64_tr_b16 a[230:231], %[rd1] offset:25600\n"
    "s_add_u32 m0, %[ldsw], 122880\n"
    "s_mov_b32 s40, 443136\n"
    "v_cvt_pk_bf16_f32 v31, v22, v23\n"
    "v_mfma_f32_32x32x16_bf16 v[192:207], a[236:239], v[68:71], v[192:207]\n"
    "s_waitcnt lgkmcnt(4)\n"
    "ds_read_b64_tr_b16 a[232:233], %[rd0] offset:26624\n"
    "ds_read_b64_tr_b16 a[234:235], %[rd0] offset:27648\n"
    "buffer_load_dwordx4 %[w2a], %[srd2], s40 offen lds\n"
    "buffer_store_dwordx4 v[28:31], %[vl], %[srdp], s41 offen offset:1024\n"
    "s_mov_b32 s41, 18432\n"
    "v_mfma_f32_32x32x16_bf16 v[176:191], a[240:243], v[72:75], v[176:191]\n"
    "ds_read_b64_tr_b16 a[236:237], %[rd1] offset:26624\n"
    "ds_read_b64_tr_b16 a[238:239], %[rd1] offset:27648\n"
    "v_accvgpr_read_b32 v20, a16\n"
    "v_accvgpr_read_b32 v21, a17\n"
    "v_mfma_f32_32x32x16_bf16 v[192:207], a[244:247], v[72:75], v[192:207]\n"
    "ds_read_b64_tr_b16 a[240:241], %[rd0] offset:28672\n"
    "ds_read_b64_tr_b16 a[242:243], %[rd0] offset:29696\n"
    "v_cvt_pk_bf16_f32 v24, v20, v21\n"
    "v_accvgpr_read_b32 v22, a18\n"
    "v_mfma_f32_32x32x16_bf16 v[176:191], a[248:251], v[76:79], v[176:191]\n"
    "ds_read_b64_tr_b16 a[244:245], %[rd1] offset:28672\n"
    "ds_read_b64_tr_b16 a[246:247], %[rd1] offset:29696\n"
    "v_accvgpr_read_b32 v23, a19\n"
    "v_mfma_f32_32x32x16_bf16 v[192:207], a[252:255], v[76:79], v[192:207]\n"
    "s_waitcnt lgkmcnt(4)\n"
    "ds_read_b64_tr_b16 a[248:249], %[rd0] offset:30720\n"
    "ds_read_b64_tr_b16 a[250:251], %[rd0] offset:31744\n"
    "buffer_load_dwordx4 %[w2b], %[srd2], s40 offen offset:1024 lds\n"
    "v_cvt_pk_bf16_f32 v25, v22, v23\n"
    "v_accvgpr_read_b32 v20, a20\n"
    "v_mfma_f32_32x32x16_bf16 v[176:191], a[224:227], v[80:83], v[176:191]\n"
    "ds_read_b64_tr_b16 a[252:253], %[rd1] offset:30720\n"
    "ds_read_b64_tr_b16 a[254:255], %[rd1] offset:31744\n"
    "v_accvgpr_read_b32 v21, a21\n"
    "v_cvt_pk_bf16_f32 v26, v20, v21\n"
    "v_mfma_f32_32x32x16_bf16 v[192:207], a[228:231], v[80:83], v[192:207]\n"
    "ds_read_b64_tr_b16 a[224:225], %[rd0] offset:32768\n"
    "ds_read_b64_tr_b16 a[226:227], %[rd0] offset:33792\n"
    "v_accvgpr_read_b32 v22, a22\n"
    "v_mfma_f32_32x32x16_bf16 v[176:191], a[232:235], v[84:87], v[176:191]\n"
    "ds_read_b64_tr_b16 a[228:229], %[rd1] offset:32768\n"
    "ds_read_b64_tr_b16 a[230:231], %[rd1] offset:33792\n"
    "s_add_u32 m0, %[ldsw], 131072\n"
    "s_mov_b32 s40, 516864\n"
    "v_accvgpr_read_b32 v23, a23\n"
    "v_cvt_pk_bf16_f32 v27, v22, v23\n"
    "v_mfma_f32_32x32x16_bf16 v[192:207], a[236:239], v[84:87], v[192:207]\n"
    "s_waitcnt lgkmcnt(4)\n"
    "ds_read_b64_tr_b16 a[232:233], %[rd0] offset:34816\n"
    "ds_read_b64_tr_b16 a[234:235], %[rd0] offset:35840\n"
    "buffer_load_dwordx4 %[w2a], %[srd2], s40 offen lds\n"
    "buffer_store_dwordx4 v[24:27], %[vl], %[srdp], s41 offen offset:0\n"
    "v_accvgpr_read_b32 v20, a24\n"
    "v_mfma_f32_32x32x16_bf16 v[176:191], a[240:243], v[88:91], v[176:191]\n"
    "ds_read_b64_tr_b16 a[236:237], %[rd1] offset:34816\n"
    "ds_read_b64_tr_b16 a[238:239], %[rd1] offset:35840\n"
    "v_accvgpr_read_b32 v21, a25\n"
    "v_cvt_pk_bf16_f32 v28, v20, v21\n"
    "v_mfma_f32_32x32x16_bf16 v[192:207], a[244:247], v[88:91], v[192:207]\n"
    "ds_read_b64_tr_b16 a[240:241], %[rd0] offset:36864\n"
    "ds_read_b64_tr_b16 a[242:243], %[rd0] offset:37888\n"
    "v_accvgpr_read_b32 v22, a26\n"
    "v_mfma_f32_32x32x16_bf16 v[176:191], a[248:251], v[92:95], v[176:191]\n"
    "ds_read_b64_tr_b16 a[244:245], %[rd1] offset:36864\n"
    "ds_read_b64_tr_b16 a[246:247], %[rd1] offset:37888\n"
    "v_accvgpr_read_b32 v23, a27\n"
    "v_cvt_pk_bf16_f32 v29, v22, v23\n"
    "v_mfma_f32_32x32x16_bf16 v[192:207], a[252:255], v[92:95], v[192:207]\n"
    "s_waitcnt lgkmcnt(4)\n"
    "ds_read_b64_tr_b16 a[248:249], %[rd0] offset:38912\n"
    "ds_read_b64_tr_b16 a[250:251], %[rd0] offset:39936\n"
    "buffer_load_dwordx4 %[w2b], %[srd2], s40 offen offset:1024 lds\n"
    "v_accvgpr_read_b32 v20, a28\n"
    "v_accvgpr_read_b32 v21, a29\n"
    "v_mfma_f32_32x32x16_bf16 v[176:191], a[224:227], v[96:99], v[176:191]\n"
    "ds_read_b64_tr_b16 a[252:253], %[rd1] offset:38912\n"
    "ds_read_b64_tr_b16 a[254:255], %[rd1] offset:39936\n"
    "v_cvt_pk_bf16_f32 v30, v20, v21\n"
    "v_accvgpr_read_b32 v22, a30\n"
    "v_mfma_f32_32x32x16_bf16 v[192:207], a[228:231], v[96:99], v[192:207]\n"
    "ds_read_b64_tr_b16 a[224:225], %[rd0] offset:40960\n"
    "ds_read_b64_tr_b16 a[226:227], %[rd0] offset:41984\n"
    "v_accvgpr_read_b32 v23, a31\n"
    "v_mfma_f32_32x32x16_bf16 v[176:191], a[232:235], v[100:103], v[176:191]\n"
    "ds_read_b64_tr_b16 a[228:229], %[rd1] offset:40960\n"
    "ds_read_b64_tr_b16 a[230:231], %[rd1] offset:41984\n"
    "s_add_u32 m0, %[ldsw], 139264\n"
    "s_mov_b32 s40, 590592\n"
    "v_cvt_pk_bf16_f32 v31, v22, v23\n"
    "buffer_store_dwordx4 v[28:31], %[vl], %[srdp], s41 offen offset:1024\n"
    "v_mfma_f32_32x32x16_bf16 v[192:207], a[236:239], v[100:103], v[192:207]\n"
    "s_waitcnt lgkmcnt(4)\n"
    "ds_read_b64_tr_b16 a[232:233], %[rd0] offset:43008\n"
    "ds_read_b64_tr_b16 a[234:235], %[rd0] offset:44032\n"
    "buffer_load_dwordx4 %[w2a], %[srd2], s40 offen lds\n"
    "s_mov_b32 s41, 0\n"
    "v_cvt_pk_bf16_f32 v24, v208, v209\n"
    "v_mfma_f32_32x32x16_bf16 v[176:191], a[240:243], v[104:107], v[176:191]\n"
    "ds_read_b64_tr_b16 a[236:237], %[rd1] offset:43008\n"
    "ds_read_b64_tr_b16 a[238:239], %[rd1] offset:44032\n"
    "v_cvt_pk_bf16_f32 v25, v210, v211\n"
    "v_mfma_f32_32x32x16_bf16 v[192:207], a[244:247], v[104:107], v[192:207]\n"
    "ds_read_b64_tr_b16 a[240:241], %[rd0] offset:45056\n"
    "ds_read_b64_tr_b16 a[242:243], %[rd0] offset:46080\n"
    "v_cvt_pk_bf16_f32 v26, v212, v213\n"
    "v_cvt_pk_bf16_f32 v27, v214, v215\n"
    "v_mfma_f32_32x32x16_bf16 v[176:191], a[248:251], v[108:111], v[176:191]\n"
    "ds_read_b64_tr_b16 a[244:245], %[rd1] offset:45056\n"
    "ds_read_b64_tr_b16 a[246:247], %[rd1] offset:46080\n"
    "buffer_store_dwordx4 v[24:27], %[vl], %[srdp], s41 offen offset:0\n"
    "v_cvt_pk_bf16_f32 v28, v216, v217\n"
    "v_mfma_f32_32x32x16_bf16 v[192:207], a[252:255], v[108:111], v[192:207]\n"
    "s_waitcnt lgkmcnt(4)\n"
    "ds_read_b64_tr_b16 a[248:249], %[rd0] offset:47104\n"
    "ds_read_b64_tr_b16 a[250:251], %[rd0] offset:48128\n"
    "buffer_load_dwordx4 %[w2b], %[srd2], s40 offen offset:1024 lds\n"
    "v_cvt_pk_bf16_f32 v29, v218, v219\n"
    "v_cvt_pk_bf16_f32 v30, v220, v221\n"
    "v_mfma_f32_32x32x16_bf16 v[176:191], a[224:227], v[112:115], v[176:191]\n"
    "ds_read_b64_tr_b16 a[252:253], %[rd1] offset:47104\n"
    "ds_read_b64_tr_b16 a[254:255], %[rd1] offset:48128\n"
    "v_cvt_pk_bf16_f32 v31, v222, v223\n"
    "v_mfma_f32_32x32x16_bf16 v[192:207], a[228:231], v[112:115], v[192:207]\n"
    "s_waitcnt vmcnt(23)\n"
    "s_barrier\n"
    "ds_read_b64_tr_b16 a[224:225], v16 offset:0\n"
    "ds_read_b64_tr_b16 a[226:227], v16 offset:1024\n"
    "v_mfma_f32_32x32x16_bf16 v[176:191], a[232:235], v[116:119], v[176:191]\n"
    "ds_read_b64_tr_b16 a[228:229], v17 offset:0\n"
    "ds_read_b64_tr_b16 a[230:231], v17 offset:1024\n"
    "s_add_u32 m0, %[ldsw], 0\n"
    "s_mov_b32 s40, 896\n"
    "buffer_store_dwordx4 v[28:31], %[vl], %[srdp], s41 offen offset:1024\n"
    "s_mov_b32 s41, 2048\n"
    "v_mfma_f32_32x32x16_bf16 v[192:207], a[236:239], v[116:119], v[192:207]\n"
    "s_waitcnt lgkmcnt(4)\n"
    "ds_read_b64_tr_b16 a[232:233], v16 offset:2048\n"
    "ds_read_b64_tr_b16 a[234:235], v16 offset:3072\n"
    "buffer_load_dwordx4 %[w2a], %[srd2], s40 offen lds\n"
    "v_cvt_pk_bf16_f32 v24, v224, v225\n"
    "v_cvt_pk_bf16_f32 v25, v226, v227\n"
    "v_mfma_f32_32x32x16_bf16 v[176:191], a[240:243], v[120:123], v[176:191]\n"
    "ds_read_b64_tr_b16 a[236:237], v17 offset:2048\n"
    "ds_read_b64_tr_b16 a[238:239], v17 offset:3072\n"
    "v_cvt_pk_bf16_f32 v26, v228, v229\n"
    "v_cvt_pk_bf16_f32 v27, v230, v231\n"
    "v_mfma_f32_32x32x16_bf16 v[192:207], a[244:247], v[120:123], v[192:207]\n"
    "ds_read_b64_tr_b16 a[240:241], v16 offset:4096\n"
    "ds_read_b64_tr_b16 a[242:243], v16 offset:5120\n"
    "buffer_store_dwordx4 v[24:27], %[vl], %[srdp], s41 offen offset:0\n"
    "v_mfma_f32_32x32x16_bf16 v[176:191], a[248:251], v[124:127], v[176:191]\n"
    "ds_read_b64_tr_b16 a[244:245], v17 offset:4096\n"
    "ds_read_b64_tr_b16 a[246:247], v17 offset:5120\n"
    "v_cvt_pk_bf16_f32 v28, v232, v233\n"
    "v_cvt_pk_bf16_f32 v29, v234, v235\n"
    "v_mfma_f32_32x32x16_bf16 v[192:207], a[252:255], v[124:127], v[192:207]\n"
    "s_waitcnt lgkmcnt(4)\n"
    "ds_read_b64_tr_b16 a[248:249], v16 offset:6144\n"
    "ds_read_b64_tr_b16 a[250:251], v16 offset:7168\n"
    "buffer_load_dwordx4 %[w2b], %[srd2], s40 offen offset:1024 lds\n"
    "v_cvt_pk_bf16_f32 v30, v236, v237\n"
    "v_cvt_pk_bf16_f32 v31, v238, v239\n"
    "v_mfma_f32_32x32x16_bf16 v[176:191], a[224:227], v[128:131], v[176:191]\n"
    "ds_read_b64_tr_b16 a[252:253], v17 offset:6144\n"
    "ds_read_b64_tr_b16 a[254:255], v17 offset:7168\n"
    "buffer_store_dwordx4 v[28:31], %[vl], %[srdp], s41 offen offset:1024\n"
    "v_mfma_f32_32x32x16_bf16 v[192:207], a[228:231], v[128:131], v[192:207]\n"
    "ds_read_b64_tr_b16 a[224:225], v16 offset:8192\n"
    "ds_read_b64_tr_b16 a[226:227], v16 offset:9216\n"
    "s_mov_b32 s41, 4096\n"
    "v_cvt_pk_bf16_f32 v24, v240, v241\n"
    "v_mfma_f32_32x32x16_bf16 v[176:191], a[232:235], v[132:135], v[176:191]\n"
    "ds_read_b64_tr_b16 a[228:229], v17 offset:8192\n"
    "ds_read_b64_tr_b16 a[230:231], v17 offset:9216\n"
    "s_add_u32 m0, %[ldsw], 8192\n"
    "s_mov_b32 s40, 74624\n"
    "v_cvt_pk_bf16_f32 v25, v242, v243\n"
    "v_cvt_pk_bf16_f32 v26, v244, v245\n"
    "v_mfma_f32_32x32x16_bf16 v[192:207], a[236:239], v[132:135], v[192:207]\n"
    "s_waitcnt lgkmcnt(4)\n"
    "ds_read_b64_tr_b16 a[232:233], v16 offset:10240\n"
    "ds_read_b64_tr_b16 a[234:235], v16 offset:11264\n"
    "buffer_load_dwordx4 %[w2a], %[srd2], s40 offen lds\n"
    "v_cvt_pk_bf16_f32 v27, v246, v247\n"
    "buffer_store_dwordx4 v[24:27], %[vl], %[srdp], s41 offen offset:0\n"
    "v_mfma_f32_32x32x16_bf16 v[176:191], a[240:243], v[136:139], v[176:191]\n"
    "ds_read_b64_tr_b16 a[236:237], v17 offset:10240\n"
    "ds_read_b64_tr_b16 a[238:239], v17 offset:11264\n"
    "v_cvt_pk_bf16_f32 v28, v248, v249\n"
    "v_mfma_f32_32x32x16_bf16 v[192:207], a[244:247], v[136:139], v[192:207]\n"
    "ds_read_b64_tr_b16 a[240:241], v16 offset:12288\n"
    "ds_read_b64_tr_b16 a[242:243], v16 offset:13312\n"
    "v_cvt_pk_bf16_f32 v29, v250, v251\n"
    "v_cvt_pk_bf16_f32 v30, v252, v253\n"
    "v_mfma_f32_32x32x16_bf16 v[176:191], a[248:251], v[140:143], v[176:191]\n"
    "ds_read_b64_tr_b16 a[244:245], v17 offset:12288\n"
    "ds_read_b64_tr_b16 a[246:247], v17 offset:13312\n"
    "v_cvt_pk_bf16_f32 v31, v254, v255\n"
    "buffer_store_dwordx4 v[28:31], %[vl], %[srdp], s41 offen offset:1024\n"
    "v_mfma_f32_32x32x16_bf16 v[192:207], a[252:255], v[140:143], v[192:207]\n"
    "s_waitcnt lgkmcnt(4)\n"
    "ds_read_b64_tr_b16 a[248:249], v16 offset:14336\n"
    "ds_read_b64_tr_b16 a[250:251], v16 offset:15360\n"
    "buffer_load_dwordx4 %[w2b], %[srd2], s40 offen offset:1024 lds\n"
    "s_mov_b32 s41, 6144\n"
    "v_accvgpr_read_b32 v20, a144\n"
    "v_mfma_f32_32x32x16_bf16 v[176:191], a[224:227], v[144:147], v[176:191]\n"
    "ds_read_b64_tr_b16 a[252:253], v17 offset:14336\n"
    "ds_read_b64_tr_b16 a[254:255], v17 offset:15360\n"
    "v_accvgpr_read_b32 v21, a145\n"
    "v_mfma_f32_32x32x16_bf16 v[192:207], a[228:231], v[144:147], v[192:207]\n"
    "ds_read_b64_tr_b16 a[224:225], v16 offset:16384\n"
    "ds_read_b64_tr_b16 a[226:227], v16 offset:17408\n"
    "v_cvt_pk_bf16_f32 v24, v20, v21\n"
    "v_accvgpr_read_b32 v22, a146\n"
    "v_mfma_f32_32x32x16_bf16 v[176:191], a[232:235], v[148:151], v[176:191]\n"
    "ds_read_b64_tr_b16 a[228:229], v17 offset:16384\n"
    "ds_read_b64_tr_b16 a[230:231], v17 offset:17408\n"
    "s_add_u32 m0, %[ldsw], 16384\n"
    "s_mov_b32 s40, 148352\n"
    "v_accvgpr_read_b32 v23, a147\n"
    "v_cvt_pk_bf16_f32 v25, v22, v23\n"
    "v_mfma_f32_32x32x16_bf16 v[192:207], a[236:239], v[148:151], v[192:207]\n"
    "s_waitcnt lgkmcnt(4)\n"
    "ds_read_b64_tr_b16 a[232:233], v16 offset:18432\n"
    "ds_read_b64_tr_b16 a[234:235], v16 offset:19456\n"
    "buffer_load_dwordx4 %[w2a], %[srd2], s40 offen lds\n"
    "v_accvgpr_read_b32 v20, a148\n"
    "v_mfma_f32_32x32x16_bf16 v[176:191], a[240:243], v[152:155], v[176:191]\n"
    "ds_read_b64_tr_b16 a[236:237], v17 offset:18432\n"
    "ds_read_b64_tr_b16 a[238:239], v17 offset:19456\n"
    "v_accvgpr_read_b32 v21, a149\n"
    "v_cvt_pk_bf16_f32 v26, v20, v21\n"
    "v_mfma_f32_32x32x16_bf16 v[192:207], a[244:247], v[152:155], v[192:207]\n"
    "ds_read_b64_tr_b16 a[240:241], v16 offset:20480\n"
    "ds_read_b64_tr_b16 a[242:243], v16 offset:21504\n"
    "v_accvgpr_read_b32 v22, a150\n"
    "v_accvgpr_read_b32 v23, a151\n"
    "v_mfma_f32_32x32x16_bf16 v[176:191], a[248:251], v[156:159], v[176:191]\n"
    "ds_read_b64_tr_b16 a[244:245], v17 offset:20480\n"
    "ds_read_b64_tr_b16 a[246:247], v17 offset:21504\n"
    "v_cvt_pk_bf16_f32 v27, v22, v23\n"
    "buffer_store_dwordx4 v[24:27], %[vl], %[srdp], s41 offen offset:0\n"
    "v_mfma_f32_32x32x16_bf16 v[192:207], a[252:255], v[156:159], v[192:207]\n"
    "s_waitcnt lgkmcnt(4)\n"
    "ds_read_b64_tr_b16 a[248:249], v16 offset:22528\n"
    "ds_read_b64_tr_b16 a[250:251], v16 offset:23552\n"
    "buffer_load_dwordx4 %[w2b], %[srd2], s40 offen offset:1024 lds\n"
    "v_accvgpr_read_b32 v20, a152\n"
    "v_mfma_f32_32x32x16_bf16 v[176:191], a[224:227], v[160:163], v[176:191]\n"
    "ds_read_b64_tr_b16 a[252:253], v17 offset:22528\n"
    "ds_read_b64_tr_b16 a[254:255], v17 offset:23552\n"
    "v_accvgpr_read_b32 v21, a153\n"
    "v_cvt_pk_bf16_f32 v28, v20, v21\n"
    "v_mfma_f32_32x32x16_bf16 v[192:207], a[228:231], v[160:163], v[192:207]\n"
    "s_waitcnt vmcnt(29)\n"
    "s_barrier\n"
    "ds_read_b64_tr_b16 a[224:225], v16 offset:24576\n"
    "ds_read_b64_tr_b16 a[226:227], v16 offset:25600\n"
    "v_mfma_f32_32x32x16_bf16 v[176:191], a[232:235], v[164:167], v[176:191]\n"
    "ds_read_b64_tr_b16 a[228:229], v17 offset:24576\n"
    "ds_read_b64_tr_b16 a[230:231], v17 offset:25600\n"
    "s_add_u32 m0, %[ldsw], 24576\n"
    "s_mov_b32 s40, 222080\n"
    "v_accvgpr_read_b32 v22, a154\n"
    "v_accvgpr_read_b32 v23, a155\n"
    "v_mfma_f32_32x32x16_bf16 v[192:207], a[236:239], v[164:167], v[192:207]\n"
    "s_waitcnt lgkmcnt(4)\n"
    "ds_read_b64_tr_b16 a[232:233], v16 offset:26624\n"
    "ds_read_b64_tr_b16 a[234:235], v16 offset:27648\n"
    "buffer_load_dwordx4 %[w2a], %[srd2], s40 offen lds\n"
    "v_cvt_pk_bf16_f32 v29, v22, v23\n"
    "v_accvgpr_read_b32 v20, a156\n"
    "v_mfma_f32_32x32x16_bf16 v[176:191], a[240:243], v[168:171], v[176:191]\n"
    "ds_read_b64_tr_b16 a[236:237], v17 offset:26624\n"
    "ds_read_b64_tr_b16 a[238:239], v17 offset:27648\n"
    "v_accvgpr_read_b32 v21, a157\n"
    "v_mfma_f32_32x32x16_bf16 v[192:207], a[244:247], v[168:171], v[192:207]\n"
    "ds_read_b64_tr_b16 a[240:241], v16 offset:28672\n"
    "ds_read_b64_tr_b16 a[242:243], v16 offset:29696\n"
    "v_cvt_pk_bf16_f32 v30, v20, v21\n"
    "v_accvgpr_read_b32 v22, a158\n"
    "v_mfma_f32_32x32x16_bf16 v[176:191], a[248:251], v[172:175], v[176:191]\n"
    "ds_read_b64_tr_b16 a[244:245], v17 offset:28672\n"
    "ds_read_b64_tr_b16 a[246:247], v17 offset:29696\n"
    "v_accvgpr_read_b32 v23, a159\n"
    "v_cvt_pk_bf16_f32 v31, v22, v23\n"
    "v_mfma_f32_32x32x16_bf16 v[192:207], a[252:255], v[172:175], v[192:207]\n"
    "s_waitcnt lgkmcnt(4)\n"
    "ds_read_b64_tr_b16 a[248:249], v16 offset:30720\n"
    "ds_read_b64_tr_b16 a[250:251], v16 offset:31744\n"
    "buffer_load_dwordx4 %[w2b], %[srd2], s40 offen offset:1024 lds\n"
    "buffer_store_dwordx4 v[28:31], %[vl], %[srdp], s41 offen offset:1024\n"
    "v_mfma_f32_32x32x16_bf16 a[32:47], a[224:227], v[32:35], 0\n"
    "ds_read_b64_tr_b16 a[252:253], v17 offset:30720\n"
    "ds_read_b64_tr_b16 a[254:255], v17 offset:31744\n"
    "v_mfma_f32_32x32x16_bf16 a[48:63], a[228:231], v[32:35], 0\n"
    "ds_read_b64_tr_b16 a[224:225], v16 offset:32768\n"
    "ds_read_b64_tr_b16 a[226:227], v16 offset:33792\n"
    "v_mfma_f32_32x32x16_bf16 a[32:47], a[232:235], v[36:39], a[32:47]\n"
    "ds_read_b64_tr_b16 a[228:229], v17 offset:32768\n"
    "ds_read_b64_tr_b16 a[230:231], v17 offset:33792\n"
    "s_add_u32 m0, %[ldsw], 32768\n"
    "s_mov_b32 s40, 295808\n"
    "v_mfma_f32_32x32x16_bf16 a[48:63], a[236:239], v[36:39], a[48:63]\n"
    "s_waitcnt lgkmcnt(4)\n"
    "ds_read_b64_tr_b16 a[232:233], v16 offset:34816\n"
    "ds_read_b64_tr_b16 a[234:235], v16 offset:35840\n"
    "buffer_load_dwordx4 %[w2a], %[srd2], s40 offen lds\n"
    "s_mov_b32 s41, 20480\n"
    "v_cvt_pk_bf16_f32 v24, v176, v177\n"
    "v_mfma_f32_32x32x16_bf16 a[32:47], a[240:243], v[40:43], a[32:47]\n"
    "ds_read_b64_tr_b16 a[236:237], v17 offset:34816\n"
    "ds_read_b64_tr_b16 a[238:239], v17 offset:35840\n"
    "v_cvt_pk_bf16_f32 v25, v178, v179\n"
    "v_cvt_pk_bf16_f32 v26, v180, v181\n"
    "v_mfma_f32_32x32x16_bf16 a[48:63], a[244:247], v[40:43], a[48:63]\n"
    "ds_read_b64_tr_b16 a[240:241], v16 offset:36864\n"
    "ds_read_b64_tr_b16 a[242:243], v16 offset:37888\n"
    "v_cvt_pk_bf16_f32 v27, v182, v183\n"
    "v_mfma_f32_32x32x16_bf16 a[32:47], a[248:251], v[44:47], a[32:47]\n"
    "ds_read_b64_tr_b16 a[244:245], v17 offset:36864\n"
    "ds_read_b64_tr_b16 a[246:247], v17 offset:37888\n"
    "buffer_store_dwordx4 v[24:27], %[vl], %[srdp], s41 offen offset:0\n"
    "v_cvt_pk_bf16_f32 v28, v184, v185\n"
    "v_mfma_f32_32x32x16_bf16 a[48:63], a[252:255], v[44:47], a[48:63]\n"
    "s_waitcnt lgkmcnt(4)\n"
    "ds_read_b64_tr_b16 a[248:249], v16 offset:38912\n"
    "ds_read_b64_tr_b16 a[250:251], v16 offset:39936\n"
    "buffer_load_dwordx4 %[w2b], %[srd2], s40 offen offset:1024 lds\n"
    "v_cvt_pk_bf16_f32 v29, v186, v187\n"
    "v_mfma_f32_32x32x16_bf16 a[32:47], a[224:227], v[48:51], a[32:47]\n"
    "ds_read_b64_tr_b16 a[252:253], v17 offset:38912\n"
    "ds_read_b64_tr_b16 a[254:255], v17 offset:39936\n"
    "v_cvt_pk_bf16_f32 v30, v188, v189\n"
    "v_cvt_pk_bf16_f32 v31, v190, v191\n"
    "v_mfma_f32_32x32x16_bf16 a[48:63], a[228:231], v[48:51], a[48:63]\n"
    "ds_read_b64_tr_b16 a[224:225], v16 offset:40960\n"
    "ds_read_b64_tr_b16 a[226:227], v16 offset:41984\n"
    "buffer_store_dwordx4 v[28:31], %[vl], %[srdp], s41 offen offset:1024\n"
    "v_mfma_f32_32x32x16_bf16 a[32:47], a[232:235], v[52:55], a[32:47]\n"
    "ds_read_b64_tr_b16 a[228:229], v17 offset:40960\n"
    "ds_read_b64_tr_b16 a[230:231], v17 offset:41984\n"
    "s_add_u32 m0, %[ldsw], 40960\n"
    "s_mov_b32 s40, 369536\n"
    "s_mov_b32 s41, 22528\n"
    "v_cvt_pk_bf16_f32 v24, v192, v193\n"
    "v_mfma_f32_32x32x16_bf16 a[48:63], a[236:239], v[52:55], a[48:63]\n"
    "s_waitcnt lgkmcnt(4)\n"
    "ds_read_b64_tr_b16 a[232:233], v16 offset:43008\n"
    "ds_read_b64_tr_b16 a[234:235], v16 offset:44032\n"
    "buffer_load_dwordx4 %[w2a], %[srd2], s40 offen lds\n"
    "v_cvt_pk_bf16_f32 v25, v194, v195\n"
    "v_cvt_pk_bf16_f32 v26, v196, v197\n"
    "v_mfma_f32_32x32x16_bf16 a[32:47], a[240:243], v[56:59], a[32:47]\n"
    "ds_read_b64_tr_b16 a[236:237], v17 offset:43008\n"
    "ds_read_b64_tr_b16 a[238:239], v17 offset:44032\n"
    "v_cvt_pk_bf16_f32 v27, v198, v199\n"
    "v_mfma_f32_32x32x16_bf16 a[48:63], a[244:247], v[56:59], a[48:63]\n"
    "ds_read_b64_tr_b16 a[240:241], v16 offset:45056\n"
    "ds_read_b64_tr_b16 a[242:243], v16 offset:46080\n"
    "buffer_store_dwordx4 v[24:27], %[vl], %[srdp], s41 offen offset:0\n"
    "v_cvt_pk_bf16_f32 v28, v200, v201\n"
    "v_mfma_f32_32x32x16_bf16 a[32:47], a[248:251], v[60:63], a[32:47]\n"
    "ds_read_b64_tr_b16 a[244:245], v17 offset:45056\n"
    "ds_read_b64_tr_b16 a[246:247], v17 offset:46080\n"
    "v_cvt_pk_bf16_f32 v29, v202, v203\n"
    "v_mfma_f32_32x32x16_bf16 a[48:63], a[252:255], v[60:63], a[48:63]\n"
    "s_waitcnt lgkmcnt(4)\n"
    "ds_read_b64_tr_b16 a[248:249], v16 offset:47104\n"
    "ds_read_b64_tr_b16 a[250:251], v16 offset:48128\n"
    "buffer_load_dwordx4 %[w2b], %[srd2], s40 offen offset:1024 lds\n"
    "v_cvt_pk_bf16_f32 v30, v204, v205\n"
    "v_cvt_pk_bf16_f32 v31, v206, v207\n"
    "v_mfma_f32_32x32x16_bf16 a[32:47], a[224:227], v[64:67], a[32:47]\n"
    "ds_read_b64_tr_b16 a[252:253], v17 offset:47104\n"
    "ds_read_b64_tr_b16 a[254:255], v17 offset:48128\n"
    "buffer_store_dwordx4 v[28:31], %[vl], %[srdp], s41 offen offset:1024\n"
    "v_mfma_f32_32x32x16_bf16 a[48:63], a[228:231], v[64:67], a[48:63]\n"
    "s_waitcnt vmcnt(33)\n"
    "s_barrier\n"
    "ds_read_b64_tr_b16 a[224:225], v18 offset:0\n"
    "ds_read_b64_tr_b16 a[226:227], v18 offset:1024\n"
    "v_mfma_f32_32x32x16_bf16 a[32:47], a[232:235], v[68:71], a[32:47]\n"
    "ds_read_b64_tr_b16 a[228:229], v19 offset:0\n"
    "ds_read_b64_tr_b16 a[230:231], v19 offset:1024\n"
    "s_add_u32 m0, %[ldsw], 49152\n"
    "s_mov_b32 s40, 443264\n"
    "s_mov_b32 s41, 8192\n"
    "v_accvgpr_read_b32 v20, a160\n"
    "v_mfma_f32_32x32x16_bf16 a[48:63], a[236:239], v[68:71], a[48:63]\n"
    "s_waitcnt lgkmcnt(4)\n"
    "ds_read_b64_tr_b16 a[232:233], v18 offset:2048\n"
    "ds_read_b64_tr_b16 a[234:235], v18 offset:3072\n"
    "buffer_load_dwordx4 %[w2a], %[srd2], s40 offen lds\n"
    "v_accvgpr_read_b32 v21, a161\n"
    "v_mfma_f32_32x32x16_bf16 a[32:47], a[240:243], v[72:75], a[32:47]\n"
    "ds_read_b64_tr_b16 a[236:237], v19 offset:2048\n"
    "ds_read_b64_tr_b16 a[238:239], v19 offset:3072\n"
    "v_cvt_pk_bf16_f32 v24, v20, v21\n"
    "v_accvgpr_read_b32 v22, a162\n"
    "v_mfma_f32_32x32x16_bf16 a[48:63], a[244:247], v[72:75], a[48:63]\n"
    "ds_read_b64_tr_b16 a[240:241], v18 offset:4096\n"
    "ds_read_b64_tr_b16 a[242:243], v18 offset:5120\n"
    "v_accvgpr_read_b32 v23, a163\n"
    "v_cvt_pk_bf16_f32 v25, v22, v23\n"
    "v_mfma_f32_32x32x16_bf16 a[32:47], a[248:251], v[76:79], a[32:47]\n"
    "ds_read_b64_tr_b16 a[244:245], v19 offset:4096\n"
    "ds_read_b64_tr_b16 a[246:247], v19 offset:5120\n"
    "v_accvgpr_read_b32 v20, a164\n"
    "v_mfma_f32_32x32x16_bf16 a[48:63], a[252:255], v[76:79], a[48:63]\n"
    "s_waitcnt lgkmcnt(4)\n"
    "ds_read_b64_tr_b16 a[248:249], v18 offset:6144\n"
    "ds_read_b64_tr_b16 a[250:251], v18 offset:7168\n"
    "buffer_load_dwordx4 %[w2b], %[srd2], s40 offen offset:1024 lds\n"
    "v_accvgpr_read_b32 v21, a165\n"
    "v_cvt_pk_bf16_f32 v26, v20, v21\n"
    "v_mfma_f32_32x32x16_bf16 a[32:47], a[224:227], v[80:83], a[32:47]\n"
    "ds_read_b64_tr_b16 a[252:253], v19 offset:6144\n"
    "ds_read_b64_tr_b16 a[254:255], v19 offset:7168\n"
    "v_accvgpr_read_b32 v22, a166\n"
    "v_mfma_f32_32x32x16_bf16 a[48:63], a[228:231], v[80:83], a[48:63]\n"
    "ds_read_b64_tr_b16 a[224:225], v18 offset:8192\n"
    "ds_read_b64_tr_b16 a[226:227], v18 offset:9216\n"
    "v_accvgpr_read_b32 v23, a167\n"
    "v_cvt_pk_bf16_f32 v27, v22, v23\n"
    "v_mfma_f32_32x32x16_bf16 a[32:47], a[232:235], v[84:87], a[32:47]\n"
    "ds_read_b64_tr_b16 a[228:229], v19 offset:8192\n"
    "ds_read_b64_tr_b16 a[230:231], v19 offset:9216\n"
    "s_add_u32 m0, %[ldsw], 57344\n"
    "s_mov_b32 s40, 516992\n"
    "buffer_store_dwordx4 v[24:27], %[vl], %[srdp], s41 offen offset:0\n"
    "v_mfma_f32_32x32x16_bf16 a[48:63], a[236:239], v[84:87], a[48:63]\n"
    "s_waitcnt lgkmcnt(4)\n"
    "ds_read_b64_tr_b16 a[232:233], v18 offset:10240\n"
    "ds_read_b64_tr_b16 a[234:235], v18 offset:11264\n"
    "buffer_load_dwordx4 %[w2a], %[srd2], s40 offen lds\n"
    "v_accvgpr_read_b32 v20, a168\n"
    "v_accvgpr_read_b32 v21, a169\n"
    "v_mfma_f32_32x32x16_bf16 a[32:47], a[240:243], v[88:91], a[32:47]\n"
    "ds_read_b64_tr_b16 a[236:237], v19 offset:10240\n"
    "ds_read_b64_tr_b16 a[238:239], v19 offset:11264\n"
    "v_cvt_pk_bf16_f32 v28, v20, v21\n"
    "v_accvgpr_read_b32 v22, a170\n"
    "v_mfma_f32_32x32x16_bf16 a[48:63], a[244:247], v[88:91], a[48:63]\n"
    "ds_read_b64_tr_b16 a[240:241], v18 offset:12288\n"
    "ds_read_b64_tr_b16 a[242:243], v18 offset:13312\n"
    "v_accvgpr_read_b32 v23, a171\n"
    "v_mfma_f32_32x32x16_bf16 a[32:47], a[248:251], v[92:95], a[32:47]\n"
    "ds_read_b64_tr_b16 a[244:245], v19 offset:12288\n"
    "ds_read_b64_tr_b16 a[246:247], v19 offset:13312\n"
    "v_cvt_pk_bf16_f32 v29, v22, v23\n"
    "v_accvgpr_read_b32 v20, a172\n"
    "v_mfma_f32_32x32x16_bf16 a[48:63], a[252:255], v[92:95], a[48:63]\n"
    "s_waitcnt lgkmcnt(4)\n"
    "ds_read_b64_tr_b16 a[248:249], v18 offset:14336\n"
    "ds_read_b64_tr_b16 a[250:251], v18 offset:15360\n"
    "buffer_load_dwordx4 %[w2b], %[srd2], s40 offen offset:1024 lds\n"
    "v_accvgpr_read_b32 v21, a173\n"
    "v_mfma_f32_32x32x16_bf16 a[32:47], a[224:227], v[96:99], a[32:47]\n"
    "ds_read_b64_tr_b16 a[252:253], v19 offset:14336\n"
    "ds_read_b64_tr_b16 a[254:255], v19 offset:15360\n"
    "v_cvt_pk_bf16_f32 v30, v20, v21\n"
    "v_accvgpr_read_b32 v22, a174\n"
    "v_mfma_f32_32x32x16_bf16 a[48:63], a[228:231], v[96:99], a[48:63]\n"
    "ds_read_b64_tr_b16 a[224:225], v18 offset:16384\n"
    "ds_read_b64_tr_b16 a[226:227], v18 offset:17408\n"
    "v_accvgpr_read_b32 v23, a175\n"
    "v_mfma_f32_32x32x16_bf16 a[32:47], a[232:235], v[100:103], a[32:47]\n"
    "ds_read_b64_tr_b16 a[228:229], v19 offset:16384\n"
    "ds_read_b64_tr_b16 a[230:231], v19 offset:17408\n"
    "s_add_u32 m0, %[ldsw], 65536\n"
    "s_mov_b32 s40, 590720\n"
    "v_cvt_pk_bf16_f32 v31, v22, v23\n"
    "buffer_store_dwordx4 v[28:31], %[vl], %[srdp], s41 offen offset:1024\n"
    "v_mfma_f32_32x32x16_bf16 a[48:63], a[236:239], v[100:103], a[48:63]\n"
    "s_waitcnt lgkmcnt(4)\n"
    "ds_read_b64_tr_b16 a[232:233], v18 offset:18432\n"
    "ds_read_b64_tr_b16 a[234:235], v18 offset:19456\n"
    "buffer_load_dwordx4 %[w2a], %[srd2], s40 offen lds\n"
    "s_mov_b32 s41, 10240\n"
    "v_mfma_f32_32x32x16_bf16 a[32:47], a[240:243], v[104:107], a[32:47]\n"
    "ds_read_b64_tr_b16 a[236:237], v19 offset:18432\n"
    "ds_read_b64_tr_b16 a[238:239], v19 offset:19456\n"
    "v_accvgpr_read_b32 v20, a176\n"
    "v_accvgpr_read_b32 v21, a177\n"
    "v_mfma_f32_32x32x16_bf16 a[48:63], a[244:247], v[104:107], a[48:63]\n"
    "ds_read_b64_tr_b16 a[240:241], v18 offset:20480\n"
    "ds_read_b64_tr_b16 a[242:243], v18 offset:21504\n"
    "v_cvt_pk_bf16_f32 v24, v20, v21\n"
    "v_accvgpr_read_b32 v22, a178\n"
    "v_mfma_f32_32x32x16_bf16 a[32:47], a[248:251], v[108:111], a[32:47]\n"
    "ds_read_b64_tr_b16 a[244:245], v19 offset:20480\n"
    "ds_read_b64_tr_b16 a[246:247], v19 offset:21504\n"
    "v_accvgpr_read_b32 v23, a179\n"
    "v_mfma_f32_32x32x16_bf16 a[48:63], a[252:255], v[108:111], a[48:63]\n"
    "s_waitcnt lgkmcnt(4)\n"
    "ds_read_b64_tr_b16 a[248:249], v18 offset:22528\n"
    "ds_read_b64_tr_b16 a[250:251], v18 offset:23552\n"
    "buffer_load_dwordx4 %[w2b], %[srd2], s40 offen offset:1024 lds\n"
    "v_cvt_pk_bf16_f32 v25, v22, v23\n"
    "v_accvgpr_read_b32 v20, a180\n"
    "v_mfma_f32_32x32x16_bf16 a[32:47], a[224:227], v[112:115], a[32:47]\n"
    "ds_read_b64_tr_b16 a[252:253], v19 offset:22528\n"
    "ds_read_b64_tr_b16 a[254:255], v19 offset:23552\n"
    "v_accvgpr_read_b32 v21, a181\n"
    "v_mfma_f32_32x32x16_bf16 a[48:63], a[228:231], v[112:115], a[48:63]\n"
    "s_waitcnt vmcnt(31)\n"
    "s_barrier\n"
    "ds_read_b64_tr_b16 a[224:225], v18 offset:24576\n"
    "ds_read_b64_tr_b16 a[226:227], v18 offset:25600\n"
    "v_mfma_f32_32x32x16_bf16 a[32:47], a[232:235], v[116:119], a[32:47]\n"
    "ds_read_b64_tr_b16 a[228:229], v19 offset:24576\n"
    "ds_read_b64_tr_b16 a[230:231], v19 offset:25600\n"
    "s_add_u32 m0, %[ldsw], 73728\n"
    "s_mov_b32 s40, 1024\n"
    "v_cvt_pk_bf16_f32 v26, v20, v21\n"
    "v_accvgpr_read_b32 v22, a182\n"
    "v_mfma_f32_32x32x16_bf16 a[48:63], a[236:239], v[116:119], a[48:63]\n"
    "s_waitcnt lgkmcnt(4)\n"
    "ds_read_b64_tr_b16 a[232:233], v18 offset:26624\n"
    "ds_read_b64_tr_b16 a[234:235], v18 offset:27648\n"
    "buffer_load_dwordx4 %[w2a], %[srd2], s40 offen lds\n"
    "v_accvgpr_read_b32 v23, a183\n"
    "v_mfma_f32_32x32x16_bf16 a[32:47], a[240:243], v[120:123], a[32:47]\n"
    "ds_read_b64_tr_b16 a[236:237], v19 offset:26624\n"
    "ds_read_b64_tr_b16 a[238:239], v19 offset:27648\n"
    "v_cvt_pk_bf16_f32 v27, v22, v23\n"
    "buffer_store_dwordx4 v[24:27], %[vl], %[srdp], s41 offen offset:0\n"
    "v_mfma_f32_32x32x16_bf16 a[48:63], a[244:247], v[120:123], a[48:63]\n"
    "ds_read_b64_tr_b16 a[240:241], v18 offset:28672\n"
    "ds_read_b64_tr_b16 a[242:243], v18 offset:29696\n"
    "v_accvgpr_read_b32 v20, a184\n"
    "v_mfma_f32_32x32x16_bf16 a[32:47], a[248:251], v[124:127], a[32:47]\n"
    "ds_read_b64_tr_b16 a[244:245], v19 offset:28672\n"
    "ds_read_b64_tr_b16 a[246:247], v19 offset:29696\n"
    "v_accvgpr_read_b32 v21, a185\n"
    "v_cvt_pk_bf16_f32 v28, v20, v21\n"
    "v_mfma_f32_32x32x16_bf16 a[48:63], a[252:255], v[124:127], a[48:63]\n"
    "s_waitcnt lgkmcnt(4)\n"
    "ds_read_b64_tr_b16 a[248:249], v18 offset:30720\n"
    "ds_read_b64_tr_b16 a[250:251], v18 offset:31744\n"
    "buffer_load_dwordx4 %[w2b], %[srd2], s40 offen offset:1024 lds\n"
    "v_accvgpr_read_b32 v22, a186\n"
    "v_accvgpr_read_b32 v23, a187\n"
    "v_mfma_f32_32x32x16_bf16 a[32:47], a[224:227], v[128:131], a[32:47]\n"
    "ds_read_b64_tr_b16 a[252:253], v19 offset:30720\n"
    "ds_read_b64_tr_b16 a[254:255], v19 offset:31744\n"
    "v_cvt_pk_bf16_f32 v29, v22, v23\n"
    "v_mfma_f32_32x32x16_bf16 a[48:63], a[228:231], v[128:131], a[48:63]\n"
    "ds_read_b64_tr_b16 a[224:225], v18 offset:32768\n"
    "ds_read_b64_tr_b16 a[226:227], v18 offset:33792\n"
    "v_accvgpr_read_b32 v20, a188\n"
    "v_accvgpr_read_b32 v21, a189\n"
    "v_mfma_f32_32x32x16_bf16 a[32:47], a[232:235], v[132:135], a[32:47]\n"
    "ds_read_b64_tr_b16 a[228:229], v19 offset:32768\n"
    "ds_read_b64_tr_b16 a[230:231], v19 offset:33792\n"
    "s_add_u32 m0, %[ldsw], 81920\n"
    "s_mov_b32 s40, 74752\n"
    "v_cvt_pk_bf16_f32 v30, v20, v21\n"
    "v_mfma_f32_32x32x16_bf16 a[48:63], a[236:239], v[132:135], a[48:63]\n"
    "s_waitcnt lgkmcnt(4)\n"
    "ds_read_b64_tr_b16 a[232:233], v18 offset:34816\n"
    "ds_read_b64_tr_b16 a[234:235], v18 offset:35840\n"
    "buffer_load_dwordx4 %[w2a], %[srd2], s40 offen lds\n"
    "v_accvgpr_read_b32 v22, a190\n"
    "v_accvgpr_read_b32 v23, a191\n"
    "v_mfma_f32_32x32x16_bf16 a[32:47], a[240:243], v[136:139], a[32:47]\n"
    "ds_read_b64_tr_b16 a[236:237], v19 offset:34816\n"
    "ds_read_b64_tr_b16 a[238:239], v19 offset:35840\n"
    "v_cvt_pk_bf16_f32 v31, v22, v23\n"
    "v_mfma_f32_32x32x16_bf16 a[48:63], a[244:247], v[136:139], a[48:63]\n"
    "ds_read_b64_tr_b16 a[240:241], v18 offset:36864\n"
    "ds_read_b64_tr_b16 a[242:243], v18 offset:37888\n"
    "buffer_store_dwordx4 v[28:31], %[vl], %[srdp], s41 offen offset:1024\n"
    "s_mov_b32 s41, 12288\n"
    "v_mfma_f32_32x32x16_bf16 a[32:47], a[248:251], v[140:143], a[32:47]\n"
    "ds_read_b64_tr_b16 a[244:245], v19 offset:36864\n"
    "ds_read_b64_tr_b16 a[246:247], v19 offset:37888\n"
    "v_accvgpr_read_b32 v20, a192\n"
    "v_accvgpr_read_b32 v21, a193\n"
    "v_mfma_f32_32x32x16_bf16 a[48:63], a[252:255], v[140:143], a[48:63]\n"
    "s_waitcnt lgkmcnt(4)\n"
    "ds_read_b64_tr_b16 a[248:249], v18 offset:38912\n"
    "ds_read_b64_tr_b16 a[250:251], v18 offset:39936\n"
    "buffer_load_dwordx4 %[w2b], %[srd2], s40 offen offset:1024 lds\n"
    "v_cvt_pk_bf16_f32 v24, v20, v21\n"
    "v_mfma_f32_32x32x16_bf16 a[32:47], a[224:227], v[144:147], a[32:47]\n"
    "ds_read_b64_tr_b16 a[252:253], v19 offset:38912\n"
    "ds_read_b64_tr_b16 a[254:255], v19 offset:39936\n"
    "v_accvgpr_read_b32 v22, a194\n"
    "v_accvgpr_read_b32 v23, a195\n"
    "v_mfma_f32_32x32x16_bf16 a[48:63], a[228:231], v[144:147], a[48:63]\n"
    "ds_read_b64_tr_b16 a[224:225], v18 offset:40960\n"
    "ds_read_b64_tr_b16 a[226:227], v18 offset:41984\n"
    "v_cvt_pk_bf16_f32 v25, v22, v23\n"
    "v_mfma_f32_32x32x16_bf16 a[32:47], a[232:235], v[148:151], a[32:47]\n"
    "ds_read_b64_tr_b16 a[228:229], v19 offset:40960\n"
    "ds_read_b64_tr_b16 a[230:231], v19 offset:41984\n"
    "s_add_u32 m0, %[ldsw], 90112\n"
    "s_mov_b32 s40, 148480\n"
    "v_accvgpr_read_b32 v20, a196\n"
    "v_accvgpr_read_b32 v21, a197\n"
    "v_mfma_f32_32x32x16_bf16 a[48:63], a[236:239], v[148:151], a[48:63]\n"
    "s_waitcnt lgkmcnt(4)\n"
    "ds_read_b64_tr_b16 a[232:233], v18 offset:43008\n"
    "ds_read_b64_tr_b16 a[234:235], v18 offset:44032\n"
    "buffer_load_dwordx4 %[w2a], %[srd2], s40 offen lds\n"
    "v_cvt_pk_bf16_f32 v26, v20, v21\n"
    "v_mfma_f32_32x32x16_bf16 a[32:47], a[240:243], v[152:155], a[32:47]\n"
    "ds_read_b64_tr_b16 a[236:237], v19 offset:43008\n"
    "ds_read_b64_tr_b16 a[238:239], v19 offset:44032\n"
    "v_accvgpr_read_b32 v22, a198\n"
    "v_accvgpr_read_b32 v23, a199\n"
    "v_mfma_f32_32x32x16_bf16 a[48:63], a[244:247], v[152:155], a[48:63]\n"
    "ds_read_b64_tr_b16 a[240:241], v18 offset:45056\n"
    "ds_read_b64_tr_b16 a[242:243], v18 offset:46080\n"
    "v_cvt_pk_bf16_f32 v27, v22, v23\n"
    "v_mfma_f32_32x32x16_bf16 a[32:47], a[248:251], v[156:159], a[32:47]\n"
    "ds_read_b64_tr_b16 a[244:245], v19 offset:45056\n"
    "ds_read_b64_tr_b16 a[246:247], v19 offset:46080\n"
    "buffer_store_dwordx4 v[24:27], %[vl], %[srdp], s41 offen offset:0\n"
    "v_accvgpr_read_b32 v20, a200\n"
    "v_mfma_f32_32x32x16_bf16 a[48:63], a[252:255], v[156:159], a[48:63]\n"
    "s_waitcnt lgkmcnt(4)\n"
    "ds_read_b64_tr_b16 a[248:249], v18 offset:47104\n"
    "ds_read_b64_tr_b16 a[250:251], v18 offset:48128\n"
    "buffer_load_dwordx4 %[w2b], %[srd2], s40 offen offset:1024 lds\n"
    "v_accvgpr_read_b32 v21, a201\n"
    "v_cvt_pk_bf16_f32 v28, v20, v21\n"
    "v_mfma_f32_32x32x16_bf16 a[32:47], a[224:227], v[160:163], a[32:47]\n"
    "ds_read_b64_tr_b16 a[252:253], v19 offset:47104\n"
    "ds_read_b64_tr_b16 a[254:255], v19 offset:48128\n"
    "v_accvgpr_read_b32 v22, a202\n"
    "v_mfma_f32_32x32x16_bf16 a[48:63], a[228:231], v[160:163], a[48:63]\n"
    "s_waitcnt vmcnt(28)\n"
    "s_barrier\n"
    "ds_read_b64_tr_b16 a[224:225], %[rd0] offset:0\n"
    "ds_read_b64_tr_b16 a[226:227], %[rd0] offset:1024\n"
    "v_mfma_f32_32x32x16_bf16 a[32:47], a[232:235], v[164:167], a[32:47]\n"
    "ds_read_b64_tr_b16 a[228:229], %[rd1] offset:0\n"
    "ds_read_b64_tr_b16 a[230:231], %[rd1] offset:1024\n"
    "s_add_u32 m0, %[ldsw], 98304\n"
    "s_mov_b32 s40, 222208\n"
    "v_accvgpr_read_b32 v23, a203\n"
    "v_cvt_pk_bf16_f32 v29, v22, v23\n"
    "v_mfma_f32_32x32x16_bf16 a[48:63], a[236:239], v[164:167], a[48:63]\n"
    "s_waitcnt lgkmcnt(4)\n"
    "ds_read_b64_tr_b16 a[232:233], %[rd0] offset:2048\n"
    "ds_read_b64_tr_b16 a[234:235], %[rd0] offset:3072\n"
    "buffer_load_dwordx4 %[w2a], %[srd2], s40 offen lds\n"
    "v_accvgpr_read_b32 v20, a204\n"
    "v_mfma_f32_32x32x16_bf16 a[32:47], a[240:243], v[168:171], a[32:47]\n"
    "ds_read_b64_tr_b16 a[236:237], %[rd1] offset:2048\n"
    "ds_read_b64_tr_b16 a[238:239], %[rd1] offset:3072\n"
    "v_accvgpr_read_b32 v21, a205\n"
    "v_cvt_pk_bf16_f32 v30, v20, v21\n"
    "v_mfma_f32_32x32x16_bf16 a[48:63], a[244:247], v[168:171], a[48:63]\n"
    "ds_read_b64_tr_b16 a[240:241], %[rd0] offset:4096\n"
    "ds_read_b64_tr_b16 a[242:243], %[rd0] offset:5120\n"
    "v_accvgpr_read_b32 v22, a206\n"
    "v_mfma_f32_32x32x16_bf16 a[32:47], a[248:251], v[172:175], a[32:47]\n"
    "ds_read_b64_tr_b16 a[244:245], %[rd1] offset:4096\n"
    "ds_read_b64_tr_b16 a[246:247], %[rd1] offset:5120\n"
    "v_accvgpr_read_b32 v23, a207\n"
    "v_cvt_pk_bf16_f32 v31, v22, v23\n"
    "v_mfma_f32_32x32x16_bf16 a[48:63], a[252:255], v[172:175], a[48:63]\n"
    "s_waitcnt lgkmcnt(4)\n"
    "ds_read_b64_tr_b16 a[248:249], %[rd0] offset:6144\n"
    "ds_read_b64_tr_b16 a[250:251], %[rd0] offset:7168\n"
    "buffer_load_dwordx4 %[w2b], %[srd2], s40 offen offset:1024 lds\n"
    "buffer_store_dwordx4 v[28:31], %[vl], %[srdp], s41 offen offset:1024\n"
    "v_mfma_f32_32x32x16_bf16 v[208:223], a[224:227], v[32:35], 0\n"
    "ds_read_b64_tr_b16 a[252:253], %[rd1] offset:6144\n"
    "ds_read_b64_tr_b16 a[254:255], %[rd1] offset:7168\n"
    "v_mfma_f32_32x32x16_bf16 v[224:239], a[228:231], v[32:35], 0\n"
    "ds_read_b64_tr_b16 a[224:225], %[rd0] offset:8192\n"
    "ds_read_b64_tr_b16 a[226:227], %[rd0] offset:9216\n"
    "v_mfma_f32_32x32x16_bf16 v[208:223], a[232:235], v[36:39], v[208:223]\n"
    "ds_read_b64_tr_b16 a[228:229], %[rd1] offset:8192\n"
    "ds_read_b64_tr_b16 a[230:231], %[rd1] offset:9216\n"
    "s_add_u32 m0, %[ldsw], 106496\n"
    "s_mov_b32 s40, 295936\n"
    "v_mfma_f32_32x32x16_bf16 v[224:239], a[236:239], v[36:39], v[224:239]\n"
    "s_waitcnt lgkmcnt(4)\n"
    "ds_read_b64_tr_b16 a[232:233], %[rd0] offset:10240\n"
    "ds_read_b64_tr_b16 a[234:235], %[rd0] offset:11264\n"
    "buffer_load_dwordx4 %[w2a], %[srd2], s40 offen lds\n"
    "s_mov_b32 s41, 24576\n"
    "v_accvgpr_read_b32 v20, a32\n"
    "v_mfma_f32_32x32x16_bf16 v[208:223], a[240:243], v[40:43], v[208:223]\n"
    "ds_read_b64_tr_b16 a[236:237], %[rd1] offset:10240\n"
    "ds_read_b64_tr_b16 a[238:239], %[rd1] offset:11264\n"
    "v_accvgpr_read_b32 v21, a33\n"
    "v_mfma_f32_32x32x16_bf16 v[224:239], a[244:247], v[40:43], v[224:239]\n"
    "ds_read_b64_tr_b16 a[240:241], %[rd0] offset:12288\n"
    "ds_read_b64_tr_b16 a[242:243], %[rd0] offset:13312\n"
    "v_cvt_pk_bf16_f32 v24, v20, v21\n"
    "v_mfma_f32_32x32x16_bf16 v[208:223], a[248:251], v[44:47], v[208:223]\n"
    "ds_read_b64_tr_b16 a[244:245], %[rd1] offset:12288\n"
    "ds_read_b64_tr_b16 a[246:247], %[rd1] offset:13312\n"
    "v_accvgpr_read_b32 v22, a34\n"
    "v_mfma_f32_32x32x16_bf16 v[224:239], a[252:255], v[44:47], v[224:239]\n"
    "s_waitcnt lgkmcnt(4)\n"
    "ds_read_b64_tr_b16 a[248:249], %[rd0] offset:14336\n"
    "ds_read_b64_tr_b16 a[250:251], %[rd0] offset:15360\n"
    "buffer_load_dwordx4 %[w2b], %[srd2], s40 offen offset:1024 lds\n"
    "v_accvgpr_read_b32 v23, a35\n"
    "v_cvt_pk_bf16_f32 v25, v22, v23\n"
    "v_mfma_f32_32x32x16_bf16 v[208:223], a[224:227], v[48:51], v[208:223]\n"
    "ds_read_b64_tr_b16 a[252:253], %[rd1] offset:14336\n"
    "ds_read_b64_tr_b16 a[254:255], %[rd1] offset:15360\n"
    "v_accvgpr_read_b32 v20, a36\n"
    "v_mfma_f32_32x32x16_bf16 v[224:239], a[228:231], v[48:51], v[224:239]\n"
    "ds_read_b64_tr_b16 a[224:225], %[rd0] offset:16384\n"
    "ds_read_b64_tr_b16 a[226:227], %[rd0] offset:17408\n"
    "v_accvgpr_read_b32 v21, a37\n"
    "v_mfma_f32_32x32x16_bf16 v[208:223], a[232:235], v[52:55], v[208:223]\n"
    "ds_read_b64_tr_b16 a[228:229], %[rd1] offset:16384\n"
    "ds_read_b64_tr_b16 a[230:231], %[rd1] offset:17408\n"
    "s_add_u32 m0, %[ldsw], 114688\n"
    "s_mov_b32 s40, 369664\n"
    "v_cvt_pk_bf16_f32 v26, v20, v21\n"
    "v_mfma_f32_32x32x16_bf16 v[224:239], a[236:239], v[52:55], v[224:239]\n"
    "s_waitcnt lgkmcnt(4)\n"
    "ds_read_b64_tr_b16 a[232:233], %[rd0] offset:18432\n"
    "ds_read_b64_tr_b16 a[234:235], %[rd0] offset:19456\n"
    "buffer_load_dwordx4 %[w2a], %[srd2], s40 offen lds\n"
    "v_accvgpr_read_b32 v22, a38\n"
    "v_accvgpr_read_b32 v23, a39\n"
    "v_mfma_f32_32x32x16_bf16 v[208:223], a[240:243], v[56:59], v[208:223]\n"
    "ds_read_b64_tr_b16 a[236:237], %[rd1] offset:18432\n"
    "ds_read_b64_tr_b16 a[238:239], %[rd1] offset:19456\n"
    "v_cvt_pk_bf16_f32 v27, v22, v23\n"
    "v_mfma_f32_32x32x16_bf16 v[224:239], a[244:247], v[56:59], v[224:239]\n"
    "ds_read_b64_tr_b16 a[240:241], %[rd0] offset:20480\n"
    "ds_read_b64_tr_b16 a[242:243], %[rd0] offset:21504\n"
    "buffer_store_dwordx4 v[24:27], %[vl], %[srdp], s41 offen offset:0\n"
    "v_mfma_f32_32x32x16_bf16 v[208:223], a[248:251], v[60:63], v[208:223]\n"
    "ds_read_b64_tr_b16 a[244:245], %[rd1] offset:20480\n"
    "ds_read_b64_tr_b16 a[246:247], %[rd1] offset:21504\n"
    "v_accvgpr_read_b32 v20, a40\n"
    "v_mfma_f32_32x32x16_bf16 v[224:239], a[252:255], v[60:63], v[224:239]\n"
    "s_waitcnt lgkmcnt(4)\n"
    "ds_read_b64_tr_b16 a[248:249], %[rd0] offset:22528\n"
    "ds_read_b64_tr_b16 a[250:251], %[rd0] offset:23552\n"
    "buffer_load_dwordx4 %[w2b], %[srd2], s40 offen offset:1024 lds\n"
    "v_accvgpr_read_b32 v21, a41\n"
    "v_mfma_f32_32x32x16_bf16 v[208:223], a[224:227], v[64:67], v[208:223]\n"
    "ds_read_b64_tr_b16 a[252:253], %[rd1] offset:22528\n"
    "ds_read_b64_tr_b16 a[254:255], %[rd1] offset:23552\n"
    "v_cvt_pk_bf16_f32 v28, v20, v21\n"
    "v_accvgpr_read_b32 v22, a42\n"
    "v_mfma_f32_32x32x16_bf16 v[224:239], a[228:231], v[64:67], v[224:239]\n"
    "s_waitcnt vmcnt(26)\n"
    "s_barrier\n"
    "ds_read_b64_tr_b16 a[224:225], %[rd0] offset:24576\n"
    "ds_read_b64_tr_b16 a[226:227], %[rd0] offset:25600\n"
    "v_mfma_f32_32x32x16_bf16 v[208:223], a[232:235], v[68:71], v[208:223]\n"
    "ds_read_b64_tr_b16 a[228:229], %[rd1] offset:24576\n"
    "ds_read_b64_tr_b16 a[230:231], %[rd1] offset:25600\n"
    "s_add_u32 m0, %[ldsw], 122880\n"
    "s_mov_b32 s40, 443392\n"
    "v_accvgpr_read_b32 v23, a43\n"
    "v_mfma_f32_32x32x16_bf16 v[224:239], a[236:239], v[68:71], v[224:239]\n"
    "s_waitcnt lgkmcnt(4)\n"
    "ds_read_b64_tr_b16 a[232:233], %[rd0] offset:26624\n"
    "ds_read_b64_tr_b16 a[234:235], %[rd0] offset:27648\n"
    "buffer_load_dwordx4 %[w2a], %[srd2], s40 offen lds\n"
    "v_cvt_pk_bf16_f32 v29, v22, v23\n"
    "v_mfma_f32_32x32x16_bf16 v[208:223], a[240:243], v[72:75], v[208:223]\n"
    "ds_read_b64_tr_b16 a[236:237], %[rd1] offset:26624\n"
    "ds_read_b64_tr_b16 a[238:239], %[rd1] offset:27648\n"
    "v_accvgpr_read_b32 v20, a44\n"
    "v_mfma_f32_32x32x16_bf16 v[224:239], a[244:247], v[72:75], v[224:239]\n"
    "ds_read_b64_tr_b16 a[240:241], %[rd0] offset:28672\n"
    "ds_read_b64_tr_b16 a[242:243], %[rd0] offset:29696\n"
    "v_accvgpr_read_b32 v21, a45\n"
    "v_cvt_pk_bf16_f32 v30, v20, v21\n"
    "v_mfma_f32_32x32x16_bf16 v[208:223], a[248:251], v[76:79], v[208:223]\n"
    "ds_read_b64_tr_b16 a[244:245], %[rd1] offset:28672\n"
    "ds_read_b64_tr_b16 a[246:247], %[rd1] offset:29696\n"
    "v_accvgpr_read_b32 v22, a46\n"
    "v_mfma_f32_32x32x16_bf16 v[224:239], a[252:255], v[76:79], v[224:239]\n"
    "s_waitcnt lgkmcnt(4)\n"
    "ds_read_b64_tr_b16 a[248:249], %[rd0] offset:30720\n"
    "ds_read_b64_tr_b16 a[250:251], %[rd0] offset:31744\n"
    "buffer_load_dwordx4 %[w2b], %[srd2], s40 offen offset:1024 lds\n"
    "v_accvgpr_read_b32 v23, a47\n"
    "v_mfma_f32_32x32x16_bf16 v[208:223], a[224:227], v[80:83], v[208:223]\n"
    "ds_read_b64_tr_b16 a[252:253], %[rd1] offset:30720\n"
    "ds_read_b64_tr_b16 a[254:255], %[rd1] offset:31744\n"
    "v_cvt_pk_bf16_f32 v31, v22, v23\n"
    "v_mfma_f32_32x32x16_bf16 v[224:239], a[228:231], v[80:83], v[224:239]\n"
    "ds_read_b64_tr_b16 a[224:225], %[rd0] offset:32768\n"
    "ds_read_b64_tr_b16 a[226:227], %[rd0] offset:33792\n"
    "buffer_store_dwordx4 v[28:31], %[vl], %[srdp], s41 offen offset:1024\n"
    "v_mfma_f32_32x32x16_bf16 v[208:223], a[232:235], v[84:87], v[208:223]\n"
    "ds_read_b64_tr_b16 a[228:229], %[rd1] offset:32768\n"
    "ds_read_b64_tr_b16 a[230:231], %[rd1] offset:33792\n"
    "s_add_u32 m0, %[ldsw], 131072\n"
    "s_mov_b32 s40, 517120\n"
    "s_mov_b32 s41, 26624\n"
    "v_accvgpr_read_b32 v20, a48\n"
    "v_mfma_f32_32x32x16_bf16 v[224:239], a[236:239], v[84:87], v[224:239]\n"
    "s_waitcnt lgkmcnt(4)\n"
    "ds_read_b64_tr_b16 a[232:233], %[rd0] offset:34816\n"
    "ds_read_b64_tr_b16 a[234:235], %[rd0] offset:35840\n"
    "buffer_load_dwordx4 %[w2a], %[srd2], s40 offen lds\n"
    "v_accvgpr_read_b32 v21, a49\n"
    "v_mfma_f32_32x32x16_bf16 v[208:223], a[240:243], v[88:91], v[208:223]\n"
    "ds_read_b64_tr_b16 a[236:237], %[rd1] offset:34816\n"
    "ds_read_b64_tr_b16 a[238:239], %[rd1] offset:35840\n"
    "v_cvt_pk_bf16_f32 v24, v20, v21\n"
    "v_mfma_f32_32x32x16_bf16 v[224:239], a[244:247], v[88:91], v[224:239]\n"
    "ds_read_b64_tr_b16 a[240:241], %[rd0] offset:36864\n"
    "ds_read_b64_tr_b16 a[242:243], %[rd0] offset:37888\n"
    "v_accvgpr_read_b32 v22, a50\n"
    "v_mfma_f32_32x32x16_bf16 v[208:223], a[248:251], v[92:95], v[208:223]\n"
    "ds_read_b64_tr_b16 a[244:245], %[rd1] offset:36864\n"
    "ds_read_b64_tr_b16 a[246:247], %[rd1] offset:37888\n"
    "v_accvgpr_read_b32 v23, a51\n"
    "v_cvt_pk_bf16_f32 v25, v22, v23\n"
    "v_mfma_f32_32x32x16_bf16 v[224:239], a[252:255], v[92:95], v[224:239]\n"
    "s_waitcnt lgkmcnt(4)\n"
    "ds_read_b64_tr_b16 a[248:249], %[rd0] offset:38912\n"
    "ds_read_b64_tr_b16 a[250:251], %[rd0] offset:39936\n"
    "buffer_load_dwordx4 %[w2b], %[srd2], s40 offen offset:1024 lds\n"
    "v_accvgpr_read_b32 v20, a52\n"
    "v_mfma_f32_32x32x16_bf16 v[208:223], a[224:227], v[96:99], v[208:223]\n"
    "ds_read_b64_tr_b16 a[252:253], %[rd1] offset:38912\n"
    "ds_read_b64_tr_b16 a[254:255], %[rd1] offset:39936\n"
    "v_accvgpr_read_b32 v21, a53\n"
    "v_mfma_f32_32x32x16_bf16 v[224:239], a[228:231], v[96:99], v[224:239]\n"
    "ds_read_b64_tr_b16 a[224:225], %[rd0] offset:40960\n"
    "ds_read_b64_tr_b16 a[226:227], %[rd0] offset:41984\n"
    "v_cvt_pk_bf16_f32 v26, v20, v21\n"
    "v_mfma_f32_32x32x16_bf16 v[208:223], a[232:235], v[100:103], v[208:223]\n"
    "ds_read_b64_tr_b16 a[228:229], %[rd1] offset:40960\n"
    "ds_read_b64_tr_b16 a[230:231], %[rd1] offset:41984\n"
    "s_add_u32 m0, %[ldsw], 139264\n"
    "s_mov_b32 s40, 590848\n"
    "v_accvgpr_read_b32 v22, a54\n"
    "v_accvgpr_read_b32 v23, a55\n"
    "v_mfma_f32_32x32x16_bf16 v[224:239], a[236:239], v[100:103], v[224:239]\n"
    "s_waitcnt lgkmcnt(4)\n"
    "ds_read_b64_tr_b16 a[232:233], %[rd0] offset:43008\n"
    "ds_read_b64_tr_b16 a[234:235], %[rd0] offset:44032\n"
    "buffer_load_dwordx4 %[w2a], %[srd2], s40 offen lds\n"
    "v_cvt_pk_bf16_f32 v27, v22, v23\n"
    "v_mfma_f32_32x32x16_bf16 v[208:223], a[240:243], v[104:107], v[208:223]\n"
    "ds_read_b64_tr_b16 a[236:237], %[rd1] offset:43008\n"
    "ds_read_b64_tr_b16 a[238:239], %[rd1] offset:44032\n"
    "buffer_store_dwordx4 v[24:27], %[vl], %[srdp], s41 offen offset:0\n"
    "v_mfma_f32_32x32x16_bf16 v[224:239], a[244:247], v[104:107], v[224:239]\n"
    "ds_read_b64_tr_b16 a[240:241], %[rd0] offset:45056\n"
    "ds_read_b64_tr_b16 a[242:243], %[rd0] offset:46080\n"
    "v_accvgpr_read_b32 v20, a56\n"
    "v_mfma_f32_32x32x16_bf16 v[208:223], a[248:251], v[108:111], v[208:223]\n"
    "ds_read_b64_tr_b16 a[244:245], %[rd1] offset:45056\n"
    "ds_read_b64_tr_b16 a[246:247], %[rd1] offset:46080\n"
    "v_accvgpr_read_b32 v21, a57\n"
    "v_mfma_f32_32x32x16_bf16 v[224:239], a[252:255], v[108:111], v[224:239]\n"
    "s_waitcnt lgkmcnt(4)\n"
    "ds_read_b64_tr_b16 a[248:249], %[rd0] offset:47104\n"
    "ds_read_b64_tr_b16 a[250:251], %[rd0] offset:48128\n"
    "buffer_load_dwordx4 %[w2b], %[srd2], s40 offen offset:1024 lds\n"
    "v_cvt_pk_bf16_f32 v28, v20, v21\n"
    "v_accvgpr_read_b32 v22, a58\n"
    "v_mfma_f32_32x32x16_bf16 v[208:223], a[224:227], v[112:115], v[208:223]\n"
    "ds_read_b64_tr_b16 a[252:253], %[rd1] offset:47104\n"
    "ds_read_b64_tr_b16 a[254:255], %[rd1] offset:48128\n"
    "v_accvgpr_read_b32 v23, a59\n"
    "v_mfma_f32_32x32x16_bf16 v[224:239], a[228:231], v[112:115], v[224:239]\n"
    "s_waitcnt vmcnt(25)\n"
    "s_barrier\n"
    "ds_read_b64_tr_b16 a[224:225], v16 offset:0\n"
    "ds_read_b64_tr_b16 a[226:227], v16 offset:1024\n"
    "v_mfma_f32_32x32x16_bf16 v[208:223], a[232:235], v[116:119], v[208:223]\n"
    "ds_read_b64_tr_b16 a[228:229], v17 offset:0\n"
    "ds_read_b64_tr_b16 a[230:231], v17 offset:1024\n"
    "v_cvt_pk_bf16_f32 v29, v22, v23\n"
    "v_mfma_f32_32x32x16_bf16 v[224:239], a[236:239], v[116:119], v[224:239]\n"
    "s_waitcnt lgkmcnt(4)\n"
    "ds_read_b64_tr_b16 a[232:233], v16 offset:2048\n"
    "ds_read_b64_tr_b16 a[234:235], v16 offset:3072\n"
    "v_accvgpr_read_b32 v20, a60\n"
    "v_mfma_f32_32x32x16_bf16 v[208:223], a[240:243], v[120:123], v[208:223]\n"
    "ds_read_b64_tr_b16 a[236:237], v17 offset:2048\n"
    "ds_read_b64_tr_b16 a[238:239], v17 offset:3072\n"
    "v_accvgpr_read_b32 v21, a61\n"
    "v_cvt_pk_bf16_f32 v30, v20, v21\n"
    "v_mfma_f32_32x32x16_bf16 v[224:239], a[244:247], v[120:123], v[224:239]\n"
    "ds_read_b64_tr_b16 a[240:241], v16 offset:4096\n"
    "ds_read_b64_tr_b16 a[242:243], v16 offset:5120\n"
    "v_accvgpr_read_b32 v22, a62\n"
    "v_mfma_f32_32x32x16_bf16 v[208:223], a[248:251], v[124:127], v[208:223]\n"
    "ds_read_b64_tr_b16 a[244:245], v17 offset:4096\n"
    "ds_read_b64_tr_b16 a[246:247], v17 offset:5120\n"
    "v_accvgpr_read_b32 v23, a63\n"
    "v_mfma_f32_32x32x16_bf16 v[224:239], a[252:255], v[124:127], v[224:239]\n"
    "s_waitcnt lgkmcnt(4)\n"
    "ds_read_b64_tr_b16 a[248:249], v16 offset:6144\n"
    "ds_read_b64_tr_b16 a[250:251], v16 offset:7168\n"
    "v_cvt_pk_bf16_f32 v31, v22, v23\n"
    "v_mfma_f32_32x32x16_bf16 v[208:223], a[224:227], v[128:131], v[208:223]\n"
    "ds_read_b64_tr_b16 a[252:253], v17 offset:6144\n"
    "ds_read_b64_tr_b16 a[254:255], v17 offset:7168\n"
    "buffer_store_dwordx4 v[28:31], %[vl], %[srdp], s41 offen offset:1024\n"
    "v_mfma_f32_32x32x16_bf16 v[224:239], a[228:231], v[128:131], v[224:239]\n"
    "ds_read_b64_tr_b16 a[224:225], v16 offset:8192\n"
    "ds_read_b64_tr_b16 a[226:227], v16 offset:9216\n"
    "s_mov_b32 s41, 14336\n"
    "v_accvgpr_read_b32 v20, a208\n"
    "v_mfma_f32_32x32x16_bf16 v[208:223], a[232:235], v[132:135], v[208:223]\n"
    "ds_read_b64_tr_b16 a[228:229], v17 offset:8192\n"
    "ds_read_b64_tr_b16 a[230:231], v17 offset:9216\n"
    "v_accvgpr_read_b32 v21, a209\n"
    "v_mfma_f32_32x32x16_bf16 v[224:239], a[236:239], v[132:135], v[224:239]\n"
    "s_waitcnt lgkmcnt(4)\n"
    "ds_read_b64_tr_b16 a[232:233], v16 offset:10240\n"
    "ds_read_b64_tr_b16 a[234:235], v16 offset:11264\n"
    "v_cvt_pk_bf16_f32 v24, v20, v21\n"
    "v_mfma_f32_32x32x16_bf16 v[208:223], a[240:243], v[136:139], v[208:223]\n"
    "ds_read_b64_tr_b16 a[236:237], v17 offset:10240\n"
    "ds_read_b64_tr_b16 a[238:239], v17 offset:11264\n"
    "v_accvgpr_read_b32 v22, a210\n"
    "v_mfma_f32_32x32x16_bf16 v[224:239], a[244:247], v[136:139], v[224:239]\n"
    "ds_read_b64_tr_b16 a[240:241], v16 offset:12288\n"
    "ds_read_b64_tr_b16 a[242:243], v16 offset:13312\n"
    "v_accvgpr_read_b32 v23, a211\n"
    "v_cvt_pk_bf16_f32 v25, v22, v23\n"
    "v_mfma_f32_32x32x16_bf16 v[208:223], a[248:251], v[140:143], v[208:223]\n"
    "ds_read_b64_tr_b16 a[244:245], v17 offset:12288\n"
    "ds_read_b64_tr_b16 a[246:247], v17 offset:13312\n"
    "v_accvgpr_read_b32 v20, a212\n"
    "v_mfma_f32_32x32x16_bf16 v[224:239], a[252:255], v[140:143], v[224:239]\n"
    "s_waitcnt lgkmcnt(4)\n"
    "ds_read_b64_tr_b16 a[248:249], v16 offset:14336\n"
    "ds_read_b64_tr_b16 a[250:251], v16 offset:15360\n"
    "v_accvgpr_read_b32 v21, a213\n"
    "v_mfma_f32_32x32x16_bf16 v[208:223], a[224:227], v[144:147], v[208:223]\n"
    "ds_read_b64_tr_b16 a[252:253], v17 offset:14336\n"
    "ds_read_b64_tr_b16 a[254:255], v17 offset:15360\n"
    "v_cvt_pk_bf16_f32 v26, v20, v21\n"
    "v_mfma_f32_32x32x16_bf16 v[224:239], a[228:231], v[144:147], v[224:239]\n"
    "ds_read_b64_tr_b16 a[224:225], v16 offset:16384\n"
    "ds_read_b64_tr_b16 a[226:227], v16 offset:17408\n"
    "v_accvgpr_read_b32 v22, a214\n"
    "v_accvgpr_read_b32 v23, a215\n"
    "v_mfma_f32_32x32x16_bf16 v[208:223], a[232:235], v[148:151], v[208:223]\n"
    "ds_read_b64_tr_b16 a[228:229], v17 offset:16384\n"
    "ds_read_b64_tr_b16 a[230:231], v17 offset:17408\n"
    "v_cvt_pk_bf16_f32 v27, v22, v23\n"
    "v_mfma_f32_32x32x16_bf16 v[224:239], a[236:239], v[148:151], v[224:239]\n"
    "s_waitcnt lgkmcnt(4)\n"
    "ds_read_b64_tr_b16 a[232:233], v16 offset:18432\n"
    "ds_read_b64_tr_b16 a[234:235], v16 offset:19456\n"
    "buffer_store_dwordx4 v[24:27], %[vl], %[srdp], s41 offen offset:0\n"
    "v_mfma_f32_32x32x16_bf16 v[208:223], a[240:243], v[152:155], v[208:223]\n"
    "ds_read_b64_tr_b16 a[236:237], v17 offset:18432\n"
    "ds_read_b64_tr_b16 a[238:239], v17 offset:19456\n"
    "v_accvgpr_read_b32 v20, a216\n"
    "v_mfma_f32_32x32x16_bf16 v[224:239], a[244:247], v[152:155], v[224:239]\n"
    "ds_read_b64_tr_b16 a[240:241], v16 offset:20480\n"
    "ds_read_b64_tr_b16 a[242:243], v16 offset:21504\n"
    "v_accvgpr_read_b32 v21, a217\n"
    "v_mfma_f32_32x32x16_bf16 v[208:223], a[248:251], v[156:159], v[208:223]\n"
    "ds_read_b64_tr_b16 a[244:245], v17 offset:20480\n"
    "ds_read_b64_tr_b16 a[246:247], v17 offset:21504\n"
    "v_cvt_pk_bf16_f32 v28, v20, v21\n"
    "v_accvgpr_read_b32 v22, a218\n"
    "v_mfma_f32_32x32x16_bf16 v[224:239], a[252:255], v[156:159], v[224:239]\n"
    "s_waitcnt lgkmcnt(4)\n"
    "ds_read_b64_tr_b16 a[248:249], v16 offset:22528\n"
    "ds_read_b64_tr_b16 a[250:251], v16 offset:23552\n"
    "v_accvgpr_read_b32 v23, a219\n"
    "v_mfma_f32_32x32x16_bf16 v[208:223], a[224:227], v[160:163], v[208:223]\n"
    "ds_read_b64_tr_b16 a[252:253], v17 offset:22528\n"
    "ds_read_b64_tr_b16 a[254:255], v17 offset:23552\n"
    "v_cvt_pk_bf16_f32 v29, v22, v23\n"
    "v_mfma_f32_32x32x16_bf16 v[224:239], a[228:231], v[160:163], v[224:239]\n"
    "s_waitcnt vmcnt(18)\n"
    "s_barrier\n"
    "ds_read_b64_tr_b16 a[224:225], v16 offset:24576\n"
    "ds_read_b64_tr_b16 a[226:227], v16 offset:25600\n"
    "v_mfma_f32_32x32x16_bf16 v[208:223], a[232:235], v[164:167], v[208:223]\n"
    "ds_read_b64_tr_b16 a[228:229], v17 offset:24576\n"
    "ds_read_b64_tr_b16 a[230:231], v17 offset:25600\n"
    "v_accvgpr_read_b32 v20, a220\n"
    "v_mfma_f32_32x32x16_bf16 v[224:239], a[236:239], v[164:167], v[224:239]\n"
    "s_waitcnt lgkmcnt(4)\n"
    "ds_read_b64_tr_b16 a[232:233], v16 offset:26624\n"
    "ds_read_b64_tr_b16 a[234:235], v16 offset:27648\n"
    "v_accvgpr_read_b32 v21, a221\n"
    "v_cvt_pk_bf16_f32 v30, v20, v21\n"
    "v_mfma_f32_32x32x16_bf16 v[208:223], a[240:243], v[168:171], v[208:223]\n"
    "ds_read_b64_tr_b16 a[236:237], v17 offset:26624\n"
    "ds_read_b64_tr_b16 a[238:239], v17 offset:27648\n"
    "v_accvgpr_read_b32 v22, a222\n"
    "v_mfma_f32_32x32x16_bf16 v[224:239], a[244:247], v[168:171], v[224:239]\n"
    "ds_read_b64_tr_b16 a[240:241], v16 offset:28672\n"
    "ds_read_b64_tr_b16 a[242:243], v16 offset:29696\n"
    "v_accvgpr_read_b32 v23, a223\n"
    "v_mfma_f32_32x32x16_bf16 v[208:223], a[248:251], v[172:175], v[208:223]\n"
    "ds_read_b64_tr_b16 a[244:245], v17 offset:28672\n"
    "ds_read_b64_tr_b16 a[246:247], v17 offset:29696\n"
    "v_cvt_pk_bf16_f32 v31, v22, v23\n"
    "v_mfma_f32_32x32x16_bf16 v[224:239], a[252:255], v[172:175], v[224:239]\n"
    "s_waitcnt lgkmcnt(4)\n"
    "ds_read_b64_tr_b16 a[248:249], v16 offset:30720\n"
    "ds_read_b64_tr_b16 a[250:251], v16 offset:31744\n"
    "buffer_store_dwordx4 v[28:31], %[vl], %[srdp], s41 offen offset:1024\n"
    "v_mfma_f32_32x32x16_bf16 v[176:191], a[224:227], v[32:35], 0\n"
    "ds_read_b64_tr_b16 a[252:253], v17 offset:30720\n"
    "ds_read_b64_tr_b16 a[254:255], v17 offset:31744\n"
    "v_mfma_f32_32x32x16_bf16 v[192:207], a[228:231], v[32:35], 0\n"
    "ds_read_b64_tr_b16 a[224:225], v16 offset:32768\n"
    "ds_read_b64_tr_b16 a[226:227], v16 offset:33792\n"
    "v_mfma_f32_32x32x16_bf16 v[176:191], a[232:235], v[36:39], v[176:191]\n"
    "ds_read_b64_tr_b16 a[228:229], v17 offset:32768\n"
    "ds_read_b64_tr_b16 a[230:231], v17 offset:33792\n"
    "v_mfma_f32_32x32x16_bf16 v[192:207], a[236:239], v[36:39], v[192:207]\n"
    "s_waitcnt lgkmcnt(4)\n"
    "ds_read_b64_tr_b16 a[232:233], v16 offset:34816\n"
    "ds_read_b64_tr_b16 a[234:235], v16 offset:35840\n"
    "s_mov_b32 s41, 28672\n"
    "v_mfma_f32_32x32x16_bf16 v[176:191], a[240:243], v[40:43], v[176:191]\n"
    "ds_read_b64_tr_b16 a[236:237], v17 offset:34816\n"
    "ds_read_b64_tr_b16 a[238:239], v17 offset:35840\n"
    "v_mfma_f32_32x32x16_bf16 v[192:207], a[244:247], v[40:43], v[192:207]\n"
    "ds_read_b64_tr_b16 a[240:241], v16 offset:36864\n"
    "ds_read_b64_tr_b16 a[242:243], v16 offset:37888\n"
    "v_cvt_pk_bf16_f32 v24, v208, v209\n"
    "v_mfma_f32_32x32x16_bf16 v[176:191], a[248:251], v[44:47], v[176:191]\n"
    "ds_read_b64_tr_b16 a[244:245], v17 offset:36864\n"
    "ds_read_b64_tr_b16 a[246:247], v17 offset:37888\n"
    "v_mfma_f32_32x32x16_bf16 v[192:207], a[252:255], v[44:47], v[192:207]\n"
    "s_waitcnt lgkmcnt(4)\n"
    "ds_read_b64_tr_b16 a[248:249], v16 offset:38912\n"
    "ds_read_b64_tr_b16 a[250:251], v16 offset:39936\n"
    "v_mfma_f32_32x32x16_bf16 v[176:191], a[224:227], v[48:51], v[176:191]\n"
    "ds_read_b64_tr_b16 a[252:253], v17 offset:38912\n"
    "ds_read_b64_tr_b16 a[254:255], v17 offset:39936\n"
    "v_cvt_pk_bf16_f32 v25, v210, v211\n"
    "v_mfma_f32_32x32x16_bf16 v[192:207], a[228:231], v[48:51], v[192:207]\n"
    "ds_read_b64_tr_b16 a[224:225], v16 offset:40960\n"
    "ds_read_b64_tr_b16 a[226:227], v16 offset:41984\n"
    "v_mfma_f32_32x32x16_bf16 v[176:191], a[232:235], v[52:55], v[176:191]\n"
    "ds_read_b64_tr_b16 a[228:229], v17 offset:40960\n"
    "ds_read_b64_tr_b16 a[230:231], v17 offset:41984\n"
    "v_mfma_f32_32x32x16_bf16 v[192:207], a[236:239], v[52:55], v[192:207]\n"
    "s_waitcnt lgkmcnt(4)\n"
    "ds_read_b64_tr_b16 a[232:233], v16 offset:43008\n"
    "ds_read_b64_tr_b16 a[234:235], v16 offset:44032\n"
    "v_cvt_pk_bf16_f32 v26, v212, v213\n"
    "v_mfma_f32_32x32x16_bf16 v[176:191], a[240:243], v[56:59], v[176:191]\n"
    "ds_read_b64_tr_b16 a[236:237], v17 offset:43008\n"
    "ds_read_b64_tr_b16 a[238:239], v17 offset:44032\n"
    "v_mfma_f32_32x32x16_bf16 v[192:207], a[244:247], v[56:59], v[192:207]\n"
    "ds_read_b64_tr_b16 a[240:241], v16 offset:45056\n"
    "ds_read_b64_tr_b16 a[242:243], v16 offset:46080\n"
    "v_cvt_pk_bf16_f32 v27, v214, v215\n"
    "v_mfma_f32_32x32x16_bf16 v[176:191], a[248:251], v[60:63], v[176:191]\n"
    "ds_read_b64_tr_b16 a[244:245], v17 offset:45056\n"
    "ds_read_b64_tr_b16 a[246:247], v17 offset:46080\n"
    "v_mfma_f32_32x32x16_bf16 v[192:207], a[252:255], v[60:63], v[192:207]\n"
    "s_waitcnt lgkmcnt(4)\n"
    "ds_read_b64_tr_b16 a[248:249], v16 offset:47104\n"
    "ds_read_b64_tr_b16 a[250:251], v16 offset:48128\n"
    "v_mfma_f32_32x32x16_bf16 v[176:191], a[224:227], v[64:67], v[176:191]\n"
    "ds_read_b64_tr_b16 a[252:253], v17 offset:47104\n"
    "ds_read_b64_tr_b16 a[254:255], v17 offset:48128\n"
    "buffer_store_dwordx4 v[24:27], %[vl], %[srdp], s41 offen offset:0\n"
    "v_mfma_f32_32x32x16_bf16 v[192:207], a[228:231], v[64:67], v[192:207]\n"
    "s_waitcnt vmcnt(12)\n"
    "s_barrier\n"
    "ds_read_b64_tr_b16 a[224:225], v18 offset:0\n"
    "ds_read_b64_tr_b16 a[226:227], v18 offset:1024\n"
    "v_mfma_f32_32x32x16_bf16 v[176:191], a[232:235], v[68:71], v[176:191]\n"
    "ds_read_b64_tr_b16 a[228:229], v19 offset:0\n"
    "ds_read_b64_tr_b16 a[230:231], v19 offset:1024\n"
    "v_mfma_f32_32x32x16_bf16 v[192:207], a[236:239], v[68:71], v[192:207]\n"
    "s_waitcnt lgkmcnt(4)\n"
    "ds_read_b64_tr_b16 a[232:233], v18 offset:2048\n"
    "ds_read_b64_tr_b16 a[234:235], v18 offset:3072\n"
    "v_mfma_f32_32x32x16_bf16 v[176:191], a[240:243], v[72:75], v[176:191]\n"
    "ds_read_b64_tr_b16 a[236:237], v19 offset:2048\n"
    "ds_read_b64_tr_b16 a[238:239], v19 offset:3072\n"
    "v_cvt_pk_bf16_f32 v28, v216, v217\n"
    "v_mfma_f32_32x32x16_bf16 v[192:207], a[244:247], v[72:75], v[192:207]\n"
    "ds_read_b64_tr_b16 a[240:241], v18 offset:4096\n"
    "ds_read_b64_tr_b16 a[242:243], v18 offset:5120\n"
    "v_mfma_f32_32x32x16_bf16 v[176:191], a[248:251], v[76:79], v[176:191]\n"
    "ds_read_b64_tr_b16 a[244:245], v19 offset:4096\n"
    "ds_read_b64_tr_b16 a[246:247], v19 offset:5120\n"
    "v_cvt_pk_bf16_f32 v29, v218, v219\n"
    "v_mfma_f32_32x32x16_bf16 v[192:207], a[252:255], v[76:79], v[192:207]\n"
    "s_waitcnt lgkmcnt(4)\n"
    "ds_read_b64_tr_b16 a[248:249], v18 offset:6144\n"
    "ds_read_b64_tr_b16 a[250:251], v18 offset:7168\n"
    "v_mfma_f32_32x32x16_bf16 v[176:191], a[224:227], v[80:83], v[176:191]\n"
    "ds_read_b64_tr_b16 a[252:253], v19 offset:6144\n"
    "ds_read_b64_tr_b16 a[254:255], v19 offset:7168\n"
    "v_mfma_f32_32x32x16_bf16 v[192:207], a[228:231], v[80:83], v[192:207]\n"
    "ds_read_b64_tr_b16 a[224:225], v18 offset:8192\n"
    "ds_read_b64_tr_b16 a[226:227], v18 offset:9216\n"
    "v_cvt_pk_bf16_f32 v30, v220, v221\n"
    "v_mfma_f32_32x32x16_bf16 v[176:191], a[232:235], v[84:87], v[176:191]\n"
    "ds_read_b64_tr_b16 a[228:229], v19 offset:8192\n"
    "ds_read_b64_tr_b16 a[230:231], v19 offset:9216\n"
    "v_mfma_f32_32x32x16_bf16 v[192:207], a[236:239], v[84:87], v[192:207]\n"
    "s_waitcnt lgkmcnt(4)\n"
    "ds_read_b64_tr_b16 a[232:233], v18 offset:10240\n"
    "ds_read_b64_tr_b16 a[234:235], v18 offset:11264\n"
    "v_mfma_f32_32x32x16_bf16 v[176:191], a[240:243], v[88:91], v[176:191]\n"
    "ds_read_b64_tr_b16 a[236:237], v19 offset:10240\n"
    "ds_read_b64_tr_b16 a[238:239], v19 offset:11264\n"
    "v_cvt_pk_bf16_f32 v31, v222, v223\n"
    "v_mfma_f32_32x32x16_bf16 v[192:207], a[244:247], v[88:91], v[192:207]\n"
    "ds_read_b64_tr_b16 a[240:241], v18 offset:12288\n"
    "ds_read_b64_tr_b16 a[242:243], v18 offset:13312\n"
    "v_mfma_f32_32x32x16_bf16 v[176:191], a[248:251], v[92:95], v[176:191]\n"
    "ds_read_b64_tr_b16 a[244:245], v19 offset:12288\n"
    "ds_read_b64_tr_b16 a[246:247], v19 offset:13312\n"
    "buffer_store_dwordx4 v[28:31], %[vl], %[srdp], s41 offen offset:1024\n"
    "v_mfma_f32_32x32x16_bf16 v[192:207], a[252:255], v[92:95], v[192:207]\n"
    "s_waitcnt lgkmcnt(4)\n"
    "ds_read_b64_tr_b16 a[248:249], v18 offset:14336\n"
    "ds_read_b64_tr_b16 a[250:251], v18 offset:15360\n"
    "v_mfma_f32_32x32x16_bf16 v[176:191], a[224:227], v[96:99], v[176:191]\n"
    "ds_read_b64_tr_b16 a[252:253], v19 offset:14336\n"
    "ds_read_b64_tr_b16 a[254:255], v19 offset:15360\n"
    "v_mfma_f32_32x32x16_bf16 v[192:207], a[228:231], v[96:99], v[192:207]\n"
    "ds_read_b64_tr_b16 a[224:225], v18 offset:16384\n"
    "ds_read_b64_tr_b16 a[226:227], v18 offset:17408\n"
    "s_mov_b32 s41, 30720\n"
    "v_mfma_f32_32x32x16_bf16 v[176:191], a[232:235], v[100:103], v[176:191]\n"
    "ds_read_b64_tr_b16 a[228:229], v19 offset:16384\n"
    "ds_read_b64_tr_b16 a[230:231], v19 offset:17408\n"
    "v_mfma_f32_32x32x16_bf16 v[192:207], a[236:239], v[100:103], v[192:207]\n"
    "s_waitcnt lgkmcnt(4)\n"
    "ds_read_b64_tr_b16 a[232:233], v18 offset:18432\n"
    "ds_read_b64_tr_b16 a[234:235], v18 offset:19456\n"
    "v_mfma_f32_32x32x16_bf16 v[176:191], a[240:243], v[104:107], v[176:191]\n"
    "ds_read_b64_tr_b16 a[236:237], v19 offset:18432\n"
    "ds_read_b64_tr_b16 a[238:239], v19 offset:19456\n"
    "v_cvt_pk_bf16_f32 v24, v224, v225\n"
    "v_mfma_f32_32x32x16_bf16 v[192:207], a[244:247], v[104:107], v[192:207]\n"
    "ds_read_b64_tr_b16 a[240:241], v18 offset:20480\n"
    "ds_read_b64_tr_b16 a[242:243], v18 offset:21504\n"
    "v_mfma_f32_32x32x16_bf16 v[176:191], a[248:251], v[108:111], v[176:191]\n"
    "ds_read_b64_tr_b16 a[244:245], v19 offset:20480\n"
    "ds_read_b64_tr_b16 a[246:247], v19 offset:21504\n"
    "v_cvt_pk_bf16_f32 v25, v226, v227\n"
    "v_mfma_f32_32x32x16_bf16 v[192:207], a[252:255], v[108:111], v[192:207]\n"
    "s_waitcnt lgkmcnt(4)\n"
    "ds_read_b64_tr_b16 a[248:249], v18 offset:22528\n"
    "ds_read_b64_tr_b16 a[250:251], v18 offset:23552\n"
    "v_mfma_f32_32x32x16_bf16 v[176:191], a[224:227], v[112:115], v[176:191]\n"
    "ds_read_b64_tr_b16 a[252:253], v19 offset:22528\n"
    "ds_read_b64_tr_b16 a[254:255], v19 offset:23552\n"
    "v_mfma_f32_32x32x16_bf16 v[192:207], a[228:231], v[112:115], v[192:207]\n"
    "s_waitcnt vmcnt(5)\n"
    "s_barrier\n"
    "ds_read_b64_tr_b16 a[224:225], v18 offset:24576\n"
    "ds_read_b64_tr_b16 a[226:227], v18 offset:25600\n"
    "v_mfma_f32_32x32x16_bf16 v[176:191], a[232:235], v[116:119], v[176:191]\n"
    "ds_read_b64_tr_b16 a[228:229], v19 offset:24576\n"
    "ds_read_b64_tr_b16 a[230:231], v19 offset:25600\n"
    "v_cvt_pk_bf16_f32 v26, v228, v229\n"
    "v_mfma_f32_32x32x16_bf16 v[192:207], a[236:239], v[116:119], v[192:207]\n"
    "s_waitcnt lgkmcnt(4)\n"
    "ds_read_b64_tr_b16 a[232:233], v18 offset:26624\n"
    "ds_read_b64_tr_b16 a[234:235], v18 offset:27648\n"
    "v_mfma_f32_32x32x16_bf16 v[176:191], a[240:243], v[120:123], v[176:191]\n"
    "ds_read_b64_tr_b16 a[236:237], v19 offset:26624\n"
    "ds_read_b64_tr_b16 a[238:239], v19 offset:27648\n"
    "v_mfma_f32_32x32x16_bf16 v[192:207], a[244:247], v[120:123], v[192:207]\n"
    "ds_read_b64_tr_b16 a[240:241], v18 offset:28672\n"
    "ds_read_b64_tr_b16 a[242:243], v18 offset:29696\n"
    "v_cvt_pk_bf16_f32 v27, v230, v231\n"
    "v_mfma_f32_32x32x16_bf16 v[176:191], a[248:251], v[124:127], v[176:191]\n"
    "ds_read_b64_tr_b16 a[244:245], v19 offset:28672\n"
    "ds_read_b64_tr_b16 a[246:247], v19 offset:29696\n"
    "v_mfma_f32_32x32x16_bf16 v[192:207], a[252:255], v[124:127], v[192:207]\n"
    "s_waitcnt lgkmcnt(4)\n"
    "ds_read_b64_tr_b16 a[248:249], v18 offset:30720\n"
    "ds_read_b64_tr_b16 a[250:251], v18 offset:31744\n"
    "buffer_store_dwordx4 v[24:27], %[vl], %[srdp], s41 offen offset:0\n"
    "v_mfma_f32_32x32x16_bf16 v[176:191], a[224:227], v[128:131], v[176:191]\n"
    "ds_read_b64_tr_b16 a[252:253], v19 offset:30720\n"
    "ds_read_b64_tr_b16 a[254:255], v19 offset:31744\n"
    "v_mfma_f32_32x32x16_bf16 v[192:207], a[228:231], v[128:131], v[192:207]\n"
    "ds_read_b64_tr_b16 a[224:225], v18 offset:32768\n"
    "ds_read_b64_tr_b16 a[226:227], v18 offset:33792\n"
    "v_mfma_f32_32x32x16_bf16 v[176:191], a[232:235], v[132:135], v[176:191]\n"
    "ds_read_b64_tr_b16 a[228:229], v19 offset:32768\n"
    "ds_read_b64_tr_b16 a[230:231], v19 offset:33792\n"
    "v_cvt_pk_bf16_f32 v28, v232, v233\n"
    "v_mfma_f32_32x32x16_bf16 v[192:207], a[236:239], v[132:135], v[192:207]\n"
    "s_waitcnt lgkmcnt(4)\n"
    "ds_read_b64_tr_b16 a[232:233], v18 offset:34816\n"
    "ds_read_b64_tr_b16 a[234:235], v18 offset:35840\n"
    "v_mfma_f32_32x32x16_bf16 v[176:191], a[240:243], v[136:139], v[176:191]\n"
    "ds_read_b64_tr_b16 a[236:237], v19 offset:34816\n"
    "ds_read_b64_tr_b16 a[238:239], v19 offset:35840\n"
    "v_mfma_f32_32x32x16_bf16 v[192:207], a[244:247], v[136:139], v[192:207]\n"
    "ds_read_b64_tr_b16 a[240:241], v18 offset:36864\n"
    "ds_read_b64_tr_b16 a[242:243], v18 offset:37888\n"
    "v_cvt_pk_bf16_f32 v29, v234, v235\n"
    "v_mfma_f32_32x32x16_bf16 v[176:191], a[248:251], v[140:143], v[176:191]\n"
    "ds_read_b64_tr_b16 a[244:245], v19 offset:36864\n"
    "ds_read_b64_tr_b16 a[246:247], v19 offset:37888\n"
    "v_mfma_f32_32x32x16_bf16 v[192:207], a[252:255], v[140:143], v[192:207]\n"
    "s_waitcnt lgkmcnt(4)\n"
    "ds_read_b64_tr_b16 a[248:249], v18 offset:38912\n"
    "ds_read_b64_tr_b16 a[250:251], v18 offset:39936\n"
    "v_cvt_pk_bf16_f32 v30, v236, v237\n"
    "v_mfma_f32_32x32x16_bf16 v[176:191], a[224:227], v[144:147], v[176:191]\n"
    "ds_read_b64_tr_b16 a[252:253], v19 offset:38912\n"
    "ds_read_b64_tr_b16 a[254:255], v19 offset:39936\n"
    "v_mfma_f32_32x32x16_bf16 v[192:207], a[228:231], v[144:147], v[192:207]\n"
    "ds_read_b64_tr_b16 a[224:225], v18 offset:40960\n"
    "ds_read_b64_tr_b16 a[226:227], v18 offset:41984\n"
    "v_mfma_f32_32x32x16_bf16 v[176:191], a[232:235], v[148:151], v[176:191]\n"
    "ds_read_b64_tr_b16 a[228:229], v19 offset:40960\n"
    "ds_read_b64_tr_b16 a[230:231], v19 offset:41984\n"
    "v_cvt_pk_bf16_f32 v31, v238, v239\n"
    "v_mfma_f32_32x32x16_bf16 v[192:207], a[236:239], v[148:151], v[192:207]\n"
    "s_waitcnt lgkmcnt(4)\n"
    "ds_read_b64_tr_b16 a[232:233], v18 offset:43008\n"
    "ds_read_b64_tr_b16 a[234:235], v18 offset:44032\n"
    "v_mfma_f32_32x32x16_bf16 v[176:191], a[240:243], v[152:155], v[176:191]\n"
    "ds_read_b64_tr_b16 a[236:237], v19 offset:43008\n"
    "ds_read_b64_tr_b16 a[238:239], v19 offset:44032\n"
    "v_mfma_f32_32x32x16_bf16 v[192:207], a[244:247], v[152:155], v[192:207]\n"
    "ds_read_b64_tr_b16 a[240:241], v18 offset:45056\n"
    "ds_read_b64_tr_b16 a[242:243], v18 offset:46080\n"
    "buffer_store_dwordx4 v[28:31], %[vl], %[srdp], s41 offen offset:1024\n"
    "v_mfma_f32_32x32x16_bf16 v[176:191], a[248:251], v[156:159], v[176:191]\n"
    "ds_read_b64_tr_b16 a[244:245], v19 offset:45056\n"
    "ds_read_b64_tr_b16 a[246:247], v19 offset:46080\n"
    "v_mfma_f32_32x32x16_bf16 v[192:207], a[252:255], v[156:159], v[192:207]\n"
    "s_waitcnt lgkmcnt(4)\n"
    "ds_read_b64_tr_b16 a[248:249], v18 offset:47104\n"
    "ds_read_b64_tr_b16 a[250:251], v18 offset:48128\n"
    "v_mfma_f32_32x32x16_bf16 v[176:191], a[224:227], v[160:163], v[176:191]\n"
    "ds_read_b64_tr_b16 a[252:253], v19 offset:47104\n"
    "ds_read_b64_tr_b16 a[254:255], v19 offset:48128\n"
    "v_mfma_f32_32x32x16_bf16 v[192:207], a[228:231], v[160:163], v[192:207]\n"
    "v_mfma_f32_32x32x16_bf16 v[176:191], a[232:235], v[164:167], v[176:191]\n"
    "v_mfma_f32_32x32x16_bf16 v[192:207], a[236:239], v[164:167], v[192:207]\n"
    "s_waitcnt lgkmcnt(0)\n"
    "v_mfma_f32_32x32x16_bf16 v[176:191], a[240:243], v[168:171], v[176:191]\n"
    "v_mfma_f32_32x32x16_bf16 v[192:207], a[244:247], v[168:171], v[192:207]\n"
    "v_mfma_f32_32x32x16_bf16 v[176:191], a[248:251], v[172:175], v[176:191]\n"
    "v_mfma_f32_32x32x16_bf16 v[192:207], a[252:255], v[172:175], v[192:207]\n"
    "s_nop 7\n"
    "s_nop 7\n"
    "s_mov_b32 s41, 32768\n"
    "v_cvt_pk_bf16_f32 v24, v176, v177\n"
    "v_cvt_pk_bf16_f32 v25, v178, v179\n"
    "v_cvt_pk_bf16_f32 v26, v180, v181\n"
    "v_cvt_pk_bf16_f32 v27, v182, v183\n"
    "buffer_store_dwordx4 v[24:27], %[vl], %[srdp], s41 offen offset:0\n"
    "v_cvt_pk_bf16_f32 v28, v184, v185\n"
    "v_cvt_pk_bf16_f32 v29, v186, v187\n"
    "v_cvt_pk_bf16_f32 v30, v188, v189\n"
    "v_cvt_pk_bf16_f32 v31, v190, v191\n"
    "buffer_store_dwordx4 v[28:31], %[vl], %[srdp], s41 offen offset:1024\n"
    "s_mov_b32 s41, 34816\n"
    "v_cvt_pk_bf16_f32 v24, v192, v193\n"
    "v_cvt_pk_bf16_f32 v25, v194, v195\n"
    "v_cvt_pk_bf16_f32 v26, v196, v197\n"
    "v_cvt_pk_bf16_f32 v27, v198, v199\n"
    "buffer_store_dwordx4 v[24:27], %[vl], %[srdp], s41 offen offset:0\n"
    "v_cvt_pk_bf16_f32 v28, v200, v201\n"
    "v_cvt_pk_bf16_f32 v29, v202, v203\n"
    "v_cvt_pk_bf16_f32 v30, v204, v205\n"
    "v_cvt_pk_bf16_f32 v31, v206, v207\n"
    "buffer_store_dwordx4 v[28:31], %[vl], %[srdp], s41 offen offset:1024\n"
    "s_waitcnt vmcnt(0)\n"
        :
        : [w1a] "v"(w1a), [w1b] "v"(w1b), [w2a] "v"(w2a), [w2b] "v"(w2b), [rd0] "v"(rd0), [rd1] "v"(rd1), [vb] "v"(lb + 147456 + h * 16), [vl] "v"(l * 16),
          [vx] "v"(xg), [xw] "v"(sb + xw), [xr0] "v"(sb + xr), [xr1] "v"(sb + (xr ^ 32)), [vb1] "v"(l < 36 ? wv * 288 + l * 8 : 0x80000000u),
           [vbw] "v"(l < 36 ? lb + 147456 + wv * 576 + l * 16 : sb + l * 16),
          [srd1] "s"(srd1), [srd2] "s"(srd2), [srdy] "s"(srdy), [srdp] "s"(srdp), [srdq] "s"(srdq), [srdb] "s"(srdb), [ldsw] "s"(lb + wv * 2048), [wv] "s"(wv)
        : "v16", "v17", "v18", "v19", "v20", "v21", "v22", "v23", "v24", "v25", "v26", "v27", "v28", "v29", "v30", "v31", "v32", "v33", "v34", "v35", "v36", "v37", "v38", "v39", "v40", "v41", "v42", "v43", "v44", "v45", "v46", "v47", "v48", "v49", "v50", "v51", "v52", "v53", "v54", "v55", "v56", "v57", "v58", "v59", "v60", "v61", "v62", "v63", "v64", "v65", "v66", "v67", "v68", "v69", "v70", "v71", "v72", "v73", "v74", "v75", "v76", "v77", "v78", "v79", "v80", "v81", "v82", "v83", "v84", "v85", "v86", "v87", "v88", "v89", "v90", "v91", "v92", "v93", "v94", "v95", "v96", "v97", "v98", "v99", "v100", "v101", "v102", "v103", "v104", "v105", "v106", "v107", "v108", "v109", "v110", "v111", "v112", "v113", "v114", "v115", "v116", "v117", "v118", "v119", "v120", "v121", "v122", "v123", "v124", "v125", "v126", "v127", "v128", "v129", "v130", "v131", "v132", "v133", "v134", "v135", "v136", "v137", "v138", "v139", "v140", "v141", "v142", "v143", "v144", "v145", "v146", "v147", "v148", "v149", "v150", "v151", "v152", "v153", "v154", "v155", "v156", "v157", "v158", "v159", "v160", "v161", "v162", "v163", "v164", "v165", "v166", "v167", "v168", "v169", "v170", "v171", "v172", "v173", "v174", "v175", "v176", "v177", "v178", "v179", "v180", "v181", "v182", "v183", "v184", "v185", "v186", "v187", "v188", "v189", "v190", "v191", "v192", "v193", "v194", "v195", "v196", "v197", "v198", "v199", "v200", "v201", "v202", "v203", "v204", "v205", "v206", "v207", "v208", "v209", "v210", "v211", "v212", "v213", "v214", "v215", "v216", "v217", "v218", "v219", "v220", "v221", "v222", "v223", "v224", "v225", "v226", "v227", "v228", "v229", "v230", "v231", "v232", "v233", "v234", "v235", "v236", "v237", "v238", "v239", "v240", "v241", "v242", "v243", "v244", "v245", "v246", "v247", "v248", "v249", "v250", "v251", "v252", "v253", "v254", "v255", "a0", "a1", "a2", "a3", "a4", "a5", "a6", "a7", "a8", "a9", "a10", "a11", "a12", "a13", "a14", "a15", "a16", "a17", "a18", "a19", "a20", "a21", "a22", "a23", "a24", "a25", "a26", "a27", "a28", "a29", "a30", "a31", "a32", "a33", "a34", "a35", "a36", "a37", "a38", "a39", "a40", "a41", "a42", "a43", "a44", "a45", "a46", "a47", "a48", "a49", "a50", "a51", "a52", "a53", "a54", "a55", "a56", "a57", "a58", "a59", "a60", "a61", "a62", "a63", "a64", "a65", "a66", "a67", "a68", "a69", "a70", "a71", "a72", "a73", "a74", "a75", "a76", "a77", "a78", "a79", "a80", "a81", "a82", "a83", "a84", "a85", "a86", "a87", "a88", "a89", "a90", "a91", "a92", "a93", "a94", "a95", "a96", "a97", "a98", "a99", "a100", "a101", "a102", "a103", "a104", "a105", "a106", "a107", "a108", "a109", "a110", "a111", "a112", "a113", "a114", "a115", "a116", "a117", "a118", "a119", "a120", "a121", "a122", "a123", "a124", "a125", "a126", "a127", "a128", "a129", "a130", "a131", "a132", "a133", "a134", "a135", "a136", "a137", "a138", "a139", "a140", "a141", "a142", "a143", "a144", "a145", "a146", "a147", "a148", "a149", "a150", "a151", "a152", "a153", "a154", "a155", "a156", "a157", "a158", "a159", "a160", "a161", "a162", "a163", "a164", "a165", "a166", "a167", "a168", "a169", "a170", "a171", "a172", "a173", "a174", "a175", "a176", "a177", "a178", "a179", "a180", "a181", "a182", "a183", "a184", "a185", "a186", "a187", "a188", "a189", "a190", "a191", "a192", "a193", "a194", "a195", "a196", "a197", "a198", "a199", "a200", "a201", "a202", "a203", "a204", "a205", "a206", "a207", "a208", "a209", "a210", "a211", "a212", "a213", "a214", "a215", "a216", "a217", "a218", "a219", "a220", "a221", "a222", "a223", "a224", "a225", "a226", "a227", "a228", "a229", "a230", "a231", "a232", "a233", "a234", "a235", "a236", "a237", "a238", "a239", "a240", "a241", "a242", "a243", "a244", "a245", "a246", "a247", "a248", "a249", "a250", "a251", "a252", "a253", "a254", "a255", "s40", "s41", "s42", "s43", "scc", "vcc", "memory");
    return;
  }
  const int hg = fs;
  const rsrc_t rw = mkr(ws, OFF_P);
  unsigned tag;
  const u32x4 srdw = make_srd(ws, OFF_P);
  f32x16 xa[9], f;
  u32x4 wfv[2] = {};
  const int f0 = ((tb * 6 + hg - 2) * 4 + wv) * 2;
#define WF_STORE for (int i = 0; i < 2; i++) __builtin_amdgcn_raw_buffer_store_b128(wfv[i], mkr(ws + OFF_WF, 648 * 1024), (f0 + i) * 1024 + l * 16, 0, SC1)
  const unsigned c4 = l & 3, qrow = (wv * 48 + (l >> 2)) * 1152 + hg * 144, sb = lb + 147456 + wv * 4096;
  const unsigned rb = lb + (8 * h + ((l >> 2) & 3)) * 64 + 32 * ((l >> 4) & 1) + 8 * (l & 3);
  const u32x4 srdx = make_srd(x + tt * 18432, 36864), srdq = make_srd(wq, 576 * 1152), srdk = make_srd(hg ? wvv : wk, 576 * 36), srdg = make_srd(gw, 1152);
#define PRO_IN [srdw] "s"(srdw), [vx] "v"(xg), [xw] "v"(sb + xw), [xr0] "v"(sb + xr), [xr1] "v"(sb + (xr ^ 32)), [srdx] "s"(srdx),  Q_IN
#define Q_IN [q0] "v"(qrow + 36 * (c4 >> 1) + 16 * (c4 & 1)), \
      [q1] "v"(qrow + 72 + 36 * (c4 >> 1) + 16 * (c4 & 1)), [q2] "v"(qrow + 36 * c4 + 20), [srdq] "s"(srdq), [ldw] "s"(__builtin_amdgcn_readfirstlane(lb + wv * 3072))
#define PRO_KV [t3] "v"(c4 < 3 ? (wv * 144 + (l >> 2)) * 36 + 16 * c4 : 0x80000000u), [rb3] "v"(rb + 110592), [srdk] "s"(srdk), [ld3] "s"(lb + 110592 + wv * 9216)
  const unsigned rec = OFF_KV + tt * KVREC, hm = -(unsigned)h;
  if (hg == 0) {
    asm volatile(
    "s_nop 4\n"
    "s_mov_b32 s40, 166400\n"
    "buffer_load_dword %[tag], off, %[srdw], s40 sc1\n"
    "s_add_u32 m0, %[ld3], 0\n"
    "v_add_u32 v80, 0, %[t3]\n"
    "buffer_load_dwordx4 v80, %[srdk], 0 offen lds\n"
    "s_add_u32 m0, %[ld3], 1024\n"
    "v_add_u32 v81, 576, %[t3]\n"
    "buffer_load_dwordx4 v81, %[srdk], 0 offen lds\n"
    "s_add_u32 m0, %[ld3], 2048\n"
    "v_add_u32 v82, 1152, %[t3]\n"
    "buffer_load_dwordx4 v82, %[srdk], 0 offen lds\n"
    "s_add_u32 m0, %[ld3], 3072\n"
    "v_add_u32 v83, 1728, %[t3]\n"
    "buffer_load_dwordx4 v83, %[srdk], 0 offen lds\n"
    "s_add_u32 m0, %[ld3], 4096\n"
    "v_add_u32 v84, 2304, %[t3]\n"
    "buffer_load_dwordx4 v84, %[srdk], 0 offen lds\n"
    "s_add_u32 m0, %[ld3], 5120\n"
    "v_add_u32 v85, 2880, %[t3]\n"
    "buffer_load_dwordx4 v85, %[srdk], 0 offen lds\n"
    "s_add_u32 m0, %[ld3], 6144\n"
    "v_add_u32 v86, 3456, %[t3]\n"
    "buffer_load_dwordx4 v86, %[srdk], 0 offen lds\n"
    "s_add_u32 m0, %[ld3], 7168\n"
    "v_add_u32 v87, 4032, %[t3]\n"
    "buffer_load_dwordx4 v87, %[srdk], 0 offen lds\n"
    "s_add_u32 m0, %[ld3], 8192\n"
    "v_add_u32 v88, 4608, %[t3]\n"
    "buffer_load_dwordx4 v88, %[srdk], 0 offen lds\n"
    "buffer_load_ushort v96, %[g0], %[srdg], 0 offen\n"
    "buffer_load_ushort v97, %[g0], %[srdg], 0 offen offset:128\n"
    "buffer_load_ushort v98, %[g2], %[srdg], 0 offen\n"
    "s_mov_b32 s41, 18432\n"
    "buffer_load_dwordx4 v[100:103], %[vx], %[srdx], 0 offen offset:0\n"
    "buffer_load_dwordx4 v[104:107], %[vx], %[srdx], s41 offen offset:0\n"
    "buffer_load_dwordx4 v[108:111], %[vx], %[srdx], 0 offen offset:64\n"
    "buffer_load_dwordx4 v[112:115], %[vx], %[srdx], s41 offen offset:64\n"
    "s_waitcnt vmcnt(4)\n"
    "buffer_load_dwordx4 v[116:119], %[vx], %[srdx], 0 offen offset:128\n"
    "buffer_load_dwordx4 v[120:123], %[vx], %[srdx], s41 offen offset:128\n"
    "buffer_load_dwordx4 v[124:127], %[vx], %[srdx], 0 offen offset:192\n"
    "buffer_load_dwordx4 v[128:131], %[vx], %[srdx], s41 offen offset:192\n"
    "buffer_load_dwordx4 v[132:135], %[vx], %[srdx], 0 offen offset:256\n"
    "buffer_load_dwordx4 v[136:139], %[vx], %[srdx], s41 offen offset:256\n"
    "ds_write_b16 %[gw0], v96\n"
    "ds_write_b16 %[gw0], v97 offset:4096\n"
    "ds_write_b16 %[gw2], v98\n"
    "s_waitcnt lgkmcnt(0)\n"
    "s_barrier\n"
    "s_waitcnt vmcnt(8)\n"
    "ds_write_b128 %[xw], v[100:103] offset:0\n"
    "ds_write_b128 %[xw], v[104:107] offset:1024\n"
    "ds_read_b128 a[64:67], %[xr0] offset:0\n"
    "ds_read_b128 a[68:71], %[xr1] offset:0\n"
    "ds_read_b64_tr_b16 a[0:1], %[rb3] offset:0\n"
    "ds_read_b64_tr_b16 a[2:3], %[rb3] offset:256\n"
    "ds_read_b64_tr_b16 a[4:5], %[rb3] offset:1024\n"
    "ds_read_b64_tr_b16 a[6:7], %[rb3] offset:1280\n"
    "buffer_load_dwordx4 v[140:143], %[vx], %[srdx], 0 offen offset:320\n"
    "buffer_load_dwordx4 v[144:147], %[vx], %[srdx], s41 offen offset:320\n"
    "s_waitcnt vmcnt(8)\n"
    "ds_write_b128 %[xw], v[108:111] offset:2048\n"
    "ds_write_b128 %[xw], v[112:115] offset:3072\n"
    "ds_read_b128 a[72:75], %[xr0] offset:2048\n"
    "ds_read_b128 a[76:79], %[xr1] offset:2048\n"
    "ds_read_b64_tr_b16 a[8:9], %[rb3] offset:2048\n"
    "ds_read_b64_tr_b16 a[10:11], %[rb3] offset:2304\n"
    "ds_read_b64_tr_b16 a[12:13], %[rb3] offset:3072\n"
    "ds_read_b64_tr_b16 a[14:15], %[rb3] offset:3328\n"
    "buffer_load_dwordx4 v[148:151], %[vx], %[srdx], 0 offen offset:384\n"
    "buffer_load_dwordx4 v[152:155], %[vx], %[srdx], s41 offen offset:384\n"
    "s_waitcnt lgkmcnt(8)\n"
    "v_mfma_f32_32x32x16_bf16 v[64:79], a[0:3], a[64:67], 0\n"
    "v_mfma_f32_32x32x16_bf16 v[80:95], a[4:7], a[68:71], 0\n"
    "s_waitcnt vmcnt(8)\n"
    "ds_write_b128 %[xw], v[116:119] offset:0\n"
    "ds_write_b128 %[xw], v[120:123] offset:1024\n"
    "ds_read_b128 a[80:83], %[xr0] offset:0\n"
    "ds_read_b128 a[84:87], %[xr1] offset:0\n"
    "ds_read_b64_tr_b16 a[16:17], %[rb3] offset:4096\n"
    "ds_read_b64_tr_b16 a[18:19], %[rb3] offset:4352\n"
    "ds_read_b64_tr_b16 a[20:21], %[rb3] offset:5120\n"
    "ds_read_b64_tr_b16 a[22:23], %[rb3] offset:5376\n"
    "buffer_load_dwordx4 v[156:159], %[vx], %[srdx], 0 offen offset:448\n"
    "buffer_load_dwordx4 v[160:163], %[vx], %[srdx], s41 offen offset:448\n"
    "s_waitcnt lgkmcnt(8)\n"
    "v_mfma_f32_32x32x16_bf16 v[64:79], a[8:11], a[72:75], v[64:79]\n"
    "v_mfma_f32_32x32x16_bf16 v[80:95], a[12:15], a[76:79], v[80:95]\n"
    "s_waitcnt vmcnt(8)\n"
    "ds_write_b128 %[xw], v[124:127] offset:2048\n"
    "ds_write_b128 %[xw], v[128:131] offset:3072\n"
    "ds_read_b128 a[88:91], %[xr0] offset:2048\n"
    "ds_read_b128 a[92:95], %[xr1] offset:2048\n"
    "ds_read_b64_tr_b16 a[24:25], %[rb3] offset:6144\n"
    "ds_read_b64_tr_b16 a[26:27], %[rb3] offset:6400\n"
    "ds_read_b64_tr_b16 a[28:29], %[rb3] offset:7168\n"
    "ds_read_b64_tr_b16 a[30:31], %[rb3] offset:7424\n"
    "buffer_load_dwordx4 v[164:167], %[vx], %[srdx], 0 offen offset:512\n"
    "buffer_load_dwordx4 v[168:171], %[vx], %[srdx], s41 offen offset:512\n"
    "s_waitcnt lgkmcnt(8)\n"
    "v_mfma_f32_32x32x16_bf16 v[64:79], a[16:19], a[80:83], v[64:79]\n"
    "v_mfma_f32_32x32x16_bf16 v[80:95], a[20:23], a[84:87], v[80:95]\n"
    "s_waitcnt vmcnt(8)\n"
    "ds_write_b128 %[xw], v[132:135] offset:0\n"
    "ds_write_b128 %[xw], v[136:139] offset:1024\n"
    "ds_read_b128 a[96:99], %[xr0] offset:0\n"
    "ds_read_b128 a[100:103], %[xr1] offset:0\n"
    "ds_read_b64_tr_b16 a[0:1], %[rb3] offset:8192\n"
    "ds_read_b64_tr_b16 a[2:3], %[rb3] offset:8448\n"
    "ds_read_b64_tr_b16 a[4:5], %[rb3] offset:9216\n"
    "ds_read_b64_tr_b16 a[6:7], %[rb3] offset:9472\n"
    "buffer_load_dwordx4 v[172:175], %[vx], %[srdx], 0 offen offset:576\n"
    "buffer_load_dwordx4 v[176:179], %[vx], %[srdx], s41 offen offset:576\n"
    "s_waitcnt lgkmcnt(8)\n"
    "v_mfma_f32_32x32x16_bf16 v[64:79], a[24:27], a[88:91], v[64:79]\n"
    "v_mfma_f32_32x32x16_bf16 v[80:95], a[28:31], a[92:95], v[80:95]\n"
    "s_waitcnt vmcnt(8)\n"
    "ds_write_b128 %[xw], v[140:143] offset:2048\n"
    "ds_write_b128 %[xw], v[144:147] offset:3072\n"
    "ds_read_b128 a[104:107], %[xr0] offset:2048\n"
    "ds_read_b128 a[108:111], %[xr1] offset:2048\n"
    "ds_read_b64_tr_b16 a[8:9], %[rb3] offset:10240\n"
    "ds_read_b64_tr_b16 a[10:11], %[rb3] offset:10496\n"
    "ds_read_b64_tr_b16 a[12:13], %[rb3] offset:11264\n"
    "ds_read_b64_tr_b16 a[14:15], %[rb3] offset:11520\n"
    "buffer_load_dwordx4 v[180:183], %[vx], %[srdx], 0 offen offset:640\n"
    "buffer_load_dwordx4 v[184:187], %[vx], %[srdx], s41 offen offset:640\n"
    "s_waitcnt lgkmcnt(8)\n"
    "v_mfma_f32_32x32x16_bf16 v[64:79], a[0:3], a[96:99], v[64:79]\n"
    "v_mfma_f32_32x32x16_bf16 v[80:95], a[4:7], a[100:103], v[80:95]\n"
    "s_waitcnt vmcnt(8)\n"
    "ds_write_b128 %[xw], v[148:151] offset:0\n"
    "ds_write_b128 %[xw], v[152:155] offset:1024\n"
    "ds_read_b128 a[112:115], %[xr0] offset:0\n"
    "ds_read_b128 a[116:119], %[xr1] offset:0\n"
    "ds_read_b64_tr_b16 a[16:17], %[rb3] offset:12288\n"
    "ds_read_b64_tr_b16 a[18:19], %[rb3] offset:12544\n"
    "ds_read_b64_tr_b16 a[20:21], %[rb3] offset:13312\n"
    "ds_read_b64_tr_b16 a[22:23], %[rb3] offset:13568\n"
    "buffer_load_dwordx4 v[188:191], %[vx], %[srdx], 0 offen offset:704\n"
    "buffer_load_dwordx4 v[192:195], %[vx], %[srdx], s41 offen offset:704\n"
    "s_waitcnt lgkmcnt(8)\n"
    "v_mfma_f32_32x32x16_bf16 v[64:79], a[8:11], a[104:107], v[64:79]\n"
    "v_mfma_f32_32x32x16_bf16 v[80:95], a[12:15], a[108:111], v[80:95]\n"
    "s_waitcnt vmcnt(8)\n"
    "ds_write_b128 %[xw], v[156:159] offset:2048\n"
    "ds_write_b128 %[xw], v[160:163] offset:3072\n"
    "ds_read_b128 a[120:123], %[xr0] offset:2048\n"
    "ds_read_b128 a[124:127], %[xr1] offset:2048\n"
    "ds_read_b64_tr_b16 a[24:25], %[rb3] offset:14336\n"
    "ds_read_b64_tr_b16 a[26:27], %[rb3] offset:14592\n"
    "ds_read_b64_tr_b16 a[28:29], %[rb3] offset:15360\n"
    "ds_read_b64_tr_b16 a[30:31], %[rb3] offset:15616\n"
    "buffer_load_dwordx4 v[196:199], %[vx], %[srdx], 0 offen offset:768\n"
    "buffer_load_dwordx4 v[200:203], %[vx], %[srdx], s41 offen offset:768\n"
    "s_waitcnt lgkmcnt(8)\n"
    "v_mfma_f32_32x32x16_bf16 v[64:79], a[16:19], a[112:115], v[64:79]\n"
    "v_mfma_f32_32x32x16_bf16 v[80:95], a[20:23], a[116:119], v[80:95]\n"
    "s_waitcnt vmcnt(8)\n"
    "ds_write_b128 %[xw], v[164:167] offset:0\n"
    "ds_write_b128 %[xw], v[168:171] offset:1024\n"
    "ds_read_b128 a[128:131], %[xr0] offset:0\n"
    "ds_read_b128 a[132:135], %[xr1] offset:0\n"
    "ds_read_b64_tr_b16 a[0:1], %[rb3] offset:16384\n"
    "ds_read_b64_tr_b16 a[2:3], %[rb3] offset:16640\n"
    "ds_read_b64_tr_b16 a[4:5], %[rb3] offset:17408\n"
    "ds_read_b64_tr_b16 a[6:7], %[rb3] offset:17664\n"
    "buffer_load_dwordx4 v[204:207], %[vx], %[srdx], 0 offen offset:832\n"
    "buffer_load_dwordx4 v[208:211], %[vx], %[srdx], s41 offen offset:832\n"
    "s_waitcnt lgkmcnt(8)\n"
    "v_mfma_f32_32x32x16_bf16 v[64:79], a[24:27], a[120:123], v[64:79]\n"
    "v_mfma_f32_32x32x16_bf16 v[80:95], a[28:31], a[124:127], v[80:95]\n"
    "s_waitcnt vmcnt(8)\n"
    "ds_write_b128 %[xw], v[172:175] offset:2048\n"
    "ds_write_b128 %[xw], v[176:179] offset:3072\n"
    "ds_read_b128 a[136:139], %[xr0] offset:2048\n"
    "ds_read_b128 a[140:143], %[xr1] offset:2048\n"
    "ds_read_b64_tr_b16 a[8:9], %[rb3] offset:18432\n"
    "ds_read_b64_tr_b16 a[10:11], %[rb3] offset:18688\n"
    "ds_read_b64_tr_b16 a[12:13], %[rb3] offset:19456\n"
    "ds_read_b64_tr_b16 a[14:15], %[rb3] offset:19712\n"
    "buffer_load_dwordx4 v[212:215], %[vx], %[srdx], 0 offen offset:896\n"
    "buffer_load_dwordx4 v[216:219], %[vx], %[srdx], s41 offen offset:896\n"
    "s_waitcnt lgkmcnt(8)\n"
    "v_mfma_f32_32x32x16_bf16 v[64:79], a[0:3], a[128:131], v[64:79]\n"
    "v_mfma_f32_32x32x16_bf16 v[80:95], a[4:7], a[132:135], v[80:95]\n"
    "s_waitcnt vmcnt(8)\n"
    "ds_write_b128 %[xw], v[180:183] offset:0\n"
    "ds_write_b128 %[xw], v[184:187] offset:1024\n"
    "ds_read_b128 a[144:147], %[xr0] offset:0\n"
    "ds_read_b128 a[148:151], %[xr1] offset:0\n"
    "ds_read_b64_tr_b16 a[16:17], %[rb3] offset:20480\n"
    "ds_read_b64_tr_b16 a[18:19], %[rb3] offset:20736\n"
    "ds_read_b64_tr_b16 a[20:21], %[rb3] offset:21504\n"
    "ds_read_b64_tr_b16 a[22:23], %[rb3] offset:21760\n"
    "buffer_load_dwordx4 v[220:223], %[vx], %[srdx], 0 offen offset:960\n"
    "buffer_load_dwordx4 v[224:227], %[vx], %[srdx], s41 offen offset:960\n"
    "s_waitcnt lgkmcnt(8)\n"
    "v_mfma_f32_32x32x16_bf16 v[64:79], a[8:11], a[136:139], v[64:79]\n"
    "v_mfma_f32_32x32x16_bf16 v[80:95], a[12:15], a[140:143], v[80:95]\n"
    "s_waitcnt vmcnt(8)\n"
    "ds_write_b128 %[xw], v[188:191] offset:2048\n"
    "ds_write_b128 %[xw], v[192:195] offset:3072\n"
    "ds_read_b128 a[152:155], %[xr0] offset:2048\n"
    "ds_read_b128 a[156:159], %[xr1] offset:2048\n"
    "ds_read_b64_tr_b16 a[24:25], %[rb3] offset:22528\n"
    "ds_read_b64_tr_b16 a[26:27], %[rb3] offset:22784\n"
    "ds_read_b64_tr_b16 a[28:29], %[rb3] offset:23552\n"
    "ds_read_b64_tr_b16 a[30:31], %[rb3] offset:23808\n"
    "buffer_load_dwordx4 v[228:231], %[vx], %[srdx], 0 offen offset:1024\n"
    "buffer_load_dwordx4 v[232:235], %[vx], %[srdx], s41 offen offset:1024\n"
    "s_waitcnt lgkmcnt(8)\n"
    "v_mfma_f32_32x32x16_bf16 v[64:79], a[16:19], a[144:147], v[64:79]\n"
    "v_mfma_f32_32x32x16_bf16 v[80:95], a[20:23], a[148:151], v[80:95]\n"
    "s_waitcnt vmcnt(8)\n"
    "ds_write_b128 %[xw], v[196:199] offset:0\n"
    "ds_write_b128 %[xw], v[200:203] offset:1024\n"
    "ds_read_b128 a[160:163], %[xr0] offset:0\n"
    "ds_read_b128 a[164:167], %[xr1] offset:0\n"
    "ds_read_b64_tr_b16 a[0:1], %[rb3] offset:24576\n"
    "ds_read_b64_tr_b16 a[2:3], %[rb3] offset:24832\n"
    "ds_read_b64_tr_b16 a[4:5], %[rb3] offset:25600\n"
    "ds_read_b64_tr_b16 a[6:7], %[rb3] offset:25856\n"
    "buffer_load_dwordx4 v[236:239], %[vx], %[srdx], 0 offen offset:1088\n"
    "buffer_load_dwordx4 v[240:243], %[vx], %[srdx], s41 offen offset:1088\n"
    "s_waitcnt lgkmcnt(8)\n"
    "v_mfma_f32_32x32x16_bf16 v[64:79], a[24:27], a[152:155], v[64:79]\n"
    "v_mfma_f32_32x32x16_bf16 v[80:95], a[28:31], a[156:159], v[80:95]\n"
    "s_waitcnt vmcnt(8)\n"
    "ds_write_b128 %[xw], v[204:207] offset:2048\n"
    "ds_write_b128 %[xw], v[208:211] offset:3072\n"
    "ds_read_b128 a[168:171], %[xr0] offset:2048\n"
    "ds_read_b128 a[172:175], %[xr1] offset:2048\n"
    "ds_read_b64_tr_b16 a[8:9], %[rb3] offset:26624\n"
    "ds_read_b64_tr_b16 a[10:11], %[rb3] offset:26880\n"
    "ds_read_b64_tr_b16 a[12:13], %[rb3] offset:27648\n"
    "ds_read_b64_tr_b16 a[14:15], %[rb3] offset:27904\n"
    "s_add_u32 m0, %[ldw], 0\n"
    "s_mov_b32 s40, 0\n"
    "buffer_load_dwordx4 %[q0], %[srdq], s40 offen lds\n"
    "s_add_u32 m0, %[ldw], 36864\n"
    "s_mov_b32 s40, 0\n"
    "buffer_load_dwordx4 %[q1], %[srdq], s40 offen lds\n"
    "s_waitcnt lgkmcnt(8)\n"
    "v_mfma_f32_32x32x16_bf16 v[64:79], a[0:3], a[160:163], v[64:79]\n"
    "v_mfma_f32_32x32x16_bf16 v[80:95], a[4:7], a[164:167], v[80:95]\n"
    "s_waitcnt vmcnt(8)\n"
    "ds_write_b128 %[xw], v[212:215] offset:0\n"
    "ds_write_b128 %[xw], v[216:219] offset:1024\n"
    "ds_read_b128 a[176:179], %[xr0] offset:0\n"
    "ds_read_b128 a[180:183], %[xr1] offset:0\n"
    "ds_read_b64_tr_b16 a[16:17], %[rb3] offset:28672\n"
    "ds_read_b64_tr_b16 a[18:19], %[rb3] offset:28928\n"
    "ds_read_b64_tr_b16 a[20:21], %[rb3] offset:29696\n"
    "ds_read_b64_tr_b16 a[22:23], %[rb3] offset:29952\n"
    "s_add_u32 m0, %[ldw], 73728\n"
    "s_mov_b32 s40, 0\n"
    "buffer_load_dwordx4 %[q2], %[srdq], s40 offen lds\n"
    "s_add_u32 m0, %[ldw], 1024\n"
    "s_mov_b32 s40, 18432\n"
    "buffer_load_dwordx4 %[q0], %[srdq], s40 offen lds\n"
    "s_waitcnt lgkmcnt(8)\n"
    "v_mfma_f32_32x32x16_bf16 v[64:79], a[8:11], a[168:171], v[64:79]\n"
    "v_mfma_f32_32x32x16_bf16 v[80:95], a[12:15], a[172:175], v[80:95]\n"
    "s_waitcnt vmcnt(8)\n"
    "ds_write_b128 %[xw], v[220:223] offset:2048\n"
    "ds_write_b128 %[xw], v[224:227] offset:3072\n"
    "ds_read_b128 a[184:187], %[xr0] offset:2048\n"
    "ds_read_b128 a[188:191], %[xr1] offset:2048\n"
    "ds_read_b64_tr_b16 a[24:25], %[rb3] offset:30720\n"
    "ds_read_b64_tr_b16 a[26:27], %[rb3] offset:30976\n"
    "ds_read_b64_tr_b16 a[28:29], %[rb3] offset:31744\n"
    "ds_read_b64_tr_b16 a[30:31], %[rb3] offset:32000\n"
    "s_add_u32 m0, %[ldw], 37888\n"
    "s_mov_b32 s40, 18432\n"
    "buffer_load_dwordx4 %[q1], %[srdq], s40 offen lds\n"
    "s_add_u32 m0, %[ldw], 74752\n"
    "s_mov_b32 s40, 18432\n"
    "buffer_load_dwordx4 %[q2], %[srdq], s40 offen lds\n"
    "s_waitcnt lgkmcnt(8)\n"
    "v_mfma_f32_32x32x16_bf16 v[64:79], a[16:19], a[176:179], v[64:79]\n"
    "v_mfma_f32_32x32x16_bf16 v[80:95], a[20:23], a[180:183], v[80:95]\n"
    "s_waitcnt vmcnt(8)\n"
    "ds_write_b128 %[xw], v[228:231] offset:0\n"
    "ds_write_b128 %[xw], v[232:235] offset:1024\n"
    "ds_read_b128 a[192:195], %[xr0] offset:0\n"
    "ds_read_b128 a[196:199], %[xr1] offset:0\n"
    "ds_read_b64_tr_b16 a[0:1], %[rb3] offset:32768\n"
    "ds_read_b64_tr_b16 a[2:3], %[rb3] offset:33024\n"
    "ds_read_b64_tr_b16 a[4:5], %[rb3] offset:33792\n"
    "ds_read_b64_tr_b16 a[6:7], %[rb3] offset:34048\n"
    "s_add_u32 m0, %[ldw], 2048\n"
    "s_mov_b32 s40, 36864\n"
    "buffer_load_dwordx4 %[q0], %[srdq], s40 offen lds\n"
    "s_add_u32 m0, %[ldw], 38912\n"
    "s_mov_b32 s40, 36864\n"
    "buffer_load_dwordx4 %[q1], %[srdq], s40 offen lds\n"
    "s_waitcnt lgkmcnt(8)\n"
    "v_mfma_f32_32x32x16_bf16 v[64:79], a[24:27], a[184:187], v[64:79]\n"
    "v_mfma_f32_32x32x16_bf16 v[80:95], a[28:31], a[188:191], v[80:95]\n"
    "s_waitcnt vmcnt(8)\n"
    "ds_write_b128 %[xw], v[236:239] offset:2048\n"
    "ds_write_b128 %[xw], v[240:243] offset:3072\n"
    "ds_read_b128 a[200:203], %[xr0] offset:2048\n"
    "ds_read_b128 a[204:207], %[xr1] offset:2048\n"
    "ds_read_b64_tr_b16 a[8:9], %[rb3] offset:34816\n"
    "ds_read_b64_tr_b16 a[10:11], %[rb3] offset:35072\n"
    "ds_read_b64_tr_b16 a[12:13], %[rb3] offset:35840\n"
    "ds_read_b64_tr_b16 a[14:15], %[rb3] offset:36096\n"
    "s_add_u32 m0, %[ldw], 75776\n"
    "s_mov_b32 s40, 36864\n"
    "buffer_load_dwordx4 %[q2], %[srdq], s40 offen lds\n"
    "s_add_u32 m0, %[ldw], 12288\n"
    "s_mov_b32 s40, 221184\n"
    "buffer_load_dwordx4 %[q0], %[srdq], s40 offen lds\n"
    "s_waitcnt lgkmcnt(8)\n"
    "v_mfma_f32_32x32x16_bf16 v[64:79], a[0:3], a[192:195], v[64:79]\n"
    "v_mfma_f32_32x32x16_bf16 v[80:95], a[4:7], a[196:199], v[80:95]\n"
    "s_waitcnt lgkmcnt(0)\n"
    "v_mfma_f32_32x32x16_bf16 v[64:79], a[8:11], a[200:203], v[64:79]\n"
    "v_mfma_f32_32x32x16_bf16 v[80:95], a[12:15], a[204:207], v[80:95]\n"
    "s_nop 7\n"
    "s_nop 7\n"
    "v_add_f32 v64, v64, v80\n"
    "v_add_f32 v65, v65, v81\n"
    "v_add_f32 v66, v66, v82\n"
    "v_add_f32 v67, v67, v83\n"
    "v_add_f32 v68, v68, v84\n"
    "v_add_f32 v69, v69, v85\n"
    "v_add_f32 v70, v70, v86\n"
    "v_add_f32 v71, v71, v87\n"
    "v_add_f32 v72, v72, v88\n"
    "v_add_f32 v73, v73, v89\n"
    "v_add_f32 v74, v74, v90\n"
    "v_add_f32 v75, v75, v91\n"
    "v_add_f32 v76, v76, v92\n"
    "v_add_f32 v77, v77, v93\n"
    "v_add_f32 v78, v78, v94\n"
    "v_add_f32 v79, v79, v95\n"
        : [tag] "=&v"(tag), "={v[64:79]}"(f), "={a[64:79]}"(xa[0]), "={a[80:95]}"(xa[1]), "={a[96:111]}"(xa[2]), "={a[112:127]}"(xa[3]), "={a[128:143]}"(xa[4]), "={a[144:159]}"(xa[5]), "={a[160:175]}"(xa[6]), "={a[176:191]}"(xa[7]), "={a[192:207]}"(xa[8])
        : PRO_IN, PRO_KV, [g0] "v"((wv * 144 + l) * 2), [g2] "v"(l < 16 ? (wv * 144 + 128 + l) * 2 : 0x80000000u), [srdg] "s"(srdg),
          [gw0] "v"(lb + 110592 + (wv * 144 + l) * 64 + 48), [gw2] "v"(l < 16 ? lb + 110592 + (wv * 144 + 128 + l) * 64 + 48 : sb + l * 16)
        : "v80", "v81", "v82", "v83", "v84", "v85", "v86", "v87", "v88", "v89", "v90", "v91", "v92", "v93", "v94", "v95", "v96", "v97", "v98", "v99", "v100", "v101", "v102", "v103", "v104", "v105", "v106", "v107", "v108", "v109", "v110", "v111", "v112", "v113", "v114", "v115", "v116", "v117", "v118", "v119", "v120", "v121", "v122", "v123", "v124", "v125", "v126", "v127", "v128", "v129", "v130", "v131", "v132", "v133", "v134", "v135", "v136", "v137", "v138", "v139", "v140", "v141", "v142", "v143", "v144", "v145", "v146", "v147", "v148", "v149", "v150", "v151", "v152", "v153", "v154", "v155", "v156", "v157", "v158", "v159", "v160", "v161", "v162", "v163", "v164", "v165", "v166", "v167", "v168", "v169", "v170", "v171", "v172", "v173", "v174", "v175", "v176", "v177", "v178", "v179", "v180", "v181", "v182", "v183", "v184", "v185", "v186", "v187", "v188", "v189", "v190", "v191", "v192", "v193", "v194", "v195", "v196", "v197", "v198", "v199", "v200", "v201", "v202", "v203", "v204", "v205", "v206", "v207", "v208", "v209", "v210", "v211", "v212", "v213", "v214", "v215", "v216", "v217", "v218", "v219", "v220", "v221", "v222", "v223", "v224", "v225", "v226", "v227", "v228", "v229", "v230", "v231", "v232", "v233", "v234", "v235", "v236", "v237", "v238", "v239", "v240", "v241", "v242", "v243", "a0", "a1", "a2", "a3", "a4", "a5", "a6", "a7", "a8", "a9", "a10", "a11", "a12", "a13", "a14", "a15", "a16", "a17", "a18", "a19", "a20", "a21", "a22", "a23", "a24", "a25", "a26", "a27", "a28", "a29", "a30", "a31", "s40", "s41", "scc", "memory");
    const u32x4 p0 = cvt8(f, 0, QSCALE);
    const u32x2 k1 = {cvtpk(f[8] * QSCALE, f[9] * QSCALE), 0x00003f80u};
    __builtin_amdgcn_raw_buffer_store_b128(p0, rw, rec + l * 16, 0, SC1);
    __builtin_amdgcn_raw_buffer_store_b64(k1, rw, h ? 0x80000000u : rec + 1024 + q * 8, 0, SC1);
    __builtin_amdgcn_raw_buffer_store_b32(__float_as_uint(1.f / (1.f + __expf(-f[12]))), rw, h ? 0x80000000u : OFF_G + (tt * 32 + q) * 4, 0, 0);
    float n2 = sq2(k1[0] & ~hm, 0.f);
#pragma unroll
    for (int e = 0; e < 4; e++) n2 = sq2(p0[e], n2);
    n2 = xhalf_sum(n2);
    n2 = n2 != n2 ? __builtin_inff() : n2;
    n2 = half_max(__float_as_uint(n2));
    if (l == 0) *(float*)(lds + 147456 + wv * 4096) = n2;
  } else if (hg == 1) {
    asm volatile(
    "s_nop 4\n"
    "s_mov_b32 s40, 166400\n"
    "buffer_load_dword %[tag], off, %[srdw], s40 sc1\n"
    "s_add_u32 m0, %[ld3], 0\n"
    "v_add_u32 v80, 0, %[t3]\n"
    "buffer_load_dwordx4 v80, %[srdk], 0 offen lds\n"
    "s_add_u32 m0, %[ld3], 1024\n"
    "v_add_u32 v81, 576, %[t3]\n"
    "buffer_load_dwordx4 v81, %[srdk], 0 offen lds\n"
    "s_add_u32 m0, %[ld3], 2048\n"
    "v_add_u32 v82, 1152, %[t3]\n"
    "buffer_load_dwordx4 v82, %[srdk], 0 offen lds\n"
    "s_add_u32 m0, %[ld3], 3072\n"
    "v_add_u32 v83, 1728, %[t3]\n"
    "buffer_load_dwordx4 v83, %[srdk], 0 offen lds\n"
    "s_add_u32 m0, %[ld3], 4096\n"
    "v_add_u32 v84, 2304, %[t3]\n"
    "buffer_load_dwordx4 v84, %[srdk], 0 offen lds\n"
    "s_add_u32 m0, %[ld3], 5120\n"
    "v_add_u32 v85, 2880, %[t3]\n"
    "buffer_load_dwordx4 v85, %[srdk], 0 offen lds\n"
    "s_add_u32 m0, %[ld3], 6144\n"
    "v_add_u32 v86, 3456, %[t3]\n"
    "buffer_load_dwordx4 v86, %[srdk], 0 offen lds\n"
    "s_add_u32 m0, %[ld3], 7168\n"
    "v_add_u32 v87, 4032, %[t3]\n"
    "buffer_load_dwordx4 v87, %[srdk], 0 offen lds\n"
    "s_add_u32 m0, %[ld3], 8192\n"
    "v_add_u32 v88, 4608, %[t3]\n"
    "buffer_load_dwordx4 v88, %[srdk], 0 offen lds\n"
    "s_mov_b32 s41, 18432\n"
    "buffer_load_dwordx4 v[100:103], %[vx], %[srdx], 0 offen offset:0\n"
    "buffer_load_dwordx4 v[104:107], %[vx], %[srdx], s41 offen offset:0\n"
    "buffer_load_dwordx4 v[108:111], %[vx], %[srdx], 0 offen offset:64\n"
    "buffer_load_dwordx4 v[112:115], %[vx], %[srdx], s41 offen offset:64\n"
    "s_waitcnt vmcnt(4)\n"
    "buffer_load_dwordx4 v[116:119], %[vx], %[srdx], 0 offen offset:128\n"
    "buffer_load_dwordx4 v[120:123], %[vx], %[srdx], s41 offen offset:128\n"
    "buffer_load_dwordx4 v[124:127], %[vx], %[srdx], 0 offen offset:192\n"
    "buffer_load_dwordx4 v[128:131], %[vx], %[srdx], s41 offen offset:192\n"
    "buffer_load_dwordx4 v[132:135], %[vx], %[srdx], 0 offen offset:256\n"
    "buffer_load_dwordx4 v[136:139], %[vx], %[srdx], s41 offen offset:256\n"
    "s_barrier\n"
    "s_waitcnt vmcnt(8)\n"
    "ds_write_b128 %[xw], v[100:103] offset:0\n"
    "ds_write_b128 %[xw], v[104:107] offset:1024\n"
    "ds_read_b128 a[64:67], %[xr0] offset:0\n"
    "ds_read_b128 a[68:71], %[xr1] offset:0\n"
    "ds_read_b64_tr_b16 a[0:1], %[rb3] offset:0\n"
    "ds_read_b64_tr_b16 a[2:3], %[rb3] offset:256\n"
    "ds_read_b64_tr_b16 a[4:5], %[rb3] offset:1024\n"
    "ds_read_b64_tr_b16 a[6:7], %[rb3] offset:1280\n"
    "buffer_load_dwordx4 v[140:143], %[vx], %[srdx], 0 offen offset:320\n"
    "buffer_load_dwordx4 v[144:147], %[vx], %[srdx], s41 offen offset:320\n"
    "s_waitcnt vmcnt(8)\n"
    "ds_write_b128 %[xw], v[108:111] offset:2048\n"
    "ds_write_b128 %[xw], v[112:115] offset:3072\n"
    "ds_read_b128 a[72:75], %[xr0] offset:2048\n"
    "ds_read_b128 a[76:79], %[xr1] offset:2048\n"
    "ds_read_b64_tr_b16 a[8:9], %[rb3] offset:2048\n"
    "ds_read_b64_tr_b16 a[10:11], %[rb3] offset:2304\n"
    "ds_read_b64_tr_b16 a[12:13], %[rb3] offset:3072\n"
    "ds_read_b64_tr_b16 a[14:15], %[rb3] offset:3328\n"
    "buffer_load_dwordx4 v[148:151], %[vx], %[srdx], 0 offen offset:384\n"
    "buffer_load_dwordx4 v[152:155], %[vx], %[srdx], s41 offen offset:384\n"
    "s_waitcnt lgkmcnt(8)\n"
    "v_mfma_f32_32x32x16_bf16 v[64:79], a[64:67], a[0:3], 0\n"
    "v_mfma_f32_32x32x16_bf16 v[80:95], a[68:71], a[4:7], 0\n"
    "s_waitcnt vmcnt(8)\n"
    "ds_write_b128 %[xw], v[116:119] offset:0\n"
    "ds_write_b128 %[xw], v[120:123] offset:1024\n"
    "ds_read_b128 a[80:83], %[xr0] offset:0\n"
    "ds_read_b128 a[84:87], %[xr1] offset:0\n"
    "ds_read_b64_tr_b16 a[16:17], %[rb3] offset:4096\n"
    "ds_read_b64_tr_b16 a[18:19], %[rb3] offset:4352\n"
    "ds_read_b64_tr_b16 a[20:21], %[rb3] offset:5120\n"
    "ds_read_b64_tr_b16 a[22:23], %[rb3] offset:5376\n"
    "buffer_load_dwordx4 v[156:159], %[vx], %[srdx], 0 offen offset:448\n"
    "buffer_load_dwordx4 v[160:163], %[vx], %[srdx], s41 offen offset:448\n"
    "s_waitcnt lgkmcnt(8)\n"
    "v_mfma_f32_32x32x16_bf16 v[64:79], a[72:75], a[8:11], v[64:79]\n"
    "v_mfma_f32_32x32x16_bf16 v[80:95], a[76:79], a[12:15], v[80:95]\n"
    "s_waitcnt vmcnt(8)\n"
    "ds_write_b128 %[xw], v[124:127] offset:2048\n"
    "ds_write_b128 %[xw], v[128:131] offset:3072\n"
    "ds_read_b128 a[88:91], %[xr0] offset:2048\n"
    "ds_read_b128 a[92:95], %[xr1] offset:2048\n"
    "ds_read_b64_tr_b16 a[24:25], %[rb3] offset:6144\n"
    "ds_read_b64_tr_b16 a[26:27], %[rb3] offset:6400\n"
    "ds_read_b64_tr_b16 a[28:29], %[rb3] offset:7168\n"
    "ds_read_b64_tr_b16 a[30:31], %[rb3] offset:7424\n"
    "buffer_load_dwordx4 v[164:167], %[vx], %[srdx], 0 offen offset:512\n"
    "buffer_load_dwordx4 v[168:171], %[vx], %[srdx], s41 offen offset:512\n"
    "s_waitcnt lgkmcnt(8)\n"
    "v_mfma_f32_32x32x16_bf16 v[64:79], a[80:83], a[16:19], v[64:79]\n"
    "v_mfma_f32_32x32x16_bf16 v[80:95], a[84:87], a[20:23], v[80:95]\n"
    "s_waitcnt vmcnt(8)\n"
    "ds_write_b128 %[xw], v[132:135] offset:0\n"
    "ds_write_b128 %[xw], v[136:139] offset:1024\n"
    "ds_read_b128 a[96:99], %[xr0] offset:0\n"
    "ds_read_b128 a[100:103], %[xr1] offset:0\n"
    "ds_read_b64_tr_b16 a[0:1], %[rb3] offset:8192\n"
    "ds_read_b64_tr_b16 a[2:3], %[rb3] offset:8448\n"
    "ds_read_b64_tr_b16 a[4:5], %[rb3] offset:9216\n"
    "ds_read_b64_tr_b16 a[6:7], %[rb3] offset:9472\n"
    "buffer_load_dwordx4 v[172:175], %[vx], %[srdx], 0 offen offset:576\n"
    "buffer_load_dwordx4 v[176:179], %[vx], %[srdx], s41 offen offset:576\n"
    "s_waitcnt lgkmcnt(8)\n"
    "v_mfma_f32_32x32x16_bf16 v[64:79], a[88:91], a[24:27], v[64:79]\n"
    "v_mfma_f32_32x32x16_bf16 v[80:95], a[92:95], a[28:31], v[80:95]\n"
    "s_waitcnt vmcnt(8)\n"
    "ds_write_b128 %[xw], v[140:143] offset:2048\n"
    "ds_write_b128 %[xw], v[144:147] offset:3072\n"
    "ds_read_b128 a[104:107], %[xr0] offset:2048\n"
    "ds_read_b128 a[108:111], %[xr1] offset:2048\n"
    "ds_read_b64_tr_b16 a[8:9], %[rb3] offset:10240\n"
    "ds_read_b64_tr_b16 a[10:11], %[rb3] offset:10496\n"
    "ds_read_b64_tr_b16 a[12:13], %[rb3] offset:11264\n"
    "ds_read_b64_tr_b16 a[14:15], %[rb3] offset:11520\n"
    "buffer_load_dwordx4 v[180:183], %[vx], %[srdx], 0 offen offset:640\n"
    "buffer_load_dwordx4 v[184:187], %[vx], %[srdx], s41 offen offset:640\n"
    "s_waitcnt lgkmcnt(8)\n"
    "v_mfma_f32_32x32x16_bf16 v[64:79], a[96:99], a[0:3], v[64:79]\n"
    "v_mfma_f32_32x32x16_bf16 v[80:95], a[100:103], a[4:7], v[80:95]\n"
    "s_waitcnt vmcnt(8)\n"
    "ds_write_b128 %[xw], v[148:151] offset:0\n"
    "ds_write_b128 %[xw], v[152:155] offset:1024\n"
    "ds_read_b128 a[112:115], %[xr0] offset:0\n"
    "ds_read_b128 a[116:119], %[xr1] offset:0\n"
    "ds_read_b64_tr_b16 a[16:17], %[rb3] offset:12288\n"
    "ds_read_b64_tr_b16 a[18:19], %[rb3] offset:12544\n"
    "ds_read_b64_tr_b16 a[20:21], %[rb3] offset:13312\n"
    "ds_read_b64_tr_b16 a[22:23], %[rb3] offset:13568\n"
    "buffer_load_dwordx4 v[188:191], %[vx], %[srdx], 0 offen offset:704\n"
    "buffer_load_dwordx4 v[192:195], %[vx], %[srdx], s41 offen offset:704\n"
    "s_waitcnt lgkmcnt(8)\n"
    "v_mfma_f32_32x32x16_bf16 v[64:79], a[104:107], a[8:11], v[64:79]\n"
    "v_mfma_f32_32x32x16_bf16 v[80:95], a[108:111], a[12:15], v[80:95]\n"
    "s_waitcnt vmcnt(8)\n"
    "ds_write_b128 %[xw], v[156:159] offset:2048\n"
    "ds_write_b128 %[xw], v[160:163] offset:3072\n"
    "ds_read_b128 a[120:123], %[xr0] offset:2048\n"
    "ds_read_b128 a[124:127], %[xr1] offset:2048\n"
    "ds_read_b64_tr_b16 a[24:25], %[rb3] offset:14336\n"
    "ds_read_b64_tr_b16 a[26:27], %[rb3] offset:14592\n"
    "ds_read_b64_tr_b16 a[28:29], %[rb3] offset:15360\n"
    "ds_read_b64_tr_b16 a[30:31], %[rb3] offset:15616\n"
    "buffer_load_dwordx4 v[196:199], %[vx], %[srdx], 0 offen offset:768\n"
    "buffer_load_dwordx4 v[200:203], %[vx], %[srdx], s41 offen offset:768\n"
    "s_waitcnt lgkmcnt(8)\n"
    "v_mfma_f32_32x32x16_bf16 v[64:79], a[112:115], a[16:19], v[64:79]\n"
    "v_mfma_f32_32x32x16_bf16 v[80:95], a[116:119], a[20:23], v[80:95]\n"
    "s_waitcnt vmcnt(8)\n"
    "ds_write_b128 %[xw], v[164:167] offset:0\n"
    "ds_write_b128 %[xw], v[168:171] offset:1024\n"
    "ds_read_b128 a[128:131], %[xr0] offset:0\n"
    "ds_read_b128 a[132:135], %[xr1] offset:0\n"
    "ds_read_b64_tr_b16 a[0:1], %[rb3] offset:16384\n"
    "ds_read_b64_tr_b16 a[2:3], %[rb3] offset:16640\n"
    "ds_read_b64_tr_b16 a[4:5], %[rb3] offset:17408\n"
    "ds_read_b64_tr_b16 a[6:7], %[rb3] offset:17664\n"
    "buffer_load_dwordx4 v[204:207], %[vx], %[srdx], 0 offen offset:832\n"
    "buffer_load_dwordx4 v[208:211], %[vx], %[srdx], s41 offen offset:832\n"
    "s_waitcnt lgkmcnt(8)\n"
    "v_mfma_f32_32x32x16_bf16 v[64:79], a[120:123], a[24:27], v[64:79]\n"
    "v_mfma_f32_32x32x16_bf16 v[80:95], a[124:127], a[28:31], v[80:95]\n"
    "s_waitcnt vmcnt(8)\n"
    "ds_write_b128 %[xw], v[172:175] offset:2048\n"
    "ds_write_b128 %[xw], v[176:179] offset:3072\n"
    "ds_read_b128 a[136:139], %[xr0] offset:2048\n"
    "ds_read_b128 a[140:143], %[xr1] offset:2048\n"
    "ds_read_b64_tr_b16 a[8:9], %[rb3] offset:18432\n"
    "ds_read_b64_tr_b16 a[10:11], %[rb3] offset:18688\n"
    "ds_read_b64_tr_b16 a[12:13], %[rb3] offset:19456\n"
    "ds_read_b64_tr_b16 a[14:15], %[rb3] offset:19712\n"
    "buffer_load_dwordx4 v[212:215], %[vx], %[srdx], 0 offen offset:896\n"
    "buffer_load_dwordx4 v[216:219], %[vx], %[srdx], s41 offen offset:896\n"
    "s_waitcnt lgkmcnt(8)\n"
    "v_mfma_f32_32x32x16_bf16 v[64:79], a[128:131], a[0:3], v[64:79]\n"
    "v_mfma_f32_32x32x16_bf16 v[80:95], a[132:135], a[4:7], v[80:95]\n"
    "s_waitcnt vmcnt(8)\n"
    "ds_write_b128 %[xw], v[180:183] offset:0\n"
    "ds_write_b128 %[xw], v[184:187] offset:1024\n"
    "ds_read_b128 a[144:147], %[xr0] offset:0\n"
    "ds_read_b128 a[148:151], %[xr1] offset:0\n"
    "ds_read_b64_tr_b16 a[16:17], %[rb3] offset:20480\n"
    "ds_read_b64_tr_b16 a[18:19], %[rb3] offset:20736\n"
    "ds_read_b64_tr_b16 a[20:21], %[rb3] offset:21504\n"
    "ds_read_b64_tr_b16 a[22:23], %[rb3] offset:21760\n"
    "buffer_load_dwordx4 v[220:223], %[vx], %[srdx], 0 offen offset:960\n"
    "buffer_load_dwordx4 v[224:227], %[vx], %[srdx], s41 offen offset:960\n"
    "s_waitcnt lgkmcnt(8)\n"
    "v_mfma_f32_32x32x16_bf16 v[64:79], a[136:139], a[8:11], v[64:79]\n"
    "v_mfma_f32_32x32x16_bf16 v[80:95], a[140:143], a[12:15], v[80:95]\n"
    "s_waitcnt vmcnt(8)\n"
    "ds_write_b128 %[xw], v[188:191] offset:2048\n"
    "ds_write_b128 %[xw], v[192:195] offset:3072\n"
    "ds_read_b128 a[152:155], %[xr0] offset:2048\n"
    "ds_read_b128 a[156:159], %[xr1] offset:2048\n"
    "ds_read_b64_tr_b16 a[24:25], %[rb3] offset:22528\n"
    "ds_read_b64_tr_b16 a[26:27], %[rb3] offset:22784\n"
    "ds_read_b64_tr_b16 a[28:29], %[rb3] offset:23552\n"
    "ds_read_b64_tr_b16 a[30:31], %[rb3] offset:23808\n"
    "buffer_load_dwordx4 v[228:231], %[vx], %[srdx], 0 offen offset:1024\n"
    "buffer_load_dwordx4 v[232:235], %[vx], %[srdx], s41 offen offset:1024\n"
    "s_waitcnt lgkmcnt(8)\n"
    "v_mfma_f32_32x32x16_bf16 v[64:79], a[144:147], a[16:19], v[64:79]\n"
    "v_mfma_f32_32x32x16_bf16 v[80:95], a[148:151], a[20:23], v[80:95]\n"
    "s_waitcnt vmcnt(8)\n"
    "ds_write_b128 %[xw], v[196:199] offset:0\n"
    "ds_write_b128 %[xw], v[200:203] offset:1024\n"
    "ds_read_b128 a[160:163], %[xr0] offset:0\n"
    "ds_read_b128 a[164:167], %[xr1] offset:0\n"
    "ds_read_b64_tr_b16 a[0:1], %[rb3] offset:24576\n"
    "ds_read_b64_tr_b16 a[2:3], %[rb3] offset:24832\n"
    "ds_read_b64_tr_b16 a[4:5], %[rb3] offset:25600\n"
    "ds_read_b64_tr_b16 a[6:7], %[rb3] offset:25856\n"
    "buffer_load_dwordx4 v[236:239], %[vx], %[srdx], 0 offen offset:1088\n"
    "buffer_load_dwordx4 v[240:243], %[vx], %[srdx], s41 offen offset:1088\n"
    "s_waitcnt lgkmcnt(8)\n"
    "v_mfma_f32_32x32x16_bf16 v[64:79], a[152:155], a[24:27], v[64:79]\n"
    "v_mfma_f32_32x32x16_bf16 v[80:95], a[156:159], a[28:31], v[80:95]\n"
    "s_waitcnt vmcnt(8)\n"
    "ds_write_b128 %[xw], v[204:207] offset:2048\n"
    "ds_write_b128 %[xw], v[208:211] offset:3072\n"
    "ds_read_b128 a[168:171], %[xr0] offset:2048\n"
    "ds_read_b128 a[172:175], %[xr1] offset:2048\n"
    "ds_read_b64_tr_b16 a[8:9], %[rb3] offset:26624\n"
    "ds_read_b64_tr_b16 a[10:11], %[rb3] offset:26880\n"
    "ds_read_b64_tr_b16 a[12:13], %[rb3] offset:27648\n"
    "ds_read_b64_tr_b16 a[14:15], %[rb3] offset:27904\n"
    "s_add_u32 m0, %[ldw], 0\n"
    "s_mov_b32 s40, 0\n"
    "buffer_load_dwordx4 %[q0], %[srdq], s40 offen lds\n"
    "s_add_u32 m0, %[ldw], 36864\n"
    "s_mov_b32 s40, 0\n"
    "buffer_load_dwordx4 %[q1], %[srdq], s40 offen lds\n"
    "s_waitcnt lgkmcnt(8)\n"
    "v_mfma_f32_32x32x16_bf16 v[64:79], a[160:163], a[0:3], v[64:79]\n"
    "v_mfma_f32_32x32x16_bf16 v[80:95], a[164:167], a[4:7], v[80:95]\n"
    "s_waitcnt vmcnt(8)\n"
    "ds_write_b128 %[xw], v[212:215] offset:0\n"
    "ds_write_b128 %[xw], v[216:219] offset:1024\n"
    "ds_read_b128 a[176:179], %[xr0] offset:0\n"
    "ds_read_b128 a[180:183], %[xr1] offset:0\n"
    "ds_read_b64_tr_b16 a[16:17], %[rb3] offset:28672\n"
    "ds_read_b64_tr_b16 a[18:19], %[rb3] offset:28928\n"
    "ds_read_b64_tr_b16 a[20:21], %[rb3] offset:29696\n"
    "ds_read_b64_tr_b16 a[22:23], %[rb3] offset:29952\n"
    "s_add_u32 m0, %[ldw], 73728\n"
    "s_mov_b32 s40, 0\n"
    "buffer_load_dwordx4 %[q2], %[srdq], s40 offen lds\n"
    "s_add_u32 m0, %[ldw], 1024\n"
    "s_mov_b32 s40, 18432\n"
    "buffer_load_dwordx4 %[q0], %[srdq], s40 offen lds\n"
    "s_waitcnt lgkmcnt(8)\n"
    "v_mfma_f32_32x32x16_bf16 v[64:79], a[168:171], a[8:11], v[64:79]\n"
    "v_mfma_f32_32x32x16_bf16 v[80:95], a[172:175], a[12:15], v[80:95]\n"
    "s_waitcnt vmcnt(8)\n"
    "ds_write_b128 %[xw], v[220:223] offset:2048\n"
    "ds_write_b128 %[xw], v[224:227] offset:3072\n"
    "ds_read_b128 a[184:187], %[xr0] offset:2048\n"
    "ds_read_b128 a[188:191], %[xr1] offset:2048\n"
    "ds_read_b64_tr_b16 a[24:25], %[rb3] offset:30720\n"
    "ds_read_b64_tr_b16 a[26:27], %[rb3] offset:30976\n"
    "ds_read_b64_tr_b16 a[28:29], %[rb3] offset:31744\n"
    "ds_read_b64_tr_b16 a[30:31], %[rb3] offset:32000\n"
    "s_add_u32 m0, %[ldw], 37888\n"
    "s_mov_b32 s40, 18432\n"
    "buffer_load_dwordx4 %[q1], %[srdq], s40 offen lds\n"
    "s_add_u32 m0, %[ldw], 74752\n"
    "s_mov_b32 s40, 18432\n"
    "buffer_load_dwordx4 %[q2], %[srdq], s40 offen lds\n"
    "s_waitcnt lgkmcnt(8)\n"
    "v_mfma_f32_32x32x16_bf16 v[64:79], a[176:179], a[16:19], v[64:79]\n"
    "v_mfma_f32_32x32x16_bf16 v[80:95], a[180:183], a[20:23], v[80:95]\n"
    "s_waitcnt vmcnt(8)\n"
    "ds_write_b128 %[xw], v[228:231] offset:0\n"
    "ds_write_b128 %[xw], v[232:235] offset:1024\n"
    "ds_read_b128 a[192:195], %[xr0] offset:0\n"
    "ds_read_b128 a[196:199], %[xr1] offset:0\n"
    "ds_read_b64_tr_b16 a[0:1], %[rb3] offset:32768\n"
    "ds_read_b64_tr_b16 a[2:3], %[rb3] offset:33024\n"
    "ds_read_b64_tr_b16 a[4:5], %[rb3] offset:33792\n"
    "ds_read_b64_tr_b16 a[6:7], %[rb3] offset:34048\n"
    "s_add_u32 m0, %[ldw], 2048\n"
    "s_mov_b32 s40, 36864\n"
    "buffer_load_dwordx4 %[q0], %[srdq], s40 offen lds\n"
    "s_add_u32 m0, %[ldw], 38912\n"
    "s_mov_b32 s40, 36864\n"
    "buffer_load_dwordx4 %[q1], %[srdq], s40 offen lds\n"
    "s_waitcnt lgkmcnt(8)\n"
    "v_mfma_f32_32x32x16_bf16 v[64:79], a[184:187], a[24:27], v[64:79]\n"
    "v_mfma_f32_32x32x16_bf16 v[80:95], a[188:191], a[28:31], v[80:95]\n"
    "s_waitcnt vmcnt(8)\n"
    "ds_write_b128 %[xw], v[236:239] offset:2048\n"
    "ds_write_b128 %[xw], v[240:243] offset:3072\n"
    "ds_read_b128 a[200:203], %[xr0] offset:2048\n"
    "ds_read_b128 a[204:207], %[xr1] offset:2048\n"
    "ds_read_b64_tr_b16 a[8:9], %[rb3] offset:34816\n"
    "ds_read_b64_tr_b16 a[10:11], %[rb3] offset:35072\n"
    "ds_read_b64_tr_b16 a[12:13], %[rb3] offset:35840\n"
    "ds_read_b64_tr_b16 a[14:15], %[rb3] offset:36096\n"
    "s_add_u32 m0, %[ldw], 75776\n"
    "s_mov_b32 s40, 36864\n"
    "buffer_load_dwordx4 %[q2], %[srdq], s40 offen lds\n"
    "s_add_u32 m0, %[ldw], 12288\n"
    "s_mov_b32 s40, 221184\n"
    "buffer_load_dwordx4 %[q0], %[srdq], s40 offen lds\n"
    "s_waitcnt lgkmcnt(8)\n"
    "v_mfma_f32_32x32x16_bf16 v[64:79], a[192:195], a[0:3], v[64:79]\n"
    "v_mfma_f32_32x32x16_bf16 v[80:95], a[196:199], a[4:7], v[80:95]\n"
    "s_waitcnt lgkmcnt(0)\n"
    "v_mfma_f32_32x32x16_bf16 v[64:79], a[200:203], a[8:11], v[64:79]\n"
    "v_mfma_f32_32x32x16_bf16 v[80:95], a[204:207], a[12:15], v[80:95]\n"
    "s_nop 7\n"
    "s_nop 7\n"
    "v_add_f32 v64, v64, v80\n"
    "v_add_f32 v65, v65, v81\n"
    "v_add_f32 v66, v66, v82\n"
    "v_add_f32 v67, v67, v83\n"
    "v_add_f32 v68, v68, v84\n"
    "v_add_f32 v69, v69, v85\n"
    "v_add_f32 v70, v70, v86\n"
    "v_add_f32 v71, v71, v87\n"
    "v_add_f32 v72, v72, v88\n"
    "v_add_f32 v73, v73, v89\n"
    "v_add_f32 v74, v74, v90\n"
    "v_add_f32 v75, v75, v91\n"
    "v_add_f32 v76, v76, v92\n"
    "v_add_f32 v77, v77, v93\n"
    "v_add_f32 v78, v78, v94\n"
    "v_add_f32 v79, v79, v95\n"
        : [tag] "=&v"(tag), "={v[64:79]}"(f), "={a[64:79]}"(xa[0]), "={a[80:95]}"(xa[1]), "={a[96:111]}"(xa[2]), "={a[112:127]}"(xa[3]), "={a[128:143]}"(xa[4]), "={a[144:159]}"(xa[5]), "={a[160:175]}"(xa[6]), "={a[176:191]}"(xa[7]), "={a[192:207]}"(xa[8])
        : PRO_IN, PRO_KV
        : "v80", "v81", "v82", "v83", "v84", "v85", "v86", "v87", "v88", "v89", "v90", "v91", "v92", "v93", "v94", "v95", "v96", "v97", "v98", "v99", "v100", "v101", "v102", "v103", "v104", "v105", "v106", "v107", "v108", "v109", "v110", "v111", "v112", "v113", "v114", "v115", "v116", "v117", "v118", "v119", "v120", "v121", "v122", "v123", "v124", "v125", "v126", "v127", "v128", "v129", "v130", "v131", "v132", "v133", "v134", "v135", "v136", "v137", "v138", "v139", "v140", "v141", "v142", "v143", "v144", "v145", "v146", "v147", "v148", "v149", "v150", "v151", "v152", "v153", "v154", "v155", "v156", "v157", "v158", "v159", "v160", "v161", "v162", "v163", "v164", "v165", "v166", "v167", "v168", "v169", "v170", "v171", "v172", "v173", "v174", "v175", "v176", "v177", "v178", "v179", "v180", "v181", "v182", "v183", "v184", "v185", "v186", "v187", "v188", "v189", "v190", "v191", "v192", "v193", "v194", "v195", "v196", "v197", "v198", "v199", "v200", "v201", "v202", "v203", "v204", "v205", "v206", "v207", "v208", "v209", "v210", "v211", "v212", "v213", "v214", "v215", "v216", "v217", "v218", "v219", "v220", "v221", "v222", "v223", "v224", "v225", "v226", "v227", "v228", "v229", "v230", "v231", "v232", "v233", "v234", "v235", "v236", "v237", "v238", "v239", "v240", "v241", "v242", "v243", "a0", "a1", "a2", "a3", "a4", "a5", "a6", "a7", "a8", "a9", "a10", "a11", "a12", "a13", "a14", "a15", "a16", "a17", "a18", "a19", "a20", "a21", "a22", "a23", "a24", "a25", "a26", "a27", "a28", "a29", "a30", "a31", "s40", "s41", "scc", "memory");
    float g[16];
#pragma unroll
    for (int e = 0; e < 16; e++) g[e] = f[e];
    const u32x4 p0 = pack8(g), p1 = pack8(g + 8), ones = {0x3f803f80u, 0x3f803f80u, 0x3f803f80u, 0x3f803f80u};
    const unsigned va = q <= 18 ? rec + 1280 + h * 304 + q * 16 : 0x80000000u;
    __builtin_amdgcn_raw_buffer_store_b128(q < 18 ? p0 : ones, rw, va, 0, SC1);
    __builtin_amdgcn_raw_buffer_store_b128(q < 18 ? p1 : ones, rw, va, 608, SC1);
  } else {
    unsigned wf[2][8];
#pragma unroll
    for (int i = 0; i < 2; i++) {
      const int n = (f0 + i) / 36, ks = (f0 + i) % 36;
      const unsigned col = n < 18 ? (n * 32 + q) * 2 : 0x80000000u;
#pragma unroll
      for (int j = 0; j < 8; j++) {
        const int ch = ks < 32 ? 18 * ks + (j & 3) + 8 * (j >> 2) + 4 * h : 72 * (2 * (ks - 32) + h) + 18 * (j >> 1) + 16 + (j & 1);
        wf[i][j] = col + ch * 1152;
      }
    }
    asm volatile(
    "s_nop 4\n"
    "s_mov_b32 s40, 166400\n"
    "buffer_load_dword %[tag], off, %[srdw], s40 sc1\n"
    "buffer_load_ushort %[w0], %[w0], %[srdo], 0 offen\n"
    "buffer_load_ushort %[w1], %[w1], %[srdo], 0 offen\n"
    "buffer_load_ushort %[w2], %[w2], %[srdo], 0 offen\n"
    "buffer_load_ushort %[w3], %[w3], %[srdo], 0 offen\n"
    "buffer_load_ushort %[w4], %[w4], %[srdo], 0 offen\n"
    "buffer_load_ushort %[w5], %[w5], %[srdo], 0 offen\n"
    "buffer_load_ushort %[w6], %[w6], %[srdo], 0 offen\n"
    "buffer_load_ushort %[w7], %[w7], %[srdo], 0 offen\n"
    "buffer_load_ushort %[w8], %[w8], %[srdo], 0 offen\n"
    "buffer_load_ushort %[w9], %[w9], %[srdo], 0 offen\n"
    "buffer_load_ushort %[w10], %[w10], %[srdo], 0 offen\n"
    "buffer_load_ushort %[w11], %[w11], %[srdo], 0 offen\n"
    "buffer_load_ushort %[w12], %[w12], %[srdo], 0 offen\n"
    "buffer_load_ushort %[w13], %[w13], %[srdo], 0 offen\n"
    "buffer_load_ushort %[w14], %[w14], %[srdo], 0 offen\n"
    "buffer_load_ushort %[w15], %[w15], %[srdo], 0 offen\n"
    "s_mov_b32 s41, 18432\n"
    "buffer_load_dwordx4 v[100:103], %[vx], %[srdx], 0 offen offset:0\n"
    "buffer_load_dwordx4 v[104:107], %[vx], %[srdx], s41 offen offset:0\n"
    "buffer_load_dwordx4 v[108:111], %[vx], %[srdx], 0 offen offset:64\n"
    "buffer_load_dwordx4 v[112:115], %[vx], %[srdx], s41 offen offset:64\n"
    "buffer_load_dwordx4 v[116:119], %[vx], %[srdx], 0 offen offset:128\n"
    "buffer_load_dwordx4 v[120:123], %[vx], %[srdx], s41 offen offset:128\n"
    "buffer_load_dwordx4 v[124:127], %[vx], %[srdx], 0 offen offset:192\n"
    "buffer_load_dwordx4 v[128:131], %[vx], %[srdx], s41 offen offset:192\n"
    "buffer_load_dwordx4 v[132:135], %[vx], %[srdx], 0 offen offset:256\n"
    "buffer_load_dwordx4 v[136:139], %[vx], %[srdx], s41 offen offset:256\n"
    "s_waitcnt vmcnt(8)\n"
    "ds_write_b128 %[xw], v[100:103] offset:0\n"
    "ds_write_b128 %[xw], v[104:107] offset:1024\n"
    "ds_read_b128 a[64:67], %[xr0] offset:0\n"
    "ds_read_b128 a[68:71], %[xr1] offset:0\n"
    "buffer_load_dwordx4 v[140:143], %[vx], %[srdx], 0 offen offset:320\n"
    "buffer_load_dwordx4 v[144:147], %[vx], %[srdx], s41 offen offset:320\n"
    "s_waitcnt vmcnt(8)\n"
    "ds_write_b128 %[xw], v[108:111] offset:2048\n"
    "ds_write_b128 %[xw], v[112:115] offset:3072\n"
    "ds_read_b128 a[72:75], %[xr0] offset:2048\n"
    "ds_read_b128 a[76:79], %[xr1] offset:2048\n"
    "buffer_load_dwordx4 v[148:151], %[vx], %[srdx], 0 offen offset:384\n"
    "buffer_load_dwordx4 v[152:155], %[vx], %[srdx], s41 offen offset:384\n"
    "s_waitcnt lgkmcnt(4)\n"
    "s_waitcnt vmcnt(8)\n"
    "ds_write_b128 %[xw], v[116:119] offset:0\n"
    "ds_write_b128 %[xw], v[120:123] offset:1024\n"
    "ds_read_b128 a[80:83], %[xr0] offset:0\n"
    "ds_read_b128 a[84:87], %[xr1] offset:0\n"
    "buffer_load_dwordx4 v[156:159], %[vx], %[srdx], 0 offen offset:448\n"
    "buffer_load_dwordx4 v[160:163], %[vx], %[srdx], s41 offen offset:448\n"
    "s_waitcnt lgkmcnt(4)\n"
    "s_waitcnt vmcnt(8)\n"
    "ds_write_b128 %[xw], v[124:127] offset:2048\n"
    "ds_write_b128 %[xw], v[128:131] offset:3072\n"
    "ds_read_b128 a[88:91], %[xr0] offset:2048\n"
    "ds_read_b128 a[92:95], %[xr1] offset:2048\n"
    "buffer_load_dwordx4 v[164:167], %[vx], %[srdx], 0 offen offset:512\n"
    "buffer_load_dwordx4 v[168:171], %[vx], %[srdx], s41 offen offset:512\n"
    "s_waitcnt lgkmcnt(4)\n"
    "s_waitcnt vmcnt(8)\n"
    "ds_write_b128 %[xw], v[132:135] offset:0\n"
    "ds_write_b128 %[xw], v[136:139] offset:1024\n"
    "ds_read_b128 a[96:99], %[xr0] offset:0\n"
    "ds_read_b128 a[100:103], %[xr1] offset:0\n"
    "buffer_load_dwordx4 v[172:175], %[vx], %[srdx], 0 offen offset:576\n"
    "buffer_load_dwordx4 v[176:179], %[vx], %[srdx], s41 offen offset:576\n"
    "s_waitcnt lgkmcnt(4)\n"
    "s_waitcnt vmcnt(8)\n"
    "ds_write_b128 %[xw], v[140:143] offset:2048\n"
    "ds_write_b128 %[xw], v[144:147] offset:3072\n"
    "ds_read_b128 a[104:107], %[xr0] offset:2048\n"
    "ds_read_b128 a[108:111], %[xr1] offset:2048\n"
    "buffer_load_dwordx4 v[180:183], %[vx], %[srdx], 0 offen offset:640\n"
    "buffer_load_dwordx4 v[184:187], %[vx], %[srdx], s41 offen offset:640\n"
    "s_waitcnt lgkmcnt(4)\n"
    "s_waitcnt vmcnt(8)\n"
    "ds_write_b128 %[xw], v[148:151] offset:0\n"
    "ds_write_b128 %[xw], v[152:155] offset:1024\n"
    "ds_read_b128 a[112:115], %[xr0] offset:0\n"
    "ds_read_b128 a[116:119], %[xr1] offset:0\n"
    "buffer_load_dwordx4 v[188:191], %[vx], %[srdx], 0 offen offset:704\n"
    "buffer_load_dwordx4 v[192:195], %[vx], %[srdx], s41 offen offset:704\n"
    "s_waitcnt lgkmcnt(4)\n"
    "s_waitcnt vmcnt(8)\n"
    "ds_write_b128 %[xw], v[156:159] offset:2048\n"
    "ds_write_b128 %[xw], v[160:163] offset:3072\n"
    "ds_read_b128 a[120:123], %[xr0] offset:2048\n"
    "ds_read_b128 a[124:127], %[xr1] offset:2048\n"
    "buffer_load_dwordx4 v[196:199], %[vx], %[srdx], 0 offen offset:768\n"
    "buffer_load_dwordx4 v[200:203], %[vx], %[srdx], s41 offen offset:768\n"
    "s_waitcnt lgkmcnt(4)\n"
    "s_waitcnt vmcnt(8)\n"
    "ds_write_b128 %[xw], v[164:167] offset:0\n"
    "ds_write_b128 %[xw], v[168:171] offset:1024\n"
    "ds_read_b128 a[128:131], %[xr0] offset:0\n"
    "ds_read_b128 a[132:135], %[xr1] offset:0\n"
    "buffer_load_dwordx4 v[204:207], %[vx], %[srdx], 0 offen offset:832\n"
    "buffer_load_dwordx4 v[208:211], %[vx], %[srdx], s41 offen offset:832\n"
    "s_waitcnt lgkmcnt(4)\n"
    "s_waitcnt vmcnt(8)\n"
    "ds_write_b128 %[xw], v[172:175] offset:2048\n"
    "ds_write_b128 %[xw], v[176:179] offset:3072\n"
    "ds_read_b128 a[136:139], %[xr0] offset:2048\n"
    "ds_read_b128 a[140:143], %[xr1] offset:2048\n"
    "buffer_load_dwordx4 v[212:215], %[vx], %[srdx], 0 offen offset:896\n"
    "buffer_load_dwordx4 v[216:219], %[vx], %[srdx], s41 offen offset:896\n"
    "s_waitcnt lgkmcnt(4)\n"
    "s_waitcnt vmcnt(8)\n"
    "ds_write_b128 %[xw], v[180:183] offset:0\n"
    "ds_write_b128 %[xw], v[184:187] offset:1024\n"
    "ds_read_b128 a[144:147], %[xr0] offset:0\n"
    "ds_read_b128 a[148:151], %[xr1] offset:0\n"
    "buffer_load_dwordx4 v[220:223], %[vx], %[srdx], 0 offen offset:960\n"
    "buffer_load_dwordx4 v[224:227], %[vx], %[srdx], s41 offen offset:960\n"
    "s_waitcnt lgkmcnt(4)\n"
    "s_waitcnt vmcnt(8)\n"
    "ds_write_b128 %[xw], v[188:191] offset:2048\n"
    "ds_write_b128 %[xw], v[192:195] offset:3072\n"
    "ds_read_b128 a[152:155], %[xr0] offset:2048\n"
    "ds_read_b128 a[156:159], %[xr1] offset:2048\n"
    "buffer_load_dwordx4 v[228:231], %[vx], %[srdx], 0 offen offset:1024\n"
    "buffer_load_dwordx4 v[232:235], %[vx], %[srdx], s41 offen offset:1024\n"
    "s_waitcnt lgkmcnt(4)\n"
    "s_waitcnt vmcnt(8)\n"
    "ds_write_b128 %[xw], v[196:199] offset:0\n"
    "ds_write_b128 %[xw], v[200:203] offset:1024\n"
    "ds_read_b128 a[160:163], %[xr0] offset:0\n"
    "ds_read_b128 a[164:167], %[xr1] offset:0\n"
    "buffer_load_dwordx4 v[236:239], %[vx], %[srdx], 0 offen offset:1088\n"
    "buffer_load_dwordx4 v[240:243], %[vx], %[srdx], s41 offen offset:1088\n"
    "s_waitcnt lgkmcnt(4)\n"
    "s_waitcnt vmcnt(8)\n"
    "ds_write_b128 %[xw], v[204:207] offset:2048\n"
    "ds_write_b128 %[xw], v[208:211] offset:3072\n"
    "ds_read_b128 a[168:171], %[xr0] offset:2048\n"
    "ds_read_b128 a[172:175], %[xr1] offset:2048\n"
    "s_add_u32 m0, %[ldw], 0\n"
    "s_mov_b32 s40, 0\n"
    "buffer_load_dwordx4 %[q0], %[srdq], s40 offen lds\n"
    "s_add_u32 m0, %[ldw], 36864\n"
    "s_mov_b32 s40, 0\n"
    "buffer_load_dwordx4 %[q1], %[srdq], s40 offen lds\n"
    "s_waitcnt lgkmcnt(4)\n"
    "s_waitcnt vmcnt(8)\n"
    "ds_write_b128 %[xw], v[212:215] offset:0\n"
    "ds_write_b128 %[xw], v[216:219] offset:1024\n"
    "ds_read_b128 a[176:179], %[xr0] offset:0\n"
    "ds_read_b128 a[180:183], %[xr1] offset:0\n"
    "s_add_u32 m0, %[ldw], 73728\n"
    "s_mov_b32 s40, 0\n"
    "buffer_load_dwordx4 %[q2], %[srdq], s40 offen lds\n"
    "s_add_u32 m0, %[ldw], 1024\n"
    "s_mov_b32 s40, 18432\n"
    "buffer_load_dwordx4 %[q0], %[srdq], s40 offen lds\n"
    "s_waitcnt lgkmcnt(4)\n"
    "s_waitcnt vmcnt(8)\n"
    "ds_write_b128 %[xw], v[220:223] offset:2048\n"
    "ds_write_b128 %[xw], v[224:227] offset:3072\n"
    "ds_read_b128 a[184:187], %[xr0] offset:2048\n"
    "ds_read_b128 a[188:191], %[xr1] offset:2048\n"
    "s_add_u32 m0, %[ldw], 37888\n"
    "s_mov_b32 s40, 18432\n"
    "buffer_load_dwordx4 %[q1], %[srdq], s40 offen lds\n"
    "s_add_u32 m0, %[ldw], 74752\n"
    "s_mov_b32 s40, 18432\n"
    "buffer_load_dwordx4 %[q2], %[srdq], s40 offen lds\n"
    "s_waitcnt lgkmcnt(4)\n"
    "s_waitcnt vmcnt(8)\n"
    "ds_write_b128 %[xw], v[228:231] offset:0\n"
    "ds_write_b128 %[xw], v[232:235] offset:1024\n"
    "ds_read_b128 a[192:195], %[xr0] offset:0\n"
    "ds_read_b128 a[196:199], %[xr1] offset:0\n"
    "s_add_u32 m0, %[ldw], 2048\n"
    "s_mov_b32 s40, 36864\n"
    "buffer_load_dwordx4 %[q0], %[srdq], s40 offen lds\n"
    "s_add_u32 m0, %[ldw], 38912\n"
    "s_mov_b32 s40, 36864\n"
    "buffer_load_dwordx4 %[q1], %[srdq], s40 offen lds\n"
    "s_waitcnt lgkmcnt(4)\n"
    "s_waitcnt vmcnt(8)\n"
    "ds_write_b128 %[xw], v[236:239] offset:2048\n"
    "ds_write_b128 %[xw], v[240:243] offset:3072\n"
    "ds_read_b128 a[200:203], %[xr0] offset:2048\n"
    "ds_read_b128 a[204:207], %[xr1] offset:2048\n"
    "s_add_u32 m0, %[ldw], 75776\n"
    "s_mov_b32 s40, 36864\n"
    "buffer_load_dwordx4 %[q2], %[srdq], s40 offen lds\n"
    "s_add_u32 m0, %[ldw], 12288\n"
    "s_mov_b32 s40, 221184\n"
    "buffer_load_dwordx4 %[q0], %[srdq], s40 offen lds\n"
    "s_waitcnt lgkmcnt(4)\n"
    "s_waitcnt lgkmcnt(0)\n"
        : [tag] "=&v"(tag), [w0] "+v"(wf[0][0]), [w1] "+v"(wf[0][1]), [w2] "+v"(wf[0][2]), [w3] "+v"(wf[0][3]), [w4] "+v"(wf[0][4]), [w5] "+v"(wf[0][5]), [w6] "+v"(wf[0][6]), [w7] "+v"(wf[0][7]), [w8] "+v"(wf[1][0]), [w9] "+v"(wf[1][1]), [w10] "+v"(wf[1][2]), [w11] "+v"(wf[1][3]), [w12] "+v"(wf[1][4]), [w13] "+v"(wf[1][5]), [w14] "+v"(wf[1][6]), [w15] "+v"(wf[1][7]), "={v[64:79]}"(f), "={a[64:79]}"(xa[0]), "={a[80:95]}"(xa[1]), "={a[96:111]}"(xa[2]), "={a[112:127]}"(xa[3]), "={a[128:143]}"(xa[4]), "={a[144:159]}"(xa[5]), "={a[160:175]}"(xa[6]), "={a[176:191]}"(xa[7]), "={a[192:207]}"(xa[8])
        : PRO_IN, [srdo] "s"(make_srd(wo, 576 * 1152))
        : "v80", "v81", "v82", "v83", "v84", "v85", "v86", "v87", "v88", "v89", "v90", "v91", "v92", "v93", "v94", "v95", "v96", "v97", "v98", "v99", "v100", "v101", "v102", "v103", "v104", "v105", "v106", "v107", "v108", "v109", "v110", "v111", "v112", "v113", "v114", "v115", "v116", "v117", "v118", "v119", "v120", "v121", "v122", "v123", "v124", "v125", "v126", "v127", "v128", "v129", "v130", "v131", "v132", "v133", "v134", "v135", "v136", "v137", "v138", "v139", "v140", "v141", "v142", "v143", "v144", "v145", "v146", "v147", "v148", "v149", "v150", "v151", "v152", "v153", "v154", "v155", "v156", "v157", "v158", "v159", "v160", "v161", "v162", "v163", "v164", "v165", "v166", "v167", "v168", "v169", "v170", "v171", "v172", "v173", "v174", "v175", "v176", "v177", "v178", "v179", "v180", "v181", "v182", "v183", "v184", "v185", "v186", "v187", "v188", "v189", "v190", "v191", "v192", "v193", "v194", "v195", "v196", "v197", "v198", "v199", "v200", "v201", "v202", "v203", "v204", "v205", "v206", "v207", "v208", "v209", "v210", "v211", "v212", "v213", "v214", "v215", "v216", "v217", "v218", "v219", "v220", "v221", "v222", "v223", "v224", "v225", "v226", "v227", "v228", "v229", "v230", "v231", "v232", "v233", "v234", "v235", "v236", "v237", "v238", "v239", "v240", "v241", "v242", "v243", "a0", "a1", "a2", "a3", "a4", "a5", "a6", "a7", "a8", "a9", "a10", "a11", "a12", "a13", "a14", "a15", "a16", "a17", "a18", "a19", "a20", "a21", "a22", "a23", "a24", "a25", "a26", "a27", "a28", "a29", "a30", "a31", "s40", "s41", "scc", "memory");
#pragma unroll
    for (int i = 0; i < 2; i++) wfv[i] = u32x4{wf[i][0] | wf[i][1] << 16, wf[i][2] | wf[i][3] << 16, wf[i][4] | wf[i][5] << 16, wf[i][6] | wf[i][7] << 16};
  }
  tag += 1u;
  asm volatile(
    "s_nop 4\n"
    "s_add_u32 m0, %[ldw], 49152\n"
    "s_mov_b32 s40, 221184\n"
    "buffer_load_dwordx4 %[q1], %[srdq], s40 offen lds\n"
    "s_add_u32 m0, %[ldw], 86016\n"
    "s_mov_b32 s40, 221184\n"
    "buffer_load_dwordx4 %[q2], %[srdq], s40 offen lds\n"
    "s_add_u32 m0, %[ldw], 13312\n"
    "s_mov_b32 s40, 239616\n"
    "buffer_load_dwordx4 %[q0], %[srdq], s40 offen lds\n"
    "s_add_u32 m0, %[ldw], 50176\n"
    "s_mov_b32 s40, 239616\n"
    "buffer_load_dwordx4 %[q1], %[srdq], s40 offen lds\n"
    "s_add_u32 m0, %[ldw], 87040\n"
    "s_mov_b32 s40, 239616\n"
    "buffer_load_dwordx4 %[q2], %[srdq], s40 offen lds\n"
    "s_add_u32 m0, %[ldw], 14336\n"
    "s_mov_b32 s40, 258048\n"
    "buffer_load_dwordx4 %[q0], %[srdq], s40 offen lds\n"
    "s_add_u32 m0, %[ldw], 51200\n"
    "s_mov_b32 s40, 258048\n"
    "buffer_load_dwordx4 %[q1], %[srdq], s40 offen lds\n"
    "s_add_u32 m0, %[ldw], 88064\n"
    "s_mov_b32 s40, 258048\n"
    "buffer_load_dwordx4 %[q2], %[srdq], s40 offen lds\n"
    "s_waitcnt vmcnt(8) lgkmcnt(0)\n"
    "s_barrier\n"
      : : Q_IN : "s40", "scc", "memory");
  if (threadIdx.x < 8 && hg < 2) {
    const float* km = (const float*)(lds + 147456);
    const u32x2 fl = {tag, hg ? 0u : __float_as_uint(fmaxf(fmaxf(km[0], km[1024]), fmaxf(km[2048], km[3072])))};
    __builtin_amdgcn_raw_buffer_store_b64(fl, rw, OFF_FLAG + threadIdx.x * 256 + (tb * 2 + hg) * 8, 0, SC1);
  }
  if (threadIdx.x == 64) __builtin_amdgcn_raw_buffer_store_b32(tag, rw, OFF_CHK + (tb * 8 + hg) * 4, 0, SC1);
  u32x4 q0[4]; u32x2 q1[4], fl;
  f32x16 kv0, kv1, kv2, o[4];
  const u32x4 srd = make_srd(ws + OFF_KV, 64 * KVREC);
#define KV_IN [vk0] "v"(l * 16), [vk1] "v"(h ? 1024u + q * 8 : 0x80000000u), [vv] "v"(q > 18 && q != 22 ? 0x80000000u : 1280u + h * 304 + (q < 18 ? q : 18) * 16), [srd] "s"(srd)
  {
    f32x16 a0, a1, a2;
    asm volatile(
    "s_nop 4\n"
    "ds_read_b64_tr_b16 a[0:1], %[rb0] offset:0\n"
    "ds_read_b64_tr_b16 a[2:3], %[rb0] offset:256\n"
    "ds_read_b64_tr_b16 a[4:5], %[rb1] offset:0\n"
    "ds_read_b64_tr_b16 a[6:7], %[rb1] offset:256\n"
    "ds_read_b64_tr_b16 a[8:9], %[rb2] offset:0\n"
    "ds_read_b64_tr_b16 a[10:11], %[rb2] offset:256\n"
    "ds_read_b64_tr_b16 a[12:13], %[rb0] offset:1024\n"
    "ds_read_b64_tr_b16 a[14:15], %[rb0] offset:1280\n"
    "ds_read_b64_tr_b16 a[16:17], %[rb1] offset:1024\n"
    "ds_read_b64_tr_b16 a[18:19], %[rb1] offset:1280\n"
    "ds_read_b64_tr_b16 a[20:21], %[rb2] offset:1024\n"
    "ds_read_b64_tr_b16 a[22:23], %[rb2] offset:1280\n"
    "s_waitcnt lgkmcnt(8)\n"
    "v_mfma_f32_32x32x16_bf16 %[a0], a[0:3], a[64:67], 0\n"
    "ds_read_b64_tr_b16 a[24:25], %[rb0] offset:2048\n"
    "ds_read_b64_tr_b16 a[26:27], %[rb0] offset:2304\n"
    "v_mfma_f32_32x32x16_bf16 %[a1], a[4:7], a[64:67], 0\n"
    "ds_read_b64_tr_b16 a[28:29], %[rb1] offset:2048\n"
    "ds_read_b64_tr_b16 a[30:31], %[rb1] offset:2304\n"
    "s_waitcnt lgkmcnt(8)\n"
    "v_mfma_f32_32x32x16_bf16 %[a2], a[8:11], a[64:67], 0\n"
    "ds_read_b64_tr_b16 a[0:1], %[rb2] offset:2048\n"
    "ds_read_b64_tr_b16 a[2:3], %[rb2] offset:2304\n"
    "s_add_u32 m0, %[ldw], 24576\n"
    "s_mov_b32 s40, 442368\n"
    "buffer_load_dwordx4 %[q0], %[srdq], s40 offen lds\n"
    "v_mfma_f32_32x32x16_bf16 %[a0], a[12:15], a[68:71], %[a0]\n"
    "ds_read_b64_tr_b16 a[4:5], %[rb0] offset:3072\n"
    "ds_read_b64_tr_b16 a[6:7], %[rb0] offset:3328\n"
    "s_waitcnt lgkmcnt(8)\n"
    "v_mfma_f32_32x32x16_bf16 %[a1], a[16:19], a[68:71], %[a1]\n"
    "ds_read_b64_tr_b16 a[8:9], %[rb1] offset:3072\n"
    "ds_read_b64_tr_b16 a[10:11], %[rb1] offset:3328\n"
    "v_mfma_f32_32x32x16_bf16 %[a2], a[20:23], a[68:71], %[a2]\n"
    "ds_read_b64_tr_b16 a[12:13], %[rb2] offset:3072\n"
    "ds_read_b64_tr_b16 a[14:15], %[rb2] offset:3328\n"
    "s_add_u32 m0, %[ldw], 61440\n"
    "s_mov_b32 s40, 442368\n"
    "buffer_load_dwordx4 %[q1], %[srdq], s40 offen lds\n"
    "s_waitcnt lgkmcnt(8)\n"
    "v_mfma_f32_32x32x16_bf16 %[a0], a[24:27], a[72:75], %[a0]\n"
    "ds_read_b64_tr_b16 a[16:17], %[rb0] offset:4096\n"
    "ds_read_b64_tr_b16 a[18:19], %[rb0] offset:4352\n"
    "v_mfma_f32_32x32x16_bf16 %[a1], a[28:31], a[72:75], %[a1]\n"
    "ds_read_b64_tr_b16 a[20:21], %[rb1] offset:4096\n"
    "ds_read_b64_tr_b16 a[22:23], %[rb1] offset:4352\n"
    "s_waitcnt lgkmcnt(8)\n"
    "v_mfma_f32_32x32x16_bf16 %[a2], a[0:3], a[72:75], %[a2]\n"
    "ds_read_b64_tr_b16 a[24:25], %[rb2] offset:4096\n"
    "ds_read_b64_tr_b16 a[26:27], %[rb2] offset:4352\n"
    "s_add_u32 m0, %[ldw], 98304\n"
    "s_mov_b32 s40, 442368\n"
    "buffer_load_dwordx4 %[q2], %[srdq], s40 offen lds\n"
    "v_mfma_f32_32x32x16_bf16 %[a0], a[4:7], a[76:79], %[a0]\n"
    "ds_read_b64_tr_b16 a[28:29], %[rb0] offset:5120\n"
    "ds_read_b64_tr_b16 a[30:31], %[rb0] offset:5376\n"
    "s_waitcnt lgkmcnt(8)\n"
    "v_mfma_f32_32x32x16_bf16 %[a1], a[8:11], a[76:79], %[a1]\n"
    "ds_read_b64_tr_b16 a[0:1], %[rb1] offset:5120\n"
    "ds_read_b64_tr_b16 a[2:3], %[rb1] offset:5376\n"
    "v_mfma_f32_32x32x16_bf16 %[a2], a[12:15], a[76:79], %[a2]\n"
    "ds_read_b64_tr_b16 a[4:5], %[rb2] offset:5120\n"
    "ds_read_b64_tr_b16 a[6:7], %[rb2] offset:5376\n"
    "s_add_u32 m0, %[ldw], 25600\n"
    "s_mov_b32 s40, 460800\n"
    "buffer_load_dwordx4 %[q0], %[srdq], s40 offen lds\n"
    "s_waitcnt lgkmcnt(8)\n"
    "v_mfma_f32_32x32x16_bf16 %[a0], a[16:19], a[80:83], %[a0]\n"
    "ds_read_b64_tr_b16 a[8:9], %[rb0] offset:6144\n"
    "ds_read_b64_tr_b16 a[10:11], %[rb0] offset:6400\n"
    "v_mfma_f32_32x32x16_bf16 %[a1], a[20:23], a[80:83], %[a1]\n"
    "ds_read_b64_tr_b16 a[12:13], %[rb1] offset:6144\n"
    "ds_read_b64_tr_b16 a[14:15], %[rb1] offset:6400\n"
    "s_waitcnt lgkmcnt(8)\n"
    "v_mfma_f32_32x32x16_bf16 %[a2], a[24:27], a[80:83], %[a2]\n"
    "ds_read_b64_tr_b16 a[16:17], %[rb2] offset:6144\n"
    "ds_read_b64_tr_b16 a[18:19], %[rb2] offset:6400\n"
    "s_add_u32 m0, %[ldw], 62464\n"
    "s_mov_b32 s40, 460800\n"
    "buffer_load_dwordx4 %[q1], %[srdq], s40 offen lds\n"
    "v_mfma_f32_32x32x16_bf16 %[a0], a[28:31], a[84:87], %[a0]\n"
    "ds_read_b64_tr_b16 a[20:21], %[rb0] offset:7168\n"
    "ds_read_b64_tr_b16 a[22:23], %[rb0] offset:7424\n"
    "s_waitcnt lgkmcnt(8)\n"
    "v_mfma_f32_32x32x16_bf16 %[a1], a[0:3], a[84:87], %[a1]\n"
    "ds_read_b64_tr_b16 a[24:25], %[rb1] offset:7168\n"
    "ds_read_b64_tr_b16 a[26:27], %[rb1] offset:7424\n"
    "v_mfma_f32_32x32x16_bf16 %[a2], a[4:7], a[84:87], %[a2]\n"
    "ds_read_b64_tr_b16 a[28:29], %[rb2] offset:7168\n"
    "ds_read_b64_tr_b16 a[30:31], %[rb2] offset:7424\n"
    "s_add_u32 m0, %[ldw], 99328\n"
    "s_mov_b32 s40, 460800\n"
    "buffer_load_dwordx4 %[q2], %[srdq], s40 offen lds\n"
    "s_waitcnt lgkmcnt(8)\n"
    "v_mfma_f32_32x32x16_bf16 %[a0], a[8:11], a[88:91], %[a0]\n"
    "ds_read_b64_tr_b16 a[0:1], %[rb0] offset:8192\n"
    "ds_read_b64_tr_b16 a[2:3], %[rb0] offset:8448\n"
    "v_mfma_f32_32x32x16_bf16 %[a1], a[12:15], a[88:91], %[a1]\n"
    "ds_read_b64_tr_b16 a[4:5], %[rb1] offset:8192\n"
    "ds_read_b64_tr_b16 a[6:7], %[rb1] offset:8448\n"
    "s_waitcnt lgkmcnt(8)\n"
    "v_mfma_f32_32x32x16_bf16 %[a2], a[16:19], a[88:91], %[a2]\n"
    "ds_read_b64_tr_b16 a[8:9], %[rb2] offset:8192\n"
    "ds_read_b64_tr_b16 a[10:11], %[rb2] offset:8448\n"
    "s_add_u32 m0, %[ldw], 26624\n"
    "s_mov_b32 s40, 479232\n"
    "buffer_load_dwordx4 %[q0], %[srdq], s40 offen lds\n"
    "v_mfma_f32_32x32x16_bf16 %[a0], a[20:23], a[92:95], %[a0]\n"
    "ds_read_b64_tr_b16 a[12:13], %[rb0] offset:9216\n"
    "ds_read_b64_tr_b16 a[14:15], %[rb0] offset:9472\n"
    "s_waitcnt lgkmcnt(8)\n"
    "v_mfma_f32_32x32x16_bf16 %[a1], a[24:27], a[92:95], %[a1]\n"
    "ds_read_b64_tr_b16 a[16:17], %[rb1] offset:9216\n"
    "ds_read_b64_tr_b16 a[18:19], %[rb1] offset:9472\n"
    "v_mfma_f32_32x32x16_bf16 %[a2], a[28:31], a[92:95], %[a2]\n"
    "ds_read_b64_tr_b16 a[20:21], %[rb2] offset:9216\n"
    "ds_read_b64_tr_b16 a[22:23], %[rb2] offset:9472\n"
    "s_add_u32 m0, %[ldw], 63488\n"
    "s_mov_b32 s40, 479232\n"
    "buffer_load_dwordx4 %[q1], %[srdq], s40 offen lds\n"
    "s_waitcnt lgkmcnt(8)\n"
    "v_mfma_f32_32x32x16_bf16 %[a0], a[0:3], a[96:99], %[a0]\n"
    "ds_read_b64_tr_b16 a[24:25], %[rb0] offset:10240\n"
    "ds_read_b64_tr_b16 a[26:27], %[rb0] offset:10496\n"
    "v_mfma_f32_32x32x16_bf16 %[a1], a[4:7], a[96:99], %[a1]\n"
    "ds_read_b64_tr_b16 a[28:29], %[rb1] offset:10240\n"
    "ds_read_b64_tr_b16 a[30:31], %[rb1] offset:10496\n"
    "s_waitcnt lgkmcnt(8)\n"
    "v_mfma_f32_32x32x16_bf16 %[a2], a[8:11], a[96:99], %[a2]\n"
    "ds_read_b64_tr_b16 a[0:1], %[rb2] offset:10240\n"
    "ds_read_b64_tr_b16 a[2:3], %[rb2] offset:10496\n"
    "s_add_u32 m0, %[ldw], 100352\n"
    "s_mov_b32 s40, 479232\n"
    "buffer_load_dwordx4 %[q2], %[srdq], s40 offen lds\n"
    "v_mfma_f32_32x32x16_bf16 %[a0], a[12:15], a[100:103], %[a0]\n"
    "ds_read_b64_tr_b16 a[4:5], %[rb0] offset:11264\n"
    "ds_read_b64_tr_b16 a[6:7], %[rb0] offset:11520\n"
    "s_waitcnt lgkmcnt(8)\n"
    "v_mfma_f32_32x32x16_bf16 %[a1], a[16:19], a[100:103], %[a1]\n"
    "ds_read_b64_tr_b16 a[8:9], %[rb1] offset:11264\n"
    "ds_read_b64_tr_b16 a[10:11], %[rb1] offset:11520\n"
    "v_mfma_f32_32x32x16_bf16 %[a2], a[20:23], a[100:103], %[a2]\n"
    "ds_read_b64_tr_b16 a[12:13], %[rb2] offset:11264\n"
    "ds_read_b64_tr_b16 a[14:15], %[rb2] offset:11520\n"
    "s_waitcnt vmcnt(9)\n"
    "s_barrier\n"
    "s_waitcnt lgkmcnt(8)\n"
    "v_mfma_f32_32x32x16_bf16 %[a0], a[24:27], a[104:107], %[a0]\n"
    "ds_read_b64_tr_b16 a[16:17], %[rb0] offset:12288\n"
    "ds_read_b64_tr_b16 a[18:19], %[rb0] offset:12544\n"
    "v_mfma_f32_32x32x16_bf16 %[a1], a[28:31], a[104:107], %[a1]\n"
    "ds_read_b64_tr_b16 a[20:21], %[rb1] offset:12288\n"
    "ds_read_b64_tr_b16 a[22:23], %[rb1] offset:12544\n"
    "s_waitcnt lgkmcnt(8)\n"
    "v_mfma_f32_32x32x16_bf16 %[a2], a[0:3], a[104:107], %[a2]\n"
    "ds_read_b64_tr_b16 a[24:25], %[rb2] offset:12288\n"
    "ds_read_b64_tr_b16 a[26:27], %[rb2] offset:12544\n"
    "v_mov_b64 v[128:129], 0\n"
    "v_mfma_f32_32x32x16_bf16 %[a0], a[4:7], a[108:111], %[a0]\n"
    "ds_read_b64_tr_b16 a[28:29], %[rb0] offset:13312\n"
    "ds_read_b64_tr_b16 a[30:31], %[rb0] offset:13568\n"
    "s_waitcnt lgkmcnt(8)\n"
    "v_mfma_f32_32x32x16_bf16 %[a1], a[8:11], a[108:111], %[a1]\n"
    "ds_read_b64_tr_b16 a[0:1], %[rb1] offset:13312\n"
    "ds_read_b64_tr_b16 a[2:3], %[rb1] offset:13568\n"
    "v_mov_b64 v[130:131], 0\n"
    "v_mfma_f32_32x32x16_bf16 %[a2], a[12:15], a[108:111], %[a2]\n"
    "ds_read_b64_tr_b16 a[4:5], %[rb2] offset:13312\n"
    "ds_read_b64_tr_b16 a[6:7], %[rb2] offset:13568\n"
    "s_waitcnt lgkmcnt(8)\n"
    "v_mfma_f32_32x32x16_bf16 %[a0], a[16:19], a[112:115], %[a0]\n"
    "ds_read_b64_tr_b16 a[8:9], %[rb0] offset:14336\n"
    "ds_read_b64_tr_b16 a[10:11], %[rb0] offset:14592\n"
    "v_mov_b64 v[132:133], 0\n"
    "v_mfma_f32_32x32x16_bf16 %[a1], a[20:23], a[112:115], %[a1]\n"
    "ds_read_b64_tr_b16 a[12:13], %[rb1] offset:14336\n"
    "ds_read_b64_tr_b16 a[14:15], %[rb1] offset:14592\n"
    "s_waitcnt lgkmcnt(8)\n"
    "v_mfma_f32_32x32x16_bf16 %[a2], a[24:27], a[112:115], %[a2]\n"
    "ds_read_b64_tr_b16 a[16:17], %[rb2] offset:14336\n"
    "ds_read_b64_tr_b16 a[18:19], %[rb2] offset:14592\n"
    "v_mov_b64 v[134:135], 0\n"
    "v_mfma_f32_32x32x16_bf16 %[a0], a[28:31], a[116:119], %[a0]\n"
    "ds_read_b64_tr_b16 a[20:21], %[rb0] offset:15360\n"
    "ds_read_b64_tr_b16 a[22:23], %[rb0] offset:15616\n"
    "s_waitcnt lgkmcnt(8)\n"
    "v_mfma_f32_32x32x16_bf16 %[a1], a[0:3], a[116:119], %[a1]\n"
    "ds_read_b64_tr_b16 a[24:25], %[rb1] offset:15360\n"
    "ds_read_b64_tr_b16 a[26:27], %[rb1] offset:15616\n"
    "v_mov_b64 v[136:137], 0\n"
    "v_mfma_f32_32x32x16_bf16 %[a2], a[4:7], a[116:119], %[a2]\n"
    "ds_read_b64_tr_b16 a[28:29], %[rb2] offset:15360\n"
    "ds_read_b64_tr_b16 a[30:31], %[rb2] offset:15616\n"
    "s_waitcnt lgkmcnt(8)\n"
    "v_mfma_f32_32x32x16_bf16 %[a0], a[8:11], a[120:123], %[a0]\n"
    "ds_read_b64_tr_b16 a[0:1], %[rb0] offset:16384\n"
    "ds_read_b64_tr_b16 a[2:3], %[rb0] offset:16640\n"
    "v_mov_b64 v[138:139], 0\n"
    "v_mfma_f32_32x32x16_bf16 %[a1], a[12:15], a[120:123], %[a1]\n"
    "ds_read_b64_tr_b16 a[4:5], %[rb1] offset:16384\n"
    "ds_read_b64_tr_b16 a[6:7], %[rb1] offset:16640\n"
    "s_waitcnt lgkmcnt(8)\n"
    "v_mfma_f32_32x32x16_bf16 %[a2], a[16:19], a[120:123], %[a2]\n"
    "ds_read_b64_tr_b16 a[8:9], %[rb2] offset:16384\n"
    "ds_read_b64_tr_b16 a[10:11], %[rb2] offset:16640\n"
    "v_mov_b64 v[140:141], 0\n"
    "v_mfma_f32_32x32x16_bf16 %[a0], a[20:23], a[124:127], %[a0]\n"
    "ds_read_b64_tr_b16 a[12:13], %[rb0] offset:17408\n"
    "ds_read_b64_tr_b16 a[14:15], %[rb0] offset:17664\n"
    "s_waitcnt lgkmcnt(8)\n"
    "v_mfma_f32_32x32x16_bf16 %[a1], a[24:27], a[124:127], %[a1]\n"
    "ds_read_b64_tr_b16 a[16:17], %[rb1] offset:17408\n"
    "ds_read_b64_tr_b16 a[18:19], %[rb1] offset:17664\n"
    "v_mov_b64 v[142:143], 0\n"
    "v_mfma_f32_32x32x16_bf16 %[a2], a[28:31], a[124:127], %[a2]\n"
    "ds_read_b64_tr_b16 a[20:21], %[rb2] offset:17408\n"
    "ds_read_b64_tr_b16 a[22:23], %[rb2] offset:17664\n"
    "s_waitcnt lgkmcnt(8)\n"
    "v_mfma_f32_32x32x16_bf16 %[a0], a[0:3], a[128:131], %[a0]\n"
    "ds_read_b64_tr_b16 a[24:25], %[rb0] offset:18432\n"
    "ds_read_b64_tr_b16 a[26:27], %[rb0] offset:18688\n"
    "v_mov_b64 v[144:145], 0\n"
    "v_mfma_f32_32x32x16_bf16 %[a1], a[4:7], a[128:131], %[a1]\n"
    "ds_read_b64_tr_b16 a[28:29], %[rb1] offset:18432\n"
    "ds_read_b64_tr_b16 a[30:31], %[rb1] offset:18688\n"
    "s_waitcnt lgkmcnt(8)\n"
    "v_mfma_f32_32x32x16_bf16 %[a2], a[8:11], a[128:131], %[a2]\n"
    "ds_read_b64_tr_b16 a[0:1], %[rb2] offset:18432\n"
    "ds_read_b64_tr_b16 a[2:3], %[rb2] offset:18688\n"
    "v_mov_b64 v[146:147], 0\n"
    "v_mfma_f32_32x32x16_bf16 %[a0], a[12:15], a[132:135], %[a0]\n"
    "ds_read_b64_tr_b16 a[4:5], %[rb0] offset:19456\n"
    "ds_read_b64_tr_b16 a[6:7], %[rb0] offset:19712\n"
    "s_waitcnt lgkmcnt(8)\n"
    "v_mfma_f32_32x32x16_bf16 %[a1], a[16:19], a[132:135], %[a1]\n"
    "ds_read_b64_tr_b16 a[8:9], %[rb1] offset:19456\n"
    "ds_read_b64_tr_b16 a[10:11], %[rb1] offset:19712\n"
    "v_mov_b64 v[148:149], 0\n"
    "v_mfma_f32_32x32x16_bf16 %[a2], a[20:23], a[132:135], %[a2]\n"
    "ds_read_b64_tr_b16 a[12:13], %[rb2] offset:19456\n"
    "ds_read_b64_tr_b16 a[14:15], %[rb2] offset:19712\n"
    "s_waitcnt lgkmcnt(8)\n"
    "v_mfma_f32_32x32x16_bf16 %[a0], a[24:27], a[136:139], %[a0]\n"
    "ds_read_b64_tr_b16 a[16:17], %[rb0] offset:20480\n"
    "ds_read_b64_tr_b16 a[18:19], %[rb0] offset:20736\n"
    "v_mov_b64 v[150:151], 0\n"
    "v_mfma_f32_32x32x16_bf16 %[a1], a[28:31], a[136:139], %[a1]\n"
    "ds_read_b64_tr_b16 a[20:21], %[rb1] offset:20480\n"
    "ds_read_b64_tr_b16 a[22:23], %[rb1] offset:20736\n"
    "s_waitcnt lgkmcnt(8)\n"
    "v_mfma_f32_32x32x16_bf16 %[a2], a[0:3], a[136:139], %[a2]\n"
    "ds_read_b64_tr_b16 a[24:25], %[rb2] offset:20480\n"
    "ds_read_b64_tr_b16 a[26:27], %[rb2] offset:20736\n"
    "v_mov_b64 v[152:153], 0\n"
    "v_mfma_f32_32x32x16_bf16 %[a0], a[4:7], a[140:143], %[a0]\n"
    "ds_read_b64_tr_b16 a[28:29], %[rb0] offset:21504\n"
    "ds_read_b64_tr_b16 a[30:31], %[rb0] offset:21760\n"
    "s_waitcnt lgkmcnt(8)\n"
    "v_mfma_f32_32x32x16_bf16 %[a1], a[8:11], a[140:143], %[a1]\n"
    "ds_read_b64_tr_b16 a[0:1], %[rb1] offset:21504\n"
    "ds_read_b64_tr_b16 a[2:3], %[rb1] offset:21760\n"
    "v_mov_b64 v[154:155], 0\n"
    "v_mfma_f32_32x32x16_bf16 %[a2], a[12:15], a[140:143], %[a2]\n"
    "ds_read_b64_tr_b16 a[4:5], %[rb2] offset:21504\n"
    "ds_read_b64_tr_b16 a[6:7], %[rb2] offset:21760\n"
    "s_waitcnt lgkmcnt(8)\n"
    "v_mfma_f32_32x32x16_bf16 %[a0], a[16:19], a[144:147], %[a0]\n"
    "ds_read_b64_tr_b16 a[8:9], %[rb0] offset:22528\n"
    "ds_read_b64_tr_b16 a[10:11], %[rb0] offset:22784\n"
    "v_mov_b64 v[156:157], 0\n"
    "v_mfma_f32_32x32x16_bf16 %[a1], a[20:23], a[144:147], %[a1]\n"
    "ds_read_b64_tr_b16 a[12:13], %[rb1] offset:22528\n"
    "ds_read_b64_tr_b16 a[14:15], %[rb1] offset:22784\n"
    "s_waitcnt lgkmcnt(8)\n"
    "v_mfma_f32_32x32x16_bf16 %[a2], a[24:27], a[144:147], %[a2]\n"
    "ds_read_b64_tr_b16 a[16:17], %[rb2] offset:22528\n"
    "ds_read_b64_tr_b16 a[18:19], %[rb2] offset:22784\n"
    "v_mov_b64 v[158:159], 0\n"
    "v_mfma_f32_32x32x16_bf16 %[a0], a[28:31], a[148:151], %[a0]\n"
    "ds_read_b64_tr_b16 a[20:21], %[rb0] offset:23552\n"
    "ds_read_b64_tr_b16 a[22:23], %[rb0] offset:23808\n"
    "s_waitcnt lgkmcnt(8)\n"
    "v_mfma_f32_32x32x16_bf16 %[a1], a[0:3], a[148:151], %[a1]\n"
    "ds_read_b64_tr_b16 a[24:25], %[rb1] offset:23552\n"
    "ds_read_b64_tr_b16 a[26:27], %[rb1] offset:23808\n"
    "v_mov_b64 v[160:161], 0\n"
    "v_mfma_f32_32x32x16_bf16 %[a2], a[4:7], a[148:151], %[a2]\n"
    "ds_read_b64_tr_b16 a[28:29], %[rb2] offset:23552\n"
    "ds_read_b64_tr_b16 a[30:31], %[rb2] offset:23808\n"
    "s_waitcnt vmcnt(0)\n"
    "s_barrier\n"
    "s_waitcnt lgkmcnt(8)\n"
    "v_mfma_f32_32x32x16_bf16 %[a0], a[8:11], a[152:155], %[a0]\n"
    "ds_read_b64_tr_b16 a[0:1], %[rb0] offset:24576\n"
    "ds_read_b64_tr_b16 a[2:3], %[rb0] offset:24832\n"
    "v_mov_b64 v[162:163], 0\n"
    "v_mfma_f32_32x32x16_bf16 %[a1], a[12:15], a[152:155], %[a1]\n"
    "ds_read_b64_tr_b16 a[4:5], %[rb1] offset:24576\n"
    "ds_read_b64_tr_b16 a[6:7], %[rb1] offset:24832\n"
    "s_waitcnt lgkmcnt(8)\n"
    "v_mfma_f32_32x32x16_bf16 %[a2], a[16:19], a[152:155], %[a2]\n"
    "ds_read_b64_tr_b16 a[8:9], %[rb2] offset:24576\n"
    "ds_read_b64_tr_b16 a[10:11], %[rb2] offset:24832\n"
    "v_mov_b64 v[164:165], 0\n"
    "v_mfma_f32_32x32x16_bf16 %[a0], a[20:23], a[156:159], %[a0]\n"
    "ds_read_b64_tr_b16 a[12:13], %[rb0] offset:25600\n"
    "ds_read_b64_tr_b16 a[14:15], %[rb0] offset:25856\n"
    "s_waitcnt lgkmcnt(8)\n"
    "v_mfma_f32_32x32x16_bf16 %[a1], a[24:27], a[156:159], %[a1]\n"
    "ds_read_b64_tr_b16 a[16:17], %[rb1] offset:25600\n"
    "ds_read_b64_tr_b16 a[18:19], %[rb1] offset:25856\n"
    "v_mov_b64 v[166:167], 0\n"
    "v_mfma_f32_32x32x16_bf16 %[a2], a[28:31], a[156:159], %[a2]\n"
    "ds_read_b64_tr_b16 a[20:21], %[rb2] offset:25600\n"
    "ds_read_b64_tr_b16 a[22:23], %[rb2] offset:25856\n"
    "s_waitcnt lgkmcnt(8)\n"
    "v_mfma_f32_32x32x16_bf16 %[a0], a[0:3], a[160:163], %[a0]\n"
    "ds_read_b64_tr_b16 a[24:25], %[rb0] offset:26624\n"
    "ds_read_b64_tr_b16 a[26:27], %[rb0] offset:26880\n"
    "v_mov_b64 v[168:169], 0\n"
    "v_mfma_f32_32x32x16_bf16 %[a1], a[4:7], a[160:163], %[a1]\n"
    "ds_read_b64_tr_b16 a[28:29], %[rb1] offset:26624\n"
    "ds_read_b64_tr_b16 a[30:31], %[rb1] offset:26880\n"
    "s_waitcnt lgkmcnt(8)\n"
    "v_mfma_f32_32x32x16_bf16 %[a2], a[8:11], a[160:163], %[a2]\n"
    "ds_read_b64_tr_b16 a[0:1], %[rb2] offset:26624\n"
    "ds_read_b64_tr_b16 a[2:3], %[rb2] offset:26880\n"
    "v_mov_b64 v[170:171], 0\n"
    "v_mfma_f32_32x32x16_bf16 %[a0], a[12:15], a[164:167], %[a0]\n"
    "ds_read_b64_tr_b16 a[4:5], %[rb0] offset:27648\n"
    "ds_read_b64_tr_b16 a[6:7], %[rb0] offset:27904\n"
    "s_waitcnt lgkmcnt(8)\n"
    "v_mfma_f32_32x32x16_bf16 %[a1], a[16:19], a[164:167], %[a1]\n"
    "ds_read_b64_tr_b16 a[8:9], %[rb1] offset:27648\n"
    "ds_read_b64_tr_b16 a[10:11], %[rb1] offset:27904\n"
    "v_mov_b64 v[172:173], 0\n"
    "v_mfma_f32_32x32x16_bf16 %[a2], a[20:23], a[164:167], %[a2]\n"
    "ds_read_b64_tr_b16 a[12:13], %[rb2] offset:27648\n"
    "ds_read_b64_tr_b16 a[14:15], %[rb2] offset:27904\n"
    "s_waitcnt lgkmcnt(8)\n"
    "v_mfma_f32_32x32x16_bf16 %[a0], a[24:27], a[168:171], %[a0]\n"
    "ds_read_b64_tr_b16 a[16:17], %[rb0] offset:28672\n"
    "ds_read_b64_tr_b16 a[18:19], %[rb0] offset:28928\n"
    "v_mov_b64 v[174:175], 0\n"
    "v_mfma_f32_32x32x16_bf16 %[a1], a[28:31], a[168:171], %[a1]\n"
    "ds_read_b64_tr_b16 a[20:21], %[rb1] offset:28672\n"
    "ds_read_b64_tr_b16 a[22:23], %[rb1] offset:28928\n"
    "s_waitcnt lgkmcnt(8)\n"
    "v_mfma_f32_32x32x16_bf16 %[a2], a[0:3], a[168:171], %[a2]\n"
    "ds_read_b64_tr_b16 a[24:25], %[rb2] offset:28672\n"
    "ds_read_b64_tr_b16 a[26:27], %[rb2] offset:28928\n"
    "v_mov_b64 v[176:177], 0\n"
    "v_mfma_f32_32x32x16_bf16 %[a0], a[4:7], a[172:175], %[a0]\n"
    "ds_read_b64_tr_b16 a[28:29], %[rb0] offset:29696\n"
    "ds_read_b64_tr_b16 a[30:31], %[rb0] offset:29952\n"
    "s_waitcnt lgkmcnt(8)\n"
    "v_mfma_f32_32x32x16_bf16 %[a1], a[8:11], a[172:175], %[a1]\n"
    "ds_read_b64_tr_b16 a[0:1], %[rb1] offset:29696\n"
    "ds_read_b64_tr_b16 a[2:3], %[rb1] offset:29952\n"
    "v_mov_b64 v[178:179], 0\n"
    "v_mfma_f32_32x32x16_bf16 %[a2], a[12:15], a[172:175], %[a2]\n"
    "ds_read_b64_tr_b16 a[4:5], %[rb2] offset:29696\n"
    "ds_read_b64_tr_b16 a[6:7], %[rb2] offset:29952\n"
    "s_waitcnt lgkmcnt(8)\n"
    "v_mfma_f32_32x32x16_bf16 %[a0], a[16:19], a[176:179], %[a0]\n"
    "ds_read_b64_tr_b16 a[8:9], %[rb0] offset:30720\n"
    "ds_read_b64_tr_b16 a[10:11], %[rb0] offset:30976\n"
    "v_mov_b64 v[180:181], 0\n"
    "v_mfma_f32_32x32x16_bf16 %[a1], a[20:23], a[176:179], %[a1]\n"
    "ds_read_b64_tr_b16 a[12:13], %[rb1] offset:30720\n"
    "ds_read_b64_tr_b16 a[14:15], %[rb1] offset:30976\n"
    "buffer_load_dwordx2 %[fl], %[vfl], %[srdw], 0 offen sc1\n"
    "s_waitcnt lgkmcnt(8)\n"
    "v_mfma_f32_32x32x16_bf16 %[a2], a[24:27], a[176:179], %[a2]\n"
    "ds_read_b64_tr_b16 a[16:17], %[rb2] offset:30720\n"
    "ds_read_b64_tr_b16 a[18:19], %[rb2] offset:30976\n"
    "v_mov_b64 v[182:183], 0\n"
    "v_mfma_f32_32x32x16_bf16 %[a0], a[28:31], a[180:183], %[a0]\n"
    "ds_read_b64_tr_b16 a[20:21], %[rb0] offset:31744\n"
    "ds_read_b64_tr_b16 a[22:23], %[rb0] offset:32000\n"
    "s_waitcnt lgkmcnt(8)\n"
    "v_mfma_f32_32x32x16_bf16 %[a1], a[0:3], a[180:183], %[a1]\n"
    "ds_read_b64_tr_b16 a[24:25], %[rb1] offset:31744\n"
    "ds_read_b64_tr_b16 a[26:27], %[rb1] offset:32000\n"
    "v_mov_b64 v[184:185], 0\n"
    "v_mfma_f32_32x32x16_bf16 %[a2], a[4:7], a[180:183], %[a2]\n"
    "ds_read_b64_tr_b16 a[28:29], %[rb2] offset:31744\n"
    "ds_read_b64_tr_b16 a[30:31], %[rb2] offset:32000\n"
    "s_waitcnt lgkmcnt(8)\n"
    "v_mfma_f32_32x32x16_bf16 %[a0], a[8:11], a[184:187], %[a0]\n"
    "ds_read_b64_tr_b16 a[0:1], %[rb0] offset:32768\n"
    "ds_read_b64_tr_b16 a[2:3], %[rb0] offset:33024\n"
    "v_mov_b64 v[186:187], 0\n"
    "v_mfma_f32_32x32x16_bf16 %[a1], a[12:15], a[184:187], %[a1]\n"
    "ds_read_b64_tr_b16 a[4:5], %[rb1] offset:32768\n"
    "ds_read_b64_tr_b16 a[6:7], %[rb1] offset:33024\n"
    "s_waitcnt lgkmcnt(8)\n"
    "v_mfma_f32_32x32x16_bf16 %[a2], a[16:19], a[184:187], %[a2]\n"
    "ds_read_b64_tr_b16 a[8:9], %[rb2] offset:32768\n"
    "ds_read_b64_tr_b16 a[10:11], %[rb2] offset:33024\n"
    "v_mov_b64 v[188:189], 0\n"
    "v_mfma_f32_32x32x16_bf16 %[a0], a[20:23], a[188:191], %[a0]\n"
    "ds_read_b64_tr_b16 a[12:13], %[rb0] offset:33792\n"
    "ds_read_b64_tr_b16 a[14:15], %[rb0] offset:34048\n"
    "s_waitcnt lgkmcnt(8)\n"
    "v_mfma_f32_32x32x16_bf16 %[a1], a[24:27], a[188:191], %[a1]\n"
    "ds_read_b64_tr_b16 a[16:17], %[rb1] offset:33792\n"
    "ds_read_b64_tr_b16 a[18:19], %[rb1] offset:34048\n"
    "v_mov_b64 v[190:191], 0\n"
    "v_mfma_f32_32x32x16_bf16 %[a2], a[28:31], a[188:191], %[a2]\n"
    "ds_read_b64_tr_b16 a[20:21], %[rb2] offset:33792\n"
    "ds_read_b64_tr_b16 a[22:23], %[rb2] offset:34048\n"
    "s_waitcnt lgkmcnt(8)\n"
    "v_mfma_f32_32x32x16_bf16 %[a0], a[0:3], a[192:195], %[a0]\n"
    "ds_read_b64_tr_b16 a[24:25], %[rb0] offset:34816\n"
    "ds_read_b64_tr_b16 a[26:27], %[rb0] offset:35072\n"
    "v_mfma_f32_32x32x16_bf16 %[a1], a[4:7], a[192:195], %[a1]\n"
    "ds_read_b64_tr_b16 a[28:29], %[rb1] offset:34816\n"
    "ds_read_b64_tr_b16 a[30:31], %[rb1] offset:35072\n"
    "s_waitcnt lgkmcnt(8)\n"
    "v_mfma_f32_32x32x16_bf16 %[a2], a[8:11], a[192:195], %[a2]\n"
    "ds_read_b64_tr_b16 a[0:1], %[rb2] offset:34816\n"
    "ds_read_b64_tr_b16 a[2:3], %[rb2] offset:35072\n"
    "v_mfma_f32_32x32x16_bf16 %[a0], a[12:15], a[196:199], %[a0]\n"
    "ds_read_b64_tr_b16 a[4:5], %[rb0] offset:35840\n"
    "ds_read_b64_tr_b16 a[6:7], %[rb0] offset:36096\n"
    "s_waitcnt lgkmcnt(8)\n"
    "v_mfma_f32_32x32x16_bf16 %[a1], a[16:19], a[196:199], %[a1]\n"
    "ds_read_b64_tr_b16 a[8:9], %[rb1] offset:35840\n"
    "ds_read_b64_tr_b16 a[10:11], %[rb1] offset:36096\n"
    "v_mfma_f32_32x32x16_bf16 %[a2], a[20:23], a[196:199], %[a2]\n"
    "ds_read_b64_tr_b16 a[12:13], %[rb2] offset:35840\n"
    "ds_read_b64_tr_b16 a[14:15], %[rb2] offset:36096\n"
    "s_waitcnt lgkmcnt(8)\n"
    "v_mfma_f32_32x32x16_bf16 %[a0], a[24:27], a[200:203], %[a0]\n"
    "v_mfma_f32_32x32x16_bf16 %[a1], a[28:31], a[200:203], %[a1]\n"
    "s_waitcnt lgkmcnt(4)\n"
    "v_mfma_f32_32x32x16_bf16 %[a2], a[0:3], a[200:203], %[a2]\n"
    "v_mfma_f32_32x32x16_bf16 %[a0], a[4:7], a[204:207], %[a0]\n"
    "s_waitcnt lgkmcnt(0)\n"
    "v_mfma_f32_32x32x16_bf16 %[a1], a[8:11], a[204:207], %[a1]\n"
    "v_mfma_f32_32x32x16_bf16 %[a2], a[12:15], a[204:207], %[a2]\n"
    "s_waitcnt vmcnt(0)\n"
    "s_nop 7\n"
    "s_nop 7\n"
        : [a0] "=&v"(a0), [a1] "=&v"(a1), [a2] "=&v"(a2), [fl] "=&v"(fl), "=&{v[128:143]}"(o[0]), "=&{v[144:159]}"(o[1]), "=&{v[160:175]}"(o[2]), "=&{v[176:191]}"(o[3])
        : "{a[64:79]}"(xa[0]), "{a[80:95]}"(xa[1]), "{a[96:111]}"(xa[2]), "{a[112:127]}"(xa[3]), "{a[128:143]}"(xa[4]), "{a[144:159]}"(xa[5]), "{a[160:175]}"(xa[6]), "{a[176:191]}"(xa[7]), "{a[192:207]}"(xa[8]), [rb0] "v"(rb), [rb1] "v"(rb + 36864), [rb2] "v"(rb + 73728), [vfl] "v"(OFF_FLAG + xcd * 256 + q * 8), [srdw] "s"(srdw), Q_IN
        : "a0", "a1", "a2", "a3", "a4", "a5", "a6", "a7", "a8", "a9", "a10", "a11", "a12", "a13", "a14", "a15", "a16", "a17", "a18", "a19", "a20", "a21", "a22", "a23", "a24", "a25", "a26", "a27", "a28", "a29", "a30", "a31", "s40", "scc", "memory");
    while (!__all(fl[0] == tag)) {
      __builtin_amdgcn_s_sleep(1);
      fl = __builtin_amdgcn_raw_buffer_load_b64(rw, OFF_FLAG + xcd * 256 + q * 8, 0, SC1);
    }
    asm volatile(
    "s_nop 4\n"
    "s_mov_b32 s40, 0\n"
    "buffer_load_dwordx4 a[0:3], %[vk0], %[srd], s40 offen sc1\n"
    "buffer_load_dwordx2 a[12:13], %[vk1], %[srd], s40 offen sc1\n"
    "buffer_load_dwordx4 a[4:7], %[vv], %[srd], s40 offen sc1\n"
    "buffer_load_dwordx4 a[8:11], %[vv], %[srd], s40 offen offset:608 sc1\n"
    "s_add_u32 s40, s40, 2560\n"
    "buffer_load_dwordx4 a[16:19], %[vk0], %[srd], s40 offen sc1\n"
    "buffer_load_dwordx2 a[28:29], %[vk1], %[srd], s40 offen sc1\n"
    "buffer_load_dwordx4 a[20:23], %[vv], %[srd], s40 offen sc1\n"
    "buffer_load_dwordx4 a[24:27], %[vv], %[srd], s40 offen offset:608 sc1\n"
    "s_add_u32 s40, s40, 2560\n"
    "buffer_load_dwordx4 a[32:35], %[vk0], %[srd], s40 offen sc1\n"
    "buffer_load_dwordx2 a[44:45], %[vk1], %[srd], s40 offen sc1\n"
    "buffer_load_dwordx4 a[36:39], %[vv], %[srd], s40 offen sc1\n"
    "buffer_load_dwordx4 a[40:43], %[vv], %[srd], s40 offen offset:608 sc1\n"
    "s_add_u32 s40, s40, 2560\n"
        : "={a[0:15]}"(kv0), "={a[16:31]}"(kv1), "={a[32:47]}"(kv2), "+v"(a0), "+v"(a1), "+v"(a2), "+v"(fl)
        : KV_IN
        : "s40", "scc", "memory");
    q0[0] = cvt8(a0, 0, 1.f); q0[1] = cvt8(a0, 8, 1.f); q0[2] = cvt8(a1, 0, 1.f); q0[3] = cvt8(a1, 8, 1.f);
#pragma unroll
    for (int r = 0; r < 4; r++) q1[r][0] = cvtpk(a2[4 * r + 2], a2[4 * r + 3]) & hm;
  }
  const float kmax2 = half_max(fl[1]) * 1.002f;
#pragma unroll
  for (int r = 0; r < 4; r++) {
    float n2 = sq2(q1[r][0], 0.f);
#pragma unroll
    for (int e = 0; e < 4; e++) n2 = sq2(q0[r][e], n2);
    const float c = __builtin_amdgcn_sqrtf(xhalf_sum(n2) * kmax2);
    q1[r][1] = (((__float_as_uint(c) + 0xffffu) >> 16) | 0x8000u) & hm;
  }
  asm volatile(
    "s_nop 4\n"
    "s_mov_b32 s40, 7680\n"
    "v_mov_b32 v120, 0\n"
    "v_mov_b32 v121, 0\n"
    "v_mov_b32 v122, 0\n"
    "v_mov_b32 v123, 0\n"
    "v_mov_b32 v124, 0\n"
    "v_mov_b32 v125, 0\n"
    "v_mov_b32 v126, 0\n"
    "v_mov_b32 v127, 0\n"
    "v_accvgpr_write_b32 a56, 0\n"
    "v_accvgpr_write_b32 a57, 0\n"
    "v_accvgpr_write_b32 a58, 0\n"
    "v_accvgpr_write_b32 a59, 0\n"
    "s_waitcnt vmcnt(8)\n"
    "v_mfma_f32_32x32x16_bf16 v[64:79], a[0:3], %[q00], 0\n"
    "v_mfma_f32_32x32x8bf16_1k v[64:79], a[12:13], %[q10], v[64:79]\n"
    "s_nop 7\n"
    "s_nop 7\n"
    "1:\n"
    "v_mfma_f32_32x32x16_bf16 v[80:95], a[0:3], %[q01], 0\n"
    "v_exp_f32 v64, v64\n"
    "v_exp_f32 v65, v65\n"
    "v_exp_f32 v66, v66\n"
    "v_exp_f32 v67, v67\n"
    "v_mfma_f32_32x32x8bf16_1k v[80:95], a[12:13], %[q11], v[80:95]\n"
    "v_exp_f32 v68, v68\n"
    "v_exp_f32 v69, v69\n"
    "v_exp_f32 v70, v70\n"
    "v_exp_f32 v71, v71\n"
    "v_smfmac_f32_32x32x32_bf16 v[176:191], a[56:59], v[120:127], %[idx]\n"
    "v_exp_f32 v72, v72\n"
    "v_exp_f32 v73, v73\n"
    "v_exp_f32 v74, v74\n"
    "v_exp_f32 v75, v75\n"
    "v_smfmac_f32_32x32x32_bf16 v[128:143], a[4:7], v[64:71], %[idx]\n"
    "v_exp_f32 v76, v76\n"
    "v_exp_f32 v77, v77\n"
    "v_exp_f32 v78, v78\n"
    "v_exp_f32 v79, v79\n"
    "v_mfma_f32_32x32x16_bf16 v[96:111], a[0:3], %[q02], 0\n"
    "buffer_load_dwordx4 a[48:51], %[vk0], %[srd], s40 offen sc1\n"
    "v_exp_f32 v80, v80\n"
    "v_exp_f32 v81, v81\n"
    "v_exp_f32 v82, v82\n"
    "v_exp_f32 v83, v83\n"
    "v_mfma_f32_32x32x8bf16_1k v[96:111], a[12:13], %[q12], v[96:111]\n"
    "buffer_load_dwordx2 a[60:61], %[vk1], %[srd], s40 offen sc1\n"
    "v_exp_f32 v84, v84\n"
    "v_exp_f32 v85, v85\n"
    "v_exp_f32 v86, v86\n"
    "v_exp_f32 v87, v87\n"
    "v_smfmac_f32_32x32x32_bf16 v[128:143], a[8:11], v[72:79], %[idx]\n"
    "v_exp_f32 v88, v88\n"
    "v_exp_f32 v89, v89\n"
    "v_exp_f32 v90, v90\n"
    "v_exp_f32 v91, v91\n"
    "v_smfmac_f32_32x32x32_bf16 v[144:159], a[4:7], v[80:87], %[idx]\n"
    "v_exp_f32 v92, v92\n"
    "v_exp_f32 v93, v93\n"
    "v_exp_f32 v94, v94\n"
    "v_exp_f32 v95, v95\n"
    "v_mfma_f32_32x32x16_bf16 v[112:127], a[0:3], %[q03], 0\n"
    "buffer_load_dwordx4 a[52:55], %[vv], %[srd], s40 offen sc1\n"
    "v_exp_f32 v96, v96\n"
    "v_exp_f32 v97, v97\n"
    "v_exp_f32 v98, v98\n"
    "v_exp_f32 v99, v99\n"
    "v_mfma_f32_32x32x8bf16_1k v[112:127], a[12:13], %[q13], v[112:127]\n"
    "buffer_load_dwordx4 a[56:59], %[vv], %[srd], s40 offen offset:608 sc1\n"
    "v_exp_f32 v100, v100\n"
    "v_exp_f32 v101, v101\n"
    "v_exp_f32 v102, v102\n"
    "v_exp_f32 v103, v103\n"
    "v_smfmac_f32_32x32x32_bf16 v[144:159], a[8:11], v[88:95], %[idx]\n"
    "s_add_u32 s40, s40, 2560\n"
    "v_exp_f32 v104, v104\n"
    "v_exp_f32 v105, v105\n"
    "v_exp_f32 v106, v106\n"
    "v_exp_f32 v107, v107\n"
    "v_smfmac_f32_32x32x32_bf16 v[160:175], a[4:7], v[96:103], %[idx]\n"
    "s_waitcnt vmcnt(8)\n"
    "v_exp_f32 v108, v108\n"
    "v_exp_f32 v109, v109\n"
    "v_exp_f32 v110, v110\n"
    "v_exp_f32 v111, v111\n"
    "v_mfma_f32_32x32x16_bf16 v[64:79], a[16:19], %[q00], 0\n"
    "v_exp_f32 v112, v112\n"
    "v_exp_f32 v113, v113\n"
    "v_exp_f32 v114, v114\n"
    "v_exp_f32 v115, v115\n"
    "v_mfma_f32_32x32x8bf16_1k v[64:79], a[28:29], %[q10], v[64:79]\n"
    "v_exp_f32 v116, v116\n"
    "v_exp_f32 v117, v117\n"
    "v_exp_f32 v118, v118\n"
    "v_exp_f32 v119, v119\n"
    "v_smfmac_f32_32x32x32_bf16 v[160:175], a[8:11], v[104:111], %[idx]\n"
    "v_exp_f32 v120, v120\n"
    "v_exp_f32 v121, v121\n"
    "v_exp_f32 v122, v122\n"
    "v_exp_f32 v123, v123\n"
    "v_smfmac_f32_32x32x32_bf16 v[176:191], a[4:7], v[112:119], %[idx]\n"
    "v_exp_f32 v124, v124\n"
    "v_exp_f32 v125, v125\n"
    "v_exp_f32 v126, v126\n"
    "v_exp_f32 v127, v127\n"
    "v_mfma_f32_32x32x16_bf16 v[80:95], a[16:19], %[q01], 0\n"
    "v_exp_f32 v64, v64\n"
    "v_exp_f32 v65, v65\n"
    "v_exp_f32 v66, v66\n"
    "v_exp_f32 v67, v67\n"
    "v_mfma_f32_32x32x8bf16_1k v[80:95], a[28:29], %[q11], v[80:95]\n"
    "v_exp_f32 v68, v68\n"
    "v_exp_f32 v69, v69\n"
    "v_exp_f32 v70, v70\n"
    "v_exp_f32 v71, v71\n"
    "v_smfmac_f32_32x32x32_bf16 v[176:191], a[8:11], v[120:127], %[idx]\n"
    "v_exp_f32 v72, v72\n"
    "v_exp_f32 v73, v73\n"
    "v_exp_f32 v74, v74\n"
    "v_exp_f32 v75, v75\n"
    "v_smfmac_f32_32x32x32_bf16 v[128:143], a[20:23], v[64:71], %[idx]\n"
    "v_exp_f32 v76, v76\n"
    "v_exp_f32 v77, v77\n"
    "v_exp_f32 v78, v78\n"
    "v_exp_f32 v79, v79\n"
    "v_mfma_f32_32x32x16_bf16 v[96:111], a[16:19], %[q02], 0\n"
    "buffer_load_dwordx4 a[0:3], %[vk0], %[srd], s40 offen sc1\n"
    "v_exp_f32 v80, v80\n"
    "v_exp_f32 v81, v81\n"
    "v_exp_f32 v82, v82\n"
    "v_exp_f32 v83, v83\n"
    "v_mfma_f32_32x32x8bf16_1k v[96:111], a[28:29], %[q12], v[96:111]\n"
    "buffer_load_dwordx2 a[12:13], %[vk1], %[srd], s40 offen sc1\n"
    "v_exp_f32 v84, v84\n"
    "v_exp_f32 v85, v85\n"
    "v_exp_f32 v86, v86\n"
    "v_exp_f32 v87, v87\n"
    "v_smfmac_f32_32x32x32_bf16 v[128:143], a[24:27], v[72:79], %[idx]\n"
    "v_exp_f32 v88, v88\n"
    "v_exp_f32 v89, v89\n"
    "v_exp_f32 v90, v90\n"
    "v_exp_f32 v91, v91\n"
    "v_smfmac_f32_32x32x32_bf16 v[144:159], a[20:23], v[80:87], %[idx]\n"
    "v_exp_f32 v92, v92\n"
    "v_exp_f32 v93, v93\n"
    "v_exp_f32 v94, v94\n"
    "v_exp_f32 v95, v95\n"
    "v_mfma_f32_32x32x16_bf16 v[112:127], a[16:19], %[q03], 0\n"
    "buffer_load_dwordx4 a[4:7], %[vv], %[srd], s40 offen sc1\n"
    "v_exp_f32 v96, v96\n"
    "v_exp_f32 v97, v97\n"
    "v_exp_f32 v98, v98\n"
    "v_exp_f32 v99, v99\n"
    "v_mfma_f32_32x32x8bf16_1k v[112:127], a[28:29], %[q13], v[112:127]\n"
    "buffer_load_dwordx4 a[8:11], %[vv], %[srd], s40 offen offset:608 sc1\n"
    "v_exp_f32 v100, v100\n"
    "v_exp_f32 v101, v101\n"
    "v_exp_f32 v102, v102\n"
    "v_exp_f32 v103, v103\n"
    "v_smfmac_f32_32x32x32_bf16 v[144:159], a[24:27], v[88:95], %[idx]\n"
    "s_add_u32 s40, s40, 2560\n"
    "v_exp_f32 v104, v104\n"
    "v_exp_f32 v105, v105\n"
    "v_exp_f32 v106, v106\n"
    "v_exp_f32 v107, v107\n"
    "v_smfmac_f32_32x32x32_bf16 v[160:175], a[20:23], v[96:103], %[idx]\n"
    "s_waitcnt vmcnt(8)\n"
    "v_exp_f32 v108, v108\n"
    "v_exp_f32 v109, v109\n"
    "v_exp_f32 v110, v110\n"
    "v_exp_f32 v111, v111\n"
    "v_mfma_f32_32x32x16_bf16 v[64:79], a[32:35], %[q00], 0\n"
    "v_exp_f32 v112, v112\n"
    "v_exp_f32 v113, v113\n"
    "v_exp_f32 v114, v114\n"
    "v_exp_f32 v115, v115\n"
    "v_mfma_f32_32x32x8bf16_1k v[64:79], a[44:45], %[q10], v[64:79]\n"
    "v_exp_f32 v116, v116\n"
    "v_exp_f32 v117, v117\n"
    "v_exp_f32 v118, v118\n"
    "v_exp_f32 v119, v119\n"
    "v_smfmac_f32_32x32x32_bf16 v[160:175], a[24:27], v[104:111], %[idx]\n"
    "v_exp_f32 v120, v120\n"
    "v_exp_f32 v121, v121\n"
    "v_exp_f32 v122, v122\n"
    "v_exp_f32 v123, v123\n"
    "v_smfmac_f32_32x32x32_bf16 v[176:191], a[20:23], v[112:119], %[idx]\n"
    "v_exp_f32 v124, v124\n"
    "v_exp_f32 v125, v125\n"
    "v_exp_f32 v126, v126\n"
    "v_exp_f32 v127, v127\n"
    "v_mfma_f32_32x32x16_bf16 v[80:95], a[32:35], %[q01], 0\n"
    "v_exp_f32 v64, v64\n"
    "v_exp_f32 v65, v65\n"
    "v_exp_f32 v66, v66\n"
    "v_exp_f32 v67, v67\n"
    "v_mfma_f32_32x32x8bf16_1k v[80:95], a[44:45], %[q11], v[80:95]\n"
    "v_exp_f32 v68, v68\n"
    "v_exp_f32 v69, v69\n"
    "v_exp_f32 v70, v70\n"
    "v_exp_f32 v71, v71\n"
    "v_smfmac_f32_32x32x32_bf16 v[176:191], a[24:27], v[120:127], %[idx]\n"
    "v_exp_f32 v72, v72\n"
    "v_exp_f32 v73, v73\n"
    "v_exp_f32 v74, v74\n"
    "v_exp_f32 v75, v75\n"
    "v_smfmac_f32_32x32x32_bf16 v[128:143], a[36:39], v[64:71], %[idx]\n"
    "v_exp_f32 v76, v76\n"
    "v_exp_f32 v77, v77\n"
    "v_exp_f32 v78, v78\n"
    "v_exp_f32 v79, v79\n"
    "v_mfma_f32_32x32x16_bf16 v[96:111], a[32:35], %[q02], 0\n"
    "buffer_load_dwordx4 a[16:19], %[vk0], %[srd], s40 offen sc1\n"
    "v_exp_f32 v80, v80\n"
    "v_exp_f32 v81, v81\n"
    "v_exp_f32 v82, v82\n"
    "v_exp_f32 v83, v83\n"
    "v_mfma_f32_32x32x8bf16_1k v[96:111], a[44:45], %[q12], v[96:111]\n"
    "buffer_load_dwordx2 a[28:29], %[vk1], %[srd], s40 offen sc1\n"
    "v_exp_f32 v84, v84\n"
    "v_exp_f32 v85, v85\n"
    "v_exp_f32 v86, v86\n"
    "v_exp_f32 v87, v87\n"
    "v_smfmac_f32_32x32x32_bf16 v[128:143], a[40:43], v[72:79], %[idx]\n"
    "v_exp_f32 v88, v88\n"
    "v_exp_f32 v89, v89\n"
    "v_exp_f32 v90, v90\n"
    "v_exp_f32 v91, v91\n"
    "v_smfmac_f32_32x32x32_bf16 v[144:159], a[36:39], v[80:87], %[idx]\n"
    "v_exp_f32 v92, v92\n"
    "v_exp_f32 v93, v93\n"
    "v_exp_f32 v94, v94\n"
    "v_exp_f32 v95, v95\n"
    "v_mfma_f32_32x32x16_bf16 v[112:127], a[32:35], %[q03], 0\n"
    "buffer_load_dwordx4 a[20:23], %[vv], %[srd], s40 offen sc1\n"
    "v_exp_f32 v96, v96\n"
    "v_exp_f32 v97, v97\n"
    "v_exp_f32 v98, v98\n"
    "v_exp_f32 v99, v99\n"
    "v_mfma_f32_32x32x8bf16_1k v[112:127], a[44:45], %[q13], v[112:127]\n"
    "buffer_load_dwordx4 a[24:27], %[vv], %[srd], s40 offen offset:608 sc1\n"
    "v_exp_f32 v100, v100\n"
    "v_exp_f32 v101, v101\n"
    "v_exp_f32 v102, v102\n"
    "v_exp_f32 v103, v103\n"
    "v_smfmac_f32_32x32x32_bf16 v[144:159], a[40:43], v[88:95], %[idx]\n"
    "s_add_u32 s40, s40, 2560\n"
    "v_exp_f32 v104, v104\n"
    "v_exp_f32 v105, v105\n"
    "v_exp_f32 v106, v106\n"
    "v_exp_f32 v107, v107\n"
    "v_smfmac_f32_32x32x32_bf16 v[160:175], a[36:39], v[96:103], %[idx]\n"
    "s_waitcnt vmcnt(8)\n"
    "v_exp_f32 v108, v108\n"
    "v_exp_f32 v109, v109\n"
    "v_exp_f32 v110, v110\n"
    "v_exp_f32 v111, v111\n"
    "v_mfma_f32_32x32x16_bf16 v[64:79], a[48:51], %[q00], 0\n"
    "v_exp_f32 v112, v112\n"
    "v_exp_f32 v113, v113\n"
    "v_exp_f32 v114, v114\n"
    "v_exp_f32 v115, v115\n"
    "v_mfma_f32_32x32x8bf16_1k v[64:79], a[60:61], %[q10], v[64:79]\n"
    "v_exp_f32 v116, v116\n"
    "v_exp_f32 v117, v117\n"
    "v_exp_f32 v118, v118\n"
    "v_exp_f32 v119, v119\n"
    "v_smfmac_f32_32x32x32_bf16 v[160:175], a[40:43], v[104:111], %[idx]\n"
    "v_exp_f32 v120, v120\n"
    "v_exp_f32 v121, v121\n"
    "v_exp_f32 v122, v122\n"
    "v_exp_f32 v123, v123\n"
    "v_smfmac_f32_32x32x32_bf16 v[176:191], a[36:39], v[112:119], %[idx]\n"
    "v_exp_f32 v124, v124\n"
    "v_exp_f32 v125, v125\n"
    "v_exp_f32 v126, v126\n"
    "v_exp_f32 v127, v127\n"
    "v_mfma_f32_32x32x16_bf16 v[80:95], a[48:51], %[q01], 0\n"
    "v_exp_f32 v64, v64\n"
    "v_exp_f32 v65, v65\n"
    "v_exp_f32 v66, v66\n"
    "v_exp_f32 v67, v67\n"
    "v_mfma_f32_32x32x8bf16_1k v[80:95], a[60:61], %[q11], v[80:95]\n"
    "v_exp_f32 v68, v68\n"
    "v_exp_f32 v69, v69\n"
    "v_exp_f32 v70, v70\n"
    "v_exp_f32 v71, v71\n"
    "v_smfmac_f32_32x32x32_bf16 v[176:191], a[40:43], v[120:127], %[idx]\n"
    "v_exp_f32 v72, v72\n"
    "v_exp_f32 v73, v73\n"
    "v_exp_f32 v74, v74\n"
    "v_exp_f32 v75, v75\n"
    "v_smfmac_f32_32x32x32_bf16 v[128:143], a[52:55], v[64:71], %[idx]\n"
    "v_exp_f32 v76, v76\n"
    "v_exp_f32 v77, v77\n"
    "v_exp_f32 v78, v78\n"
    "v_exp_f32 v79, v79\n"
    "v_mfma_f32_32x32x16_bf16 v[96:111], a[48:51], %[q02], 0\n"
    "buffer_load_dwordx4 a[32:35], %[vk0], %[srd], s40 offen sc1\n"
    "v_exp_f32 v80, v80\n"
    "v_exp_f32 v81, v81\n"
    "v_exp_f32 v82, v82\n"
    "v_exp_f32 v83, v83\n"
    "v_mfma_f32_32x32x8bf16_1k v[96:111], a[60:61], %[q12], v[96:111]\n"
    "buffer_load_dwordx2 a[44:45], %[vk1], %[srd], s40 offen sc1\n"
    "v_exp_f32 v84, v84\n"
    "v_exp_f32 v85, v85\n"
    "v_exp_f32 v86, v86\n"
    "v_exp_f32 v87, v87\n"
    "v_smfmac_f32_32x32x32_bf16 v[128:143], a[56:59], v[72:79], %[idx]\n"
    "v_exp_f32 v88, v88\n"
    "v_exp_f32 v89, v89\n"
    "v_exp_f32 v90, v90\n"
    "v_exp_f32 v91, v91\n"
    "v_smfmac_f32_32x32x32_bf16 v[144:159], a[52:55], v[80:87], %[idx]\n"
    "v_exp_f32 v92, v92\n"
    "v_exp_f32 v93, v93\n"
    "v_exp_f32 v94, v94\n"
    "v_exp_f32 v95, v95\n"
    "v_mfma_f32_32x32x16_bf16 v[112:127], a[48:51], %[q03], 0\n"
    "buffer_load_dwordx4 a[36:39], %[vv], %[srd], s40 offen sc1\n"
    "v_exp_f32 v96, v96\n"
    "v_exp_f32 v97, v97\n"
    "v_exp_f32 v98, v98\n"
    "v_exp_f32 v99, v99\n"
    "v_mfma_f32_32x32x8bf16_1k v[112:127], a[60:61], %[q13], v[112:127]\n"
    "buffer_load_dwordx4 a[40:43], %[vv], %[srd], s40 offen offset:608 sc1\n"
    "v_exp_f32 v100, v100\n"
    "v_exp_f32 v101, v101\n"
    "v_exp_f32 v102, v102\n"
    "v_exp_f32 v103, v103\n"
    "v_smfmac_f32_32x32x32_bf16 v[144:159], a[56:59], v[88:95], %[idx]\n"
    "s_add_u32 s40, s40, 2560\n"
    "v_exp_f32 v104, v104\n"
    "v_exp_f32 v105, v105\n"
    "v_exp_f32 v106, v106\n"
    "v_exp_f32 v107, v107\n"
    "v_smfmac_f32_32x32x32_bf16 v[160:175], a[52:55], v[96:103], %[idx]\n"
    "s_waitcnt vmcnt(8)\n"
    "v_exp_f32 v108, v108\n"
    "v_exp_f32 v109, v109\n"
    "v_exp_f32 v110, v110\n"
    "v_exp_f32 v111, v111\n"
    "v_mfma_f32_32x32x16_bf16 v[64:79], a[0:3], %[q00], 0\n"
    "v_exp_f32 v112, v112\n"
    "v_exp_f32 v113, v113\n"
    "v_exp_f32 v114, v114\n"
    "v_exp_f32 v115, v115\n"
    "v_mfma_f32_32x32x8bf16_1k v[64:79], a[12:13], %[q10], v[64:79]\n"
    "v_exp_f32 v116, v116\n"
    "v_exp_f32 v117, v117\n"
    "v_exp_f32 v118, v118\n"
    "v_exp_f32 v119, v119\n"
    "v_smfmac_f32_32x32x32_bf16 v[160:175], a[56:59], v[104:111], %[idx]\n"
    "s_cmp_lt_u32 s40, 171520\n"
    "v_exp_f32 v120, v120\n"
    "v_exp_f32 v121, v121\n"
    "v_exp_f32 v122, v122\n"
    "v_exp_f32 v123, v123\n"
    "v_smfmac_f32_32x32x32_bf16 v[176:191], a[52:55], v[112:119], %[idx]\n"
    "v_exp_f32 v124, v124\n"
    "v_exp_f32 v125, v125\n"
    "v_exp_f32 v126, v126\n"
    "v_exp_f32 v127, v127\n"
    "s_cbranch_scc1 1b\n"
    "s_nop 1\n"
    "v_smfmac_f32_32x32x32_bf16 v[176:191], a[56:59], v[120:127], %[idx]\n"
    "s_waitcnt vmcnt(0)\n"
    "s_nop 7\n"
    "s_nop 7\n"
      : "+{v[128:143]}"(o[0]), "+{v[144:159]}"(o[1]), "+{v[160:175]}"(o[2]), "+{v[176:191]}"(o[3]), "+{a[0:15]}"(kv0), "+{a[16:31]}"(kv1), "+{a[32:47]}"(kv2)
      : [q00] "v"(q0[0]), [q01] "v"(q0[1]), [q02] "v"(q0[2]), [q03] "v"(q0[3]), [q10] "v"(q1[0]), [q11] "v"(q1[1]), [q12] "v"(q1[2]), [q13] "v"(q1[3]),
        [idx] "v"(0xddddddddu), KV_IN
      : "v64", "v65", "v66", "v67", "v68", "v69", "v70", "v71", "v72", "v73", "v74", "v75", "v76", "v77", "v78", "v79", "v80", "v81", "v82", "v83", "v84", "v85", "v86", "v87", "v88", "v89", "v90", "v91", "v92", "v93", "v94", "v95", "v96", "v97", "v98", "v99", "v100", "v101", "v102", "v103", "v104", "v105", "v106", "v107", "v108", "v109", "v110", "v111", "v112", "v113", "v114", "v115", "v116", "v117", "v118", "v119", "v120", "v121", "v122", "v123", "v124", "v125", "v126", "v127", "a48", "a49", "a50", "a51", "a52", "a53", "a54", "a55", "a56", "a57", "a58", "a59", "a60", "a61", "a62", "a63", "s40", "scc", "memory");
  bool bad = false;
#pragma unroll
  for (int r = 0; r < 4; r++) bad |= !(o[r][10] >= 8.27e-25f && o[r][10] < 3e38f);
  if (__any(bad)) {
#pragma unroll 1
    for (int r = 0; r < 4; r++) {
      const u32x4 qa = r == 0 ? q0[0] : r == 1 ? q0[1] : r == 2 ? q0[2] : q0[3];
      const unsigned qb = r == 0 ? q1[0][0] : r == 1 ? q1[1][0] : r == 2 ? q1[2][0] : q1[3][0];
      unsigned qq[9];
#pragma unroll
      for (int e = 0; e < 4; e++) { const unsigned p = __shfl_xor(qa[e], 32); qq[e] = h ? p : qa[e]; qq[4 + e] = h ? qa[e] : p; }
      qq[8] = qb | __shfl_xor(qb, 32);
      float oo[18], m = -__builtin_inff(), sum = 0.f;
#pragma unroll
      for (int d = 0; d < 18; d++) oo[d] = 0.f;
#pragma unroll 1
      for (int key = h * 1024; key < h * 1024 + 1024; key++) {
        const unsigned rec = OFF_KV + (key >> 5) * KVREC, kk = key & 31;
        const u32x4 ka = __builtin_amdgcn_raw_buffer_load_b128(rw, rec + kk * 16, 0, SC1), kb = __builtin_amdgcn_raw_buffer_load_b128(rw, rec + 512 + kk * 16, 0, SC1);
        const unsigned kc = __builtin_amdgcn_raw_buffer_load_b32(rw, rec + 1024 + kk * 8, 0, SC1);
        float s = blo(qq[8]) * blo(kc) + bhi(qq[8]) * bhi(kc);
#pragma unroll
        for (int e = 0; e < 4; e++) s += blo(qq[e]) * blo(ka[e]) + bhi(qq[e]) * bhi(ka[e]) + blo(qq[4 + e]) * blo(kb[e]) + bhi(qq[4 + e]) * bhi(kb[e]);
        const float mn = fmaxf(m, s), sc = __builtin_amdgcn_exp2f(m - mn), p = __builtin_amdgcn_exp2f(s - mn);
        m = mn; sum = sum * sc + p;
#pragma unroll
        for (int d = 0; d < 18; d++) {
          const unsigned vb = __builtin_amdgcn_raw_buffer_load_b32(rw, rec + 1280 + (kk >> 4) * 608 + ((kk >> 3) & 1) * 304 + d * 16 + (kk & 6) * 2, 0, SC1);
          oo[d] = oo[d] * sc + p * (kk & 1 ? bhi(vb) : blo(vb));
        }
      }
      const float mn = fmaxf(m, __shfl_xor(m, 32)), sc = __builtin_amdgcn_exp2f(m - mn);
      sum = xhalf_sum(sum * sc);
#pragma unroll
      for (int d = 0; d < 18; d++) oo[d] = xhalf_sum(oo[d] * sc);
      f32x16 t = ZERO16;
#pragma unroll
      for (int e = 0; e < 8; e++) t[e] = h ? oo[(e & 3) + 8 * (e >> 2) + 4] : oo[(e & 3) + 8 * (e >> 2)];
      t[8] = h ? 0.f : oo[16]; t[9] = h ? 0.f : oo[17]; t[10] = sum;
      if (r == 0) o[0] = t; else if (r == 1) o[1] = t; else if (r == 2) o[2] = t; else o[3] = t;
    }
  }
  if (hg >= 2) WF_STORE;
  {
    const rsrc_t rf = mkr(ws + OFF_O + tt * 36864, 36864);
    u32x4 ex;
#pragma unroll
    for (int r = 0; r < 4; r++) {
      const float inv = __builtin_amdgcn_rcpf(o[r][10]);
      __builtin_amdgcn_raw_buffer_store_b128(cvt8(o[r], 0, inv), rf, (hg * 4 + r) * 1024 + l * 16, 0, SC1);
      ex[r] = cvtpk(o[r][8] * inv, o[r][9] * inv);
    }
    __builtin_amdgcn_raw_buffer_store_b128(ex, rf, h ? 0x80000000u : (32 + (hg >> 1)) * 1024 + (hg & 1) * 512 + q * 16, 0, SC1);
  }
  if (tb == 0 && hg == 7 && wv == 0) {
    while (true) {
      const u32x2 v = __builtin_amdgcn_raw_buffer_load_b64(rw, OFF_CHK + l * 8, 0, SC1);
      if (__all(v[0] == tag && v[1] == tag)) break;
      __builtin_amdgcn_s_sleep(2);
    }
    if (l == 0) __builtin_amdgcn_raw_buffer_store_b32(tag, rw, OFF_EPOCH, 0, SC1);
  }
}

__global__ __launch_bounds__(512, 2) void k_final(const u16* __restrict__ b2, const char* __restrict__ ws, u16* __restrict__ out) {
  extern __shared__ __attribute__((aligned(16))) char lds[];
  const int l = threadIdx.x & 63, wv = __builtin_amdgcn_readfirstlane(threadIdx.x >> 6) & 3;
  const int h = l >> 5, q = l & 31, tp = (blockIdx.x & 7) * 4 + ((blockIdx.x >> 3) & 3), cg = blockIdx.x >> 5;
  if (__builtin_amdgcn_readfirstlane(threadIdx.x) < 256) {
    const rsrc_t rf = mkr(ws + OFF_O, (64 * 36 + 648) * 1024);
    bf16x8 fo[9][2], fw[9][3];
    f32x16 acc[6] = {ZERO16, ZERO16, ZERO16, ZERO16, ZERO16, ZERO16};
    int ks = (3 * cg / 2 + 9 * (tp & 3) / 4) % 9;
#pragma unroll
    for (int k = 0; k < 9 + 2; k++) {
      if (k < 9) {
        const int ko = (wv * 9 + ks) * 1024;
        ks = ks == 8 ? 0 : ks + 1;
#pragma unroll
        for (int i = 0; i < 2; i++) fo[k][i] = __builtin_bit_cast(bf16x8, __builtin_amdgcn_raw_buffer_load_b128(rf, l * 16, (2 * tp + i) * 36864 + ko, 0));
#pragma unroll
        for (int c = 0; c < 3; c++) fw[k][c] = __builtin_bit_cast(bf16x8, __builtin_amdgcn_raw_buffer_load_b128(rf, l * 16, (64 + cg * 3 + c) * 36864 + ko, 0));
      }
      __builtin_amdgcn_sched_barrier(0);
      if (k >= 2) {
#pragma unroll
        for (int u = 0; u < 6; u++) acc[u] = __builtin_amdgcn_mfma_f32_32x32x16_bf16(fw[k - 2][u % 3], fo[k - 2][u / 3], acc[u], 0, 0, 0);
        __builtin_amdgcn_sched_barrier(0);
      }
    }
#pragma unroll
    for (int u = 0; u < 6; u++)
#pragma unroll
      for (int p = 0; p < 4; p++) {
        const f32x4 v = {acc[u][4 * p], acc[u][4 * p + 1], acc[u][4 * p + 2], acc[u][4 * p + 3]};
        *(f32x4*)(lds + wv * 24576 + (u * 4 + p) * 1024 + l * 16) = v;
      }
    asm volatile("s_waitcnt lgkmcnt(0)\n s_barrier" ::: "memory");
    return;
  }
  const rsrc_t rp = mkr(ws + OFF_P, 8 * 64 * 36864);
  const int tt = 2 * tp + (wv >> 1), nh0 = cg * 6 + (wv & 1) * 3;
  u32x4 pp[24];
  u32x2 bb[3][2];
  float fin[3][8], g;
#pragma unroll
  for (int j = 0; j < 24 + 4; j++) {
    if (j >= 4) {
      const int i = (j - 4) >> 3;
#pragma unroll
      for (int e = 0; e < 4; e++) {
        const unsigned v = pp[j - 4][e];
        if ((j - 4) & 7) { fin[i][2 * e] += blo(v); fin[i][2 * e + 1] += bhi(v); } else { fin[i][2 * e] = blo(v); fin[i][2 * e + 1] = bhi(v); }
      }
      __builtin_amdgcn_sched_barrier(0);
    }
    if (j < 24) pp[j] = __builtin_amdgcn_raw_buffer_load_b128(rp, l * 16, ((j & 7) * 64 + tt) * 36864 + (nh0 + (j >> 3)) * 1024, 0);
    if (j == 16) {
#pragma unroll
      for (int i = 0; i < 6; i++) bb[i >> 1][i & 1] = *(const u32x2*)(b2 + (nh0 + (i >> 1)) * 16 + 8 * (i & 1) + 4 * h);
      g = ((const float*)(ws + OFF_G))[tt * 32 + q];
    }
    __builtin_amdgcn_sched_barrier(0);
  }
#pragma unroll
  for (int i = 0; i < 3; i++)
#pragma unroll
    for (int e = 0; e < 4; e++) {
      const unsigned bv = bb[i][e >> 1][e & 1];
      fin[i][2 * e] = (1.f - g) * (fin[i][2 * e] * PSCALE + blo(bv)); fin[i][2 * e + 1] = (1.f - g) * (fin[i][2 * e + 1] * PSCALE + bhi(bv));
    }
  __builtin_amdgcn_sched_barrier(0);
  asm volatile("s_barrier" ::: "memory");
#pragma unroll
  for (int i = 0; i < 3; i++) {
#pragma unroll
    for (int p = 0; p < 2; p++) {
      const char* src = lds + ((wv * 3 + i) * 2 + p) * 1024 + l * 16;
      const f32x4 c = (*(const f32x4*)src + *(const f32x4*)(src + 24576)) + (*(const f32x4*)(src + 49152) + *(const f32x4*)(src + 73728));
#pragma unroll
      for (int e = 0; e < 4; e++) fin[i][4 * p + e] += g * c[e];
    }
    *(u32x4*)(out + (tt * 32 + q) * 576 + (nh0 + i) * 16 + 8 * h) = pack8(fin[i]);
  }
}

extern "C" void kernel_launch(void* const* d_in, const int* in_sizes, int n_in, void* d_out, int out_size, void* d_ws, size_t ws_size,
                              hipStream_t stream) {
  char* ws = (char*)d_ws;
  (void)hipFuncSetAttribute((const void*)k_main, hipFuncAttributeMaxDynamicSharedMemorySize, LDS_MAIN);
  k_main<<<256, 256, LDS_MAIN, stream>>>((const u16*)d_in[0], (const u16*)d_in[1], (const u16*)d_in[2], (const u16*)d_in[3], (const u16*)d_in[4],
                                         (const u16*)d_in[6], (const u16*)d_in[7], (const u16*)d_in[8], (const u16*)d_in[10], ws);
  (void)hipFuncSetAttribute((const void*)k_final, hipFuncAttributeMaxDynamicSharedMemorySize, LDS_FIN);
  k_final<<<192, 512, LDS_FIN, stream>>>((const u16*)d_in[9], ws, (u16*)d_out);
}
```
